```python
import torch

def _softmax(x, axis=-1):
    return torch.softmax(x, dim=axis)
def _gelu(x):
    return torch.nn.functional.gelu(x, approximate="tanh")
def _silu(x):
    return torch.nn.functional.silu(x)
def _sigmoid(x):
    return torch.sigmoid(x)
def _layernorm(x, axis=-1, eps=1e-5):
    m = x.mean(dim=axis, keepdim=True)
    v = x.var(dim=axis, keepdim=True, unbiased=False)
    return (x - m) / torch.sqrt(v + eps)
def _scatter_add(tgt, idx, src, axis=0):
    return tgt.clone().index_add_(axis, idx, src)
def _segment_sum(src, seg, n):
    out = torch.zeros((n,) + src.shape[1:], dtype=src.dtype)
    return out.index_add_(0, seg, src)
def _pad(x, pw):
    flat = tuple(v for pair in reversed(pw) for v in pair)
    return torch.nn.functional.pad(x, flat)

kernel_name = 'concat_heads__mha_block__spectral_gfnet__prompt'

def setup_inputs():

    _w = (lambda a: (lambda t: t.to(torch.bfloat16) if t.is_floating_point() else t)(torch.as_tensor(a)))
    def _A_setup():
        B, S, D, H = 1, 2048, 448, 16
        return {
            "x": _w((lambda *s: torch.randn(*s, dtype=torch.bfloat16) * 0.2)(B, S, D)),
            "wqkv": _w((lambda *s: torch.randn(*s, dtype=torch.bfloat16) * 0.2)(D, 3 * D)),
            "wo": _w((lambda *s: torch.randn(*s, dtype=torch.bfloat16) * 0.05)(D, D)),
        }
    _A_in = _A_setup()

    def _B_setup():
        B, S, D = 1, 2048, 448
        Df = D // 2 + 1
        return {
            "x": _w((lambda *s: torch.randn(*s, dtype=torch.bfloat16) * 0.05)(B, S, D)),
            "w_r": _w((lambda *s: torch.randn(*s, dtype=torch.bfloat16) * 6.4)(S, Df) * 0.02),
            "w_i": _w((lambda *s: torch.randn(*s, dtype=torch.bfloat16) * 6.4)(S, Df) * 0.02),
        }
    _B_in = _B_setup()

    return {
        "A_x": _A_in["x"],
        "A_wqkv": _A_in["wqkv"],
        "A_wo": _A_in["wo"],
        "B_x": _B_in["x"],
        "B_w_r": _B_in["w_r"],
        "B_w_i": _B_in["w_i"],
    }

def reference(A_x, A_wqkv, A_wo, B_x, B_w_r, B_w_i):
    def _A_ref(x, wqkv, wo):
        B, S, D = x.shape
        H = 16
        Dh = D // H
        qkv = (lambda x, *d: x.permute(*d))((x @ wqkv).reshape(B, S, 3, H, Dh), 2, 0, 3, 1, 4)
        q, k, v = qkv[0], qkv[1], qkv[2]
        scores = torch.einsum("bhqd,bhkd->bhqk", q, k) * (Dh ** -0.5)
        p = _softmax(scores, axis=-1)
        o = torch.einsum("bhqk,bhkd->bhqd", p, v)
        return (lambda x, *d: x.permute(*d))(o, 0, 2, 1, 3).reshape(B, S, D) @ wo

    def _B_ref(x, w_r, w_i):
        B, S, D = x.shape
        X = (lambda x, axes=(-2,-1): torch.fft.rfft2(x.float(), dim=axes))(x, axes=(-2, -1))
        W = w_r + 1j * w_i
        X = X * W
        y = (lambda x, s, axes=(-2,-1): torch.fft.irfft2(x, s=s, dim=axes).to(torch.bfloat16))(X, (S, D), axes=(-2, -1))
        return y

    a = _A_ref(A_x, A_wqkv, A_wo)
    b = _B_ref(B_x, B_w_r, B_w_i)
    return torch.concatenate([a, b], axis=-1)
```

```cpp
#include <hip/hip_runtime.h>
#include <hip/hip_bf16.h>

typedef __attribute__((ext_vector_type(8))) __bf16 bf16x8;
typedef __attribute__((ext_vector_type(4))) __bf16 bf16x4;
typedef __attribute__((ext_vector_type(2))) __bf16 bf16x2;
typedef __attribute__((ext_vector_type(4))) short s16x4;
typedef __attribute__((ext_vector_type(16))) float f32x16;
typedef __attribute__((ext_vector_type(4))) float f32x4;
typedef __attribute__((ext_vector_type(2))) float f32x2;
typedef __attribute__((ext_vector_type(4))) unsigned u32x4;
typedef __attribute__((ext_vector_type(2))) unsigned u32x2;
typedef unsigned short u16;
#define LDSP __attribute__((address_space(3)))
#define DEV __device__ __forceinline__

DEV unsigned pk2(float lo, float hi) {
  f32x2 v = {lo, hi};
  return __builtin_bit_cast(unsigned, __builtin_convertvector(v, bf16x2));
}
DEV f32x16 mfma(u32x4 a, u32x4 b, f32x16 c) {
  return __builtin_amdgcn_mfma_f32_32x32x16_bf16(__builtin_bit_cast(bf16x8, a), __builtin_bit_cast(bf16x8, b), c, 0, 0, 0);
}
template <int S> DEV u32x4 packs(const f32x16& v) {
  u32x4 r = {pk2(v[8 * S + 0], v[8 * S + 1]), pk2(v[8 * S + 2], v[8 * S + 3]), pk2(v[8 * S + 4], v[8 * S + 5]), pk2(v[8 * S + 6], v[8 * S + 7])};
  return r;
}
template <int S> DEV u32x4 packsn(const f32x16& v) {
  u32x4 r = {pk2(-v[8 * S + 0], -v[8 * S + 1]), pk2(-v[8 * S + 2], -v[8 * S + 3]), pk2(-v[8 * S + 4], -v[8 * S + 5]), pk2(-v[8 * S + 6], -v[8 * S + 7])};
  return r;
}
DEV int sw23(int r) { return (r & ~12) | ((r & 4) << 1) | ((r & 8) >> 1); }
DEV float cosr(int n, float inv) { return __builtin_amdgcn_cosf((float)n * inv); }
DEV float sinr(int n, float inv) { return __builtin_amdgcn_sinf((float)n * inv); }
DEV void glds16(const void* g, char* l) {
  __builtin_amdgcn_global_load_lds((const unsigned*)g, (LDSP unsigned*)l, 16, 0, 0);
}
DEV u32x2 ldtr(const char* l) {
  return __builtin_bit_cast(u32x2, __builtin_amdgcn_ds_read_tr16_b64_v4i16((LDSP s16x4*)l));
}
DEV float bflo(unsigned u) { return __builtin_bit_cast(float, u << 16); }
DEV float bfhi(unsigned u) { return __builtin_bit_cast(float, u & 0xffff0000u); }
#define RAW_BARRIER() do { asm volatile("s_waitcnt lgkmcnt(0)" ::: "memory"); __builtin_amdgcn_s_barrier(); asm volatile("" ::: "memory"); } while (0)
#define VMCNT(n) asm volatile("s_waitcnt vmcnt(" #n ")" ::: "memory")

constexpr float C71 = 0.62348980185873353f, C72 = -0.22252093395631440f, C73 = -0.90096886790241913f;
constexpr float S71 = 0.78183148246802981f, S72 = 0.97492791218182361f, S73 = 0.43388373911755812f;

DEV void gen_cs(int i, int k0, int mul, int add, int mask, float inv, u32x4& C, u32x4& S) {
  float c[8], s[8];
#pragma unroll
  for (int j = 0; j < 8; ++j) {
    int n = (i * (mul * (k0 + j) + add)) & mask;
    c[j] = cosr(n, inv); s[j] = sinr(n, inv);
  }
#pragma unroll
  for (int j = 0; j < 4; ++j) { C[j] = pk2(c[2 * j], c[2 * j + 1]); S[j] = pk2(s[2 * j], s[2 * j + 1]); }
}

constexpr int K1_SLOT = 30 * 1024;
constexpr int K1_LDS = 5 * K1_SLOT + 64;

template <bool ISV>
DEV void qkv_chunk(const char* slot, int xoff0, int xoff1, int woff, f32x16& a0, f32x16& a1) {
#pragma unroll
  for (int ks = 0; ks < 4; ++ks) {
    u32x4 x0 = *(const u32x4*)(slot + (xoff0 ^ (ks * 32)));
    u32x4 x1 = *(const u32x4*)(slot + (xoff1 ^ (ks * 32)));
    u32x2 wa = ldtr(slot + woff + ks * 16 * 224);
    u32x2 wb = ldtr(slot + woff + ks * 16 * 224 + 224);
    u32x4 w = {wa[0], wa[1], wb[0], wb[1]};
    if (ISV) { a0 = mfma(x0, w, a0); a1 = mfma(x1, w, a1); }
    else     { a0 = mfma(w, x0, a0); a1 = mfma(w, x1, a1); }
  }
}

__global__ __launch_bounds__(512, 2) void k1_kernel(const u16* __restrict__ ax, const u16* __restrict__ wqkv, const u16* __restrict__ bx,
                                                    u32x4* __restrict__ Qf, u32x4* __restrict__ KVf, unsigned* __restrict__ T) {
  extern __shared__ __attribute__((aligned(16))) char lds[];
  const int tid = threadIdx.x, lane = tid & 63, w = __builtin_amdgcn_readfirstlane(tid >> 6);
  const int xcd = blockIdx.x & 7, idx = blockIdx.x >> 3;
  const int l31 = lane & 31, h = lane >> 5;
  if (idx >= 8) {
    const int u = idx - 8, which = u >> 3, tm = (xcd & 1) * 8 + (u & 7), g = xcd >> 1;
    const u16* srcp[4]; int dst[4]; int sstep[4];
#pragma unroll
    for (int i = 0; i < 4; ++i) {
      int ins = w + 8 * i;
      if (ins < 16) {
        int row = 8 * ins + (lane >> 3), ch = (lane & 7) ^ ((row >> 1) & 7);
        srcp[i] = ax + (tm * 128 + row) * 448 + ch * 8; sstep[i] = 64;
      } else {
        int q = 64 * (ins - 16) + lane; if (q > 895) q = 895;
        int rho = q / 14, cc = q - rho * 14;
        int kr = (rho & ~7) + ((rho & 7) >> 1) + 4 * (rho & 1);
        srcp[i] = wqkv + kr * 1344 + which * 448 + g * 112 + cc * 8; sstep[i] = 64 * 1344;
      }
      dst[i] = ins * 1024;
    }
    auto issue = [&](int kc, int slot) {
#pragma unroll
      for (int i = 0; i < 3; ++i) glds16(srcp[i] + kc * sstep[i], lds + slot * K1_SLOT + dst[i]);
      if (w < 6) glds16(srcp[3] + kc * sstep[3], lds + slot * K1_SLOT + dst[3]);
    };
#pragma unroll
    for (int kc = 0; kc < 5; ++kc) issue(kc, kc);
    const int hh = w & 3, th = w >> 2;
    const int r0 = th * 64 + l31, r1 = r0 + 32;
    const int xoff0 = r0 * 128 + ((h ^ ((r0 >> 1) & 7)) * 16), xoff1 = r1 * 128 + ((h ^ ((r1 >> 1) & 7)) * 16);
    const int woff = 16384 + (8 * h + 2 * ((lane >> 2) & 3)) * 224 + (hh * 28 + ((lane >> 4) & 1) * 16 + 4 * (lane & 3)) * 2;
    f32x16 a0 = {}, a1 = {};
#define K1_STEP(kc, n4, n3, ISV)                                   \
    if (w < 6) VMCNT(n4); else VMCNT(n3);                          \
    RAW_BARRIER();                                                 \
    if (kc >= 1 && kc <= 2) issue(kc + 4, kc - 1);                 \
    qkv_chunk<ISV>(lds + (kc % 5) * K1_SLOT, xoff0, xoff1, woff, a0, a1);
#define K1_ALL(ISV) K1_STEP(0, 16, 12, ISV) K1_STEP(1, 12, 9, ISV) K1_STEP(2, 12, 9, ISV) K1_STEP(3, 12, 9, ISV) \
                    K1_STEP(4, 8, 6, ISV) K1_STEP(5, 4, 3, ISV) K1_STEP(6, 0, 0, ISV)
    const int head = g * 4 + hh, tile = tm * 4 + th * 2;
    if (which == 2) {
      K1_ALL(true)
      if (l31 >= 28) {
        float f = (l31 == 28) ? 1.0f : 0.0f;
#pragma unroll
        for (int i = 0; i < 16; ++i) { a0[i] = f; a1[i] = f; }
      }
      u32x4* o = KVf + ((head * 64 + tile) * 4 + 2) * 64 + lane;
      o[0] = packs<0>(a0); o[64] = packs<1>(a0); o[256] = packs<0>(a1); o[320] = packs<1>(a1);
    } else {
      K1_ALL(false)
      const float sc = which == 0 ? 0.18898223650461362f * 1.4426950408889634f : 1.0f;
      const float sc2 = h ? 0.0f : sc;
#pragma unroll
      for (int i = 0; i < 12; ++i) { a0[i] *= sc; a1[i] *= sc; }
#pragma unroll
      for (int i = 12; i < 16; ++i) { a0[i] = h ? 0.0f : a0[i] * sc2; a1[i] = h ? 0.0f : a1[i] * sc2; }
      if (which == 0) {
        u32x4* o = Qf + ((head * 64 + tile) * 2) * 64 + lane;
        o[0] = packs<0>(a0); o[64] = packs<1>(a0); o[128] = packs<0>(a1); o[192] = packs<1>(a1);
      } else {
        u32x4* o = KVf + ((head * 64 + tile) * 4) * 64 + lane;
        o[0] = packs<0>(a0); o[64] = packs<1>(a0); o[256] = packs<0>(a1); o[320] = packs<1>(a1);
      }
    }
  } else {
    const int b1 = xcd * 8 + idx, s2 = b1 >> 1, hb = b1 & 1;
    u32x4* Zf = (u32x4*)lds;
    u32x4* F64 = (u32x4*)(lds + 56 * 1024);
    u32x4* Ap = (u32x4*)(lds + 72 * 1024);
    const int mt = w >> 2, t = w & 3, o = 2 * t + h;
    const u16* xp = bx + (s2 + 32 * (32 * mt + sw23(l31))) * 448 + 8 * o;
    u32x4 xr[7];
#pragma unroll
    for (int b = 0; b < 7; ++b) { int m = (b - o + 14) % 7; xr[b] = *(const u32x4*)(xp + 64 * m); }
    {
      u32x4 C, S;
      gen_cs(32 * mt + l31, 8 * o, 1, 0, 63, 1.0f / 64, C, S);
      F64[(w * 2 + 0) * 64 + lane] = C; F64[(w * 2 + 1) * 64 + lane] = S;
      gen_cs(32 * mt + l31, 8 * o, 32, s2, 2047, 1.0f / 2048, C, S);
      Ap[(w * 2 + 0) * 64 + lane] = C; Ap[(w * 2 + 1) * 64 + lane] = S;
    }
    u32x4 z[7];
#pragma unroll
    for (int jp = 0; jp < 4; ++jp) {
      f32x2 v[7];
#pragma unroll
      for (int b = 0; b < 7; ++b) {
        v[b][0] = bflo(xr[(b - 2 * jp + 14) % 7][jp]);
        v[b][1] = bfhi(xr[(b - 2 * jp - 1 + 14) % 7][jp]);
      }
      f32x2 p1 = v[1] + v[6], p2 = v[2] + v[5], p3 = v[3] + v[4];
      f32x2 m1 = v[6] - v[1], m2 = v[5] - v[2], m3 = v[4] - v[3];
      f32x2 r2 = v[0] + p1 * C72 + p2 * C73 + p3 * C71, i2 = m1 * S72 - m2 * S73 - m3 * S71;
      z[3][jp] = pk2(r2[0], r2[1]); z[4][jp] = pk2(i2[0], i2[1]);
      if (hb == 0) {
        f32x2 r0 = v[0] + p1 + p2 + p3;
        f32x2 r1 = v[0] + p1 * C71 + p2 * C72 + p3 * C73, i1 = m1 * S71 + m2 * S72 + m3 * S73;
        z[0][jp] = pk2(r0[0], r0[1]); z[1][jp] = pk2(r1[0], r1[1]); z[2][jp] = pk2(i1[0], i1[1]);
      } else {
        f32x2 r3 = v[0] + p1 * C73 + p2 * C71 + p3 * C72, i3 = m1 * S73 - m2 * S71 + m3 * S72;
        z[5][jp] = pk2(r3[0], r3[1]); z[6][jp] = pk2(i3[0], i3[1]);
      }
    }
    Zf[(3 * 8 + w) * 64 + lane] = z[3]; Zf[(4 * 8 + w) * 64 + lane] = z[4];
    if (hb == 0) { Zf[(0 * 8 + w) * 64 + lane] = z[0]; Zf[(1 * 8 + w) * 64 + lane] = z[1]; Zf[(2 * 8 + w) * 64 + lane] = z[2]; }
    else { Zf[(5 * 8 + w) * 64 + lane] = z[5]; Zf[(6 * 8 + w) * 64 + lane] = z[6]; }
    __syncthreads();
    const int ct = hb * 4 + w;
    if (w < 4 && ct < 7) {
      const int r = (ct + 1) >> 1, kt = ct ? ((ct + 1) & 1) : 0;
      f32x16 xre[2] = {}, xim[2] = {};
      if (ct) {
#pragma unroll
        for (int tt = 0; tt < 4; ++tt) {
          u32x4 C = F64[((kt * 4 + tt) * 2) * 64 + lane], S = F64[((kt * 4 + tt) * 2 + 1) * 64 + lane];
          u32x4 nS = S ^ 0x80008000u;
#pragma unroll
          for (int m = 0; m < 2; ++m) {
            u32x4 zr = Zf[((2 * r - 1) * 8 + m * 4 + tt) * 64 + lane], zi = Zf[((2 * r) * 8 + m * 4 + tt) * 64 + lane];
            xre[m] = mfma(zr, C, xre[m]); xim[m] = mfma(zi, C, xim[m]);
            xre[m] = mfma(zi, S, xre[m]); xim[m] = mfma(zr, nS, xim[m]);
          }
        }
      } else {
#pragma unroll
        for (int tt = 0; tt < 4; ++tt) {
          u32x4 C = F64[(tt * 2) * 64 + lane], S = F64[(tt * 2 + 1) * 64 + lane];
          u32x4 nS = S ^ 0x80008000u;
          if (l31 == 0) { nS[0] = nS[1] = nS[2] = nS[3] = 0xBF803F80u; }
#pragma unroll
          for (int m = 0; m < 2; ++m) {
            u32x4 zr = Zf[(m * 4 + tt) * 64 + lane];
            xre[m] = mfma(zr, C, xre[m]); xim[m] = mfma(zr, nS, xim[m]);
          }
        }
      }
      u32x4 bre[4] = {packs<0>(xre[0]), packs<1>(xre[0]), packs<0>(xre[1]), packs<1>(xre[1])};
      u32x4 bim[4] = {packs<0>(xim[0]), packs<1>(xim[0]), packs<0>(xim[1]), packs<1>(xim[1])};
#pragma unroll
      for (int jt = 0; jt < 2; ++jt) {
        f32x16 tre = {}, tim = {};
#pragma unroll
        for (int tt = 0; tt < 4; ++tt) {
          u32x4 C = Ap[((jt * 4 + tt) * 2) * 64 + lane], S = Ap[((jt * 4 + tt) * 2 + 1) * 64 + lane];
          tre = mfma(C, bre[tt], tre); tim = mfma(C, bim[tt], tim);
          tre = mfma(S, bim[tt], tre); tim = mfma(S, bre[tt] ^ 0x80008000u, tim);
        }
        unsigned* tp = T + (s2 * 64 + jt * 32 + 4 * h) * 224 + ct * 32 + l31;
#pragma unroll
        for (int g = 0; g < 16; ++g) tp[((g & 3) + 8 * (g >> 2)) * 224] = pk2(tre[g], tim[g]);
      }
    }
  }
}

constexpr int K2_LDS = 128 * 1024 + 64;
DEV unsigned ald32(const void* base, unsigned off) {
  unsigned r; asm volatile("global_load_dword %0, %1, %2" : "=v"(r) : "v"(off), "s"(base) : "memory"); return r;
}
DEV u32x4 ald128(const void* base, unsigned off) {
  u32x4 r; asm volatile("global_load_dwordx4 %0, %1, %2" : "=v"(r) : "v"(off), "s"(base) : "memory"); return r;
}
DEV unsigned ald16h(const void* base, unsigned off) {
  unsigned r = 0; asm volatile("global_load_short_d16_hi %0, %1, %2" : "+v"(r) : "v"(off), "s"(base) : "memory"); return r;
}
#define PIN16(a) asm volatile("" : "+v"(a[0]), "+v"(a[1]), "+v"(a[2]), "+v"(a[3]), "+v"(a[4]), "+v"(a[5]), "+v"(a[6]), "+v"(a[7]), \
                                   "+v"(a[8]), "+v"(a[9]), "+v"(a[10]), "+v"(a[11]), "+v"(a[12]), "+v"(a[13]), "+v"(a[14]), "+v"(a[15]))
DEV float xhalf_max(float v) {
  u32x2 r = __builtin_amdgcn_permlane32_swap(__builtin_bit_cast(unsigned, v), __builtin_bit_cast(unsigned, v), false, false);
  return fmaxf(__builtin_bit_cast(float, r[0]), __builtin_bit_cast(float, r[1]));
}
DEV float max16(const f32x16& s) {
  float m = fmaxf(fmaxf(s[0], s[1]), s[2]);
#pragma unroll
  for (int i = 3; i < 15; i += 2) m = fmaxf(fmaxf(m, s[i]), s[i + 1]);
  return fmaxf(m, s[15]);
}

struct B2Mat { u32x4 are[4], aim[4], ic[2], is[2]; };
DEV void b2_gen(int l31, int h, B2Mat& M) {
  const int j2 = sw23(l31);
#pragma unroll
  for (int t = 0; t < 4; ++t)
#pragma unroll
    for (int i = 0; i < 4; ++i) {
      int n = (j2 * (8 * t + 4 * h + i)) & 31;
      float c = cosr(n, 1.0f / 32), s = sinr(n, 1.0f / 32);
      M.are[t][i] = pk2(c, s); M.aim[t][i] = pk2(-s, c);
    }
  gen_cs(l31, 8 * h, 1, 0, 31, 1.0f / 32, M.ic[0], M.is[0]);
  gen_cs(l31, 16 + 8 * h, 1, 0, 31, 1.0f / 32, M.ic[1], M.is[1]);
}
DEV void b2_core(const B2Mat& M, const unsigned* tin, const float* wr, const float* wi, f32x16& ure, f32x16& uim) {
  f32x16 xr = {}, xi = {};
#pragma unroll
  for (int t = 0; t < 4; ++t) {
    u32x4 b = {tin[4 * t], tin[4 * t + 1], tin[4 * t + 2], tin[4 * t + 3]};
    xr = mfma(M.are[t], b, xr); xi = mfma(M.aim[t], b, xi);
  }
  f32x16 yr, yi;
#pragma unroll
  for (int g = 0; g < 16; ++g) { yr[g] = xr[g] * wr[g] - xi[g] * wi[g]; yi[g] = xr[g] * wi[g] + xi[g] * wr[g]; }
  u32x4 r0 = packs<0>(yr), r1 = packs<1>(yr), i0 = packs<0>(yi), i1 = packs<1>(yi);
  ure = f32x16{}; uim = f32x16{};
  ure = mfma(M.ic[0], r0, ure); uim = mfma(M.ic[0], i0, uim);
  ure = mfma(M.ic[1], r1, ure); uim = mfma(M.ic[1], i1, uim);
  ure = mfma(M.is[0], i0 ^ 0x80008000u, ure); uim = mfma(M.is[0], r0, uim);
  ure = mfma(M.is[1], i1 ^ 0x80008000u, ure); uim = mfma(M.is[1], r1, uim);
}

template <bool SAFE>
DEV void attn_tiles(const char* base, u32x4 q0, u32x4 q1, f32x16& o, f32x16& negm, float& m) {
#pragma unroll
  for (int tt = 0; tt < 4; ++tt) {
    const u32x4* p = (const u32x4*)(base + tt * 4096);
    u32x4 k0 = p[0], k1 = p[64], v0 = p[128], v1 = p[192];
    f32x16 s;
    if (SAFE) {
      s = mfma(k0, q0, f32x16{}); s = mfma(k1, q1, s);
      float mn = fmaxf(m, xhalf_max(max16(s)));
      float al = __builtin_amdgcn_exp2f(m - mn);
      m = mn;
#pragma unroll
      for (int i = 0; i < 16; ++i) { o[i] *= al; s[i] -= mn; }
    } else {
      s = mfma(k0, q0, negm); s = mfma(k1, q1, s);
    }
#pragma unroll
    for (int i = 0; i < 16; ++i) s[i] = __builtin_amdgcn_exp2f(s[i]);
    o = mfma(v0, packs<0>(s), o); o = mfma(v1, packs<1>(s), o);
  }
}

__global__ __launch_bounds__(512, 2) void k2_kernel(const u32x4* __restrict__ Qf, const u32x4* __restrict__ KVf, u32x4* __restrict__ Of,
                                                    const unsigned* __restrict__ T, unsigned* __restrict__ U,
                                                    const u16* __restrict__ Wr, const u16* __restrict__ Wi) {
  extern __shared__ __attribute__((aligned(16))) char lds[];
  const int tid = threadIdx.x, lane = tid & 63, w = __builtin_amdgcn_readfirstlane(tid >> 6);
  const int xcd = blockIdx.x & 7, idx = blockIdx.x >> 3;
  const int l31 = lane & 31, h = lane >> 5;
  const int head = xcd * 2 + (idx >> 4), qt = (idx & 15) * 4 + (w & 3), grp = w >> 2;
  const u32x4* kv = KVf + head * (64 * 4 * 64) + w * 64 + lane;
  auto issue = [&](int c) {
#pragma unroll
    for (int i = 0; i < 4; ++i) glds16(kv + c * 2048 + i * 512, lds + (c & 3) * 32768 + i * 8192 + w * 1024);
  };
  const float NRM = 1.0f / (2048.0f * 448.0f);
  u32x4 q0, q1;
  const int task = blockIdx.x * 2 + w;
  if (w < 2 && task < 448) {
    const int j1 = task / 7, ct = task - j1 * 7;
    const int r = (ct + 1) >> 1, ka = (ct ? ((ct + 1) & 1) * 32 : 0) + l31;
    int k = 7 * ka + 64 * r; if (k >= 448) k -= 448;
    const bool cj = k > 224;
    unsigned tin[16], wr[16], wi[16];
    const unsigned toff = ((4 * h * 64 + j1) * 224 + ct * 32 + l31) * 4;
#pragma unroll
    for (int i = 0; i < 16; ++i) tin[i] = ald32(T, toff + (8 * (i >> 2) + (i & 3)) * (64 * 224 * 4));
#pragma unroll
    for (int g = 0; g < 16; ++g) {
      int j = j1 + 64 * ((g & 7) + 8 * h + 16 * (g >> 3));
      unsigned off = (cj ? (((2048 - j) & 2047) * 225 + 448 - k) : (j * 225 + k)) * 2;
      wr[g] = ald16h(Wr, off); wi[g] = ald16h(Wi, off);
    }
    q0 = ald128(Qf, ((head * 64 + qt) * 128 + lane) * 16); q1 = ald128(Qf, ((head * 64 + qt) * 128 + 64 + lane) * 16);
#pragma unroll
    for (int c = 0; c < 4; ++c) issue(c);
    B2Mat M; b2_gen(l31, h, M);
    VMCNT(16);
    PIN16(tin); PIN16(wr); PIN16(wi); asm volatile("" : "+v"(q0), "+v"(q1));
    float fr[16], fi[16];
    const float si = cj ? -2.0f * NRM : 2.0f * NRM;
#pragma unroll
    for (int g = 0; g < 16; ++g) { fr[g] = __builtin_bit_cast(float, wr[g]) * (2.0f * NRM); fi[g] = __builtin_bit_cast(float, wi[g]) * si; }
    f32x16 ure, uim;
    b2_core(M, tin, fr, fi, ure, uim);
    unsigned* up = U + (4 * h * 64 + j1) * 224 + ct * 32 + l31;
    if (ct | l31) {
#pragma unroll
      for (int g = 0; g < 16; ++g) up[((g & 3) + 8 * (g >> 2)) * (64 * 224)] = pk2(ure[g], uim[g]);
    }
    VMCNT(28);
  } else if (w < 2 && task < 452) {
    const int j1 = (task - 448) * 16 + (lane & 15), j1m = (64 - j1) & 63, kk = (lane & 16) ? 224 : 0;
    unsigned pa[16], pb[16], wr[16], wi[16], wr2[16], wi2[16];
#pragma unroll
    for (int i = 0; i < 16; ++i) {
      int s2 = 8 * (i >> 2) + 4 * h + (i & 3);
      pa[i] = ald32(T, (s2 * 64 + j1) * 224 * 4); pb[i] = ald32(T, (s2 * 64 + j1m) * 224 * 4);
    }
#pragma unroll
    for (int g = 0; g < 16; ++g) {
      int j = j1 + 64 * ((g & 7) + 8 * h + 16 * (g >> 3));
      unsigned o1 = (j * 225 + kk) * 2, o2 = (((2048 - j) & 2047) * 225 + kk) * 2;
      wr[g] = ald16h(Wr, o1); wi[g] = ald16h(Wi, o1); wr2[g] = ald16h(Wr, o2); wi2[g] = ald16h(Wi, o2);
    }
    q0 = ald128(Qf, ((head * 64 + qt) * 128 + lane) * 16); q1 = ald128(Qf, ((head * 64 + qt) * 128 + 64 + lane) * 16);
#pragma unroll
    for (int c = 0; c < 4; ++c) issue(c);
    B2Mat M; b2_gen(l31, h, M);
    VMCNT(16);
    PIN16(pa); PIN16(pb); PIN16(wr); PIN16(wi); PIN16(wr2); PIN16(wi2); asm volatile("" : "+v"(q0), "+v"(q1));
    unsigned tin[16];
    const float sg = (lane & 16) ? -0.5f : 0.5f;
#pragma unroll
    for (int i = 0; i < 16; ++i) {
      int s2 = j1 ? (8 * (i >> 2) + 4 * h + (i & 3)) : 0;
      float ec = cosr(s2, 1.0f / 32), es = -sinr(s2, 1.0f / 32);
      float br = bflo(pb[i]), bi = -bfhi(pb[i]);
      tin[i] = pk2(0.5f * bflo(pa[i]) + sg * (ec * br - es * bi), 0.5f * bfhi(pa[i]) + sg * (ec * bi + es * br));
    }
    float fr[16], fi[16];
#pragma unroll
    for (int g = 0; g < 16; ++g) {
      fr[g] = (__builtin_bit_cast(float, wr[g]) + __builtin_bit_cast(float, wr2[g])) * (0.5f * NRM);
      fi[g] = (__builtin_bit_cast(float, wi[g]) - __builtin_bit_cast(float, wi2[g])) * (0.5f * NRM);
    }
    f32x16 ure, uim;
    b2_core(M, tin, fr, fi, ure, uim);
    unsigned* up = U + (4 * h * 64 + j1) * 224;
#pragma unroll
    for (int g = 0; g < 16; ++g) {
      float a = ure[g] + __shfl_xor(ure[g], 16), b = uim[g] + __shfl_xor(uim[g], 16);
      if (!(lane & 16)) up[((g & 3) + 8 * (g >> 2)) * (64 * 224)] = pk2(a, b);
    }
    VMCNT(28);
  } else {
    q0 = ald128(Qf, ((head * 64 + qt) * 128 + lane) * 16); q1 = ald128(Qf, ((head * 64 + qt) * 128 + 64 + lane) * 16);
#pragma unroll
    for (int c = 0; c < 4; ++c) issue(c);
    VMCNT(12);
    asm volatile("" : "+v"(q0), "+v"(q1));
  }
  RAW_BARRIER();
  const char* tb = lds + grp * 16384 + lane * 16;
  f32x16 o = {}, negm;
  float m;
  {
    const u32x4* p = (const u32x4*)tb;
    f32x16 s = mfma(p[0], q0, f32x16{}); s = mfma(p[64], q1, s);
    m = xhalf_max(max16(s));
#pragma unroll
    for (int i = 0; i < 16; ++i) negm[i] = -m;
  }
  attn_tiles<false>(tb, q0, q1, o, negm, m);
#pragma unroll 1
  for (int c = 1; c < 8; ++c) {
    if (c < 6) VMCNT(8); else if (c == 6) VMCNT(4); else VMCNT(0);
    RAW_BARRIER();
    if (c < 5) issue(c + 3);
    attn_tiles<false>(tb + (c & 3) * 32768, q0, q1, o, negm, m);
  }
  float* ex = (float*)lds + (w & 3) * (17 * 64) + lane;
  unsigned* flags = (unsigned*)(lds + 128 * 1024);
  {
    const bool bad = h && !(o[12] < 1.0e12f);
    const unsigned any = __ballot(bad) != 0ull;
    if (lane == 0) flags[w] = any;
  }
  if (grp) {
#pragma unroll
    for (int i = 0; i < 16; ++i) ex[i * 64] = o[i];
    ex[16 * 64] = m;
  }
  RAW_BARRIER();
  {
    u32x4 fa = *(const u32x4*)flags, fb = *(const u32x4*)(flags + 4);
    const unsigned any = __builtin_amdgcn_readfirstlane(fa[0] | fa[1] | fa[2] | fa[3] | fb[0] | fb[1] | fb[2] | fb[3]);
    if (any) {
      o = f32x16{}; m = -3.0e38f;
#pragma unroll 1
      for (int c = 0; c < 8; ++c) {
        RAW_BARRIER();
        issue(c);
        VMCNT(0);
        RAW_BARRIER();
        attn_tiles<true>(tb + (c & 3) * 32768, q0, q1, o, negm, m);
      }
      if (grp) {
#pragma unroll
        for (int i = 0; i < 16; ++i) ex[i * 64] = o[i];
        ex[16 * 64] = m;
      }
      RAW_BARRIER();
    }
  }
  if (!grp) {
    float m2 = ex[16 * 64], mm = fmaxf(m, m2);
    float f1 = __builtin_amdgcn_exp2f(m - mm), f2 = __builtin_amdgcn_exp2f(m2 - mm);
#pragma unroll
    for (int i = 0; i < 16; ++i) o[i] = o[i] * f1 + ex[i * 64] * f2;
    float l = __shfl(o[12], l31 + 32);
    float inv = 1.0f / l;
#pragma unroll
    for (int i = 0; i < 16; ++i) o[i] *= inv;
    if (h) { o[12] = 0.f; o[13] = 0.f; o[14] = 0.f; o[15] = 0.f; }
    u32x4* op = Of + ((qt * 16 + head) * 2) * 64 + lane;
    op[0] = packs<0>(o); op[64] = packs<1>(o);
  }
}

constexpr int ZS = 68;
constexpr int K3_LDS = 72 * 1024;

__global__ __launch_bounds__(256, 2) void k3_kernel(const u32x4* __restrict__ Of, const u16* __restrict__ wo, const unsigned* __restrict__ U,
                                                    u16* __restrict__ out) {
  extern __shared__ __attribute__((aligned(16))) char lds[];
  const int tid = threadIdx.x, lane = tid & 63, w = __builtin_amdgcn_readfirstlane(tid >> 6);
  const int xcd = blockIdx.x & 7, idx = blockIdx.x >> 3;
  const int l31 = lane & 31, h = lane >> 5;
  if (idx >= 8) {
    const int u = idx - 8, tq = xcd * 4 + u / 7, tn = u % 7;
    const int qi = w & 1, kh = w >> 1;
    {
      const int row = 8 * w + (lane >> 3), ch = (lane & 7) ^ (((row >> 1) & 1) << 2);
      const u16* src = wo + row * 448 + tn * 64 + ch * 8;
#pragma unroll
      for (int i = 0; i < 14; ++i) glds16(src + i * 32 * 448, lds + i * 4096 + w * 1024);
    }
    u32x4 of[16];
    const u32x4* op = Of + (((tq * 2 + qi) * 16 + kh * 8) * 2) * 64 + lane;
#pragma unroll
    for (int i = 0; i < 16; ++i) of[i] = op[i * 64];
    const int q = (lane >> 2) & 3, p = lane & 3, pc = ((p & 1) << 1) | (p >> 1);
    const int colb = ((lane >> 4) & 1) * 32 + pc * 8;
    const int base = (kh * 224 + q) * 128 + (colb ^ ((q >> 1) << 6));
    VMCNT(0);
    __syncthreads();
    f32x16 a0 = {}, a1 = {};
#pragma unroll
    for (int i = 0; i < 16; ++i) {
      const int hd = i >> 1, s = i & 1;
      const char* pa = lds + (hd * 28 + 16 * s) * 128;
      const int ra = base + h * 512, rb = (s ? base : base + h * 512) + 1024;
      u32x2 x0 = ldtr(pa + ra), x1 = ldtr(pa + rb), y0 = ldtr(pa + (ra ^ 64)), y1 = ldtr(pa + (rb ^ 64));
      a0 = mfma(u32x4{x0[0], x0[1], x1[0], x1[1]}, of[i], a0);
      a1 = mfma(u32x4{y0[0], y0[1], y1[0], y1[1]}, of[i], a1);
    }
    float* ex = (float*)(lds + 56 * 1024);
#pragma unroll
    for (int i = 0; i < 16; ++i) ex[(w * 16 + i) * 64 + lane] = kh ? a0[i] : a1[i];
    __syncthreads();
#pragma unroll
    for (int i = 0; i < 16; ++i) a0[i] = (kh ? a1[i] : a0[i]) + ex[((w ^ 2) * 16 + i) * 64 + lane];
    u16* dst = out + ((tq * 2 + qi) * 32 + l31) * 896 + tn * 64 + kh * 32 + 8 * h;
    *(u32x4*)dst = packs<0>(a0); *(u32x4*)(dst + 16) = packs<1>(a0);
  } else {
    const int b3 = xcd * 8 + idx, s2 = b3 >> 1, sh = b3 & 1, r = w;
    u32x4* Dm = (u32x4*)lds;
    u32x4* F64 = (u32x4*)(lds + 16384);
    float* zs = (float*)lds;
    unsigned ua[32], ub[32];
    const unsigned* up = U + (s2 * 64 + 4 * h) * 224 + (r ? (2 * r - 1) * 32 : 0) + sw23(l31);
#pragma unroll
    for (int i = 0; i < 32; ++i) ua[i] = up[(8 * (i >> 2) + (i & 3)) * 224];
    if (r) {
#pragma unroll
      for (int i = 0; i < 32; ++i) ub[i] = up[(8 * (i >> 2) + (i & 3)) * 224 + 32];
    }
    {
      const int s = s2 + 32 * (32 * sh + l31);
#pragma unroll
      for (int tt = 0; tt < 2; ++tt) {
        u32x4 dr, di;
#pragma unroll
        for (int i = 0; i < 4; ++i) {
          int n = ((8 * (2 * w + tt) + 4 * h + i) * s) & 2047;
          float c = cosr(n, 1.0f / 2048), sn = sinr(n, 1.0f / 2048);
          dr[i] = pk2(c, -sn); di[i] = pk2(sn, c);
        }
        Dm[((2 * w + tt) * 2) * 64 + lane] = dr; Dm[((2 * w + tt) * 2 + 1) * 64 + lane] = di;
        u32x4 C, S;
        gen_cs(32 * tt + sw23(l31), 16 * w + 8 * h, 1, 0, 63, 1.0f / 64, C, S);
        F64[((tt * 4 + w) * 2) * 64 + lane] = C; F64[((tt * 4 + w) * 2 + 1) * 64 + lane] = S;
      }
    }
    __syncthreads();
    f32x16 zre[2] = {}, zim[2] = {};
    if (r) {
      f32x16 ar = {}, ai = {}, br = {}, bi = {};
#pragma unroll
      for (int t = 0; t < 8; ++t) {
        u32x4 dr = Dm[(t * 2) * 64 + lane], di = Dm[(t * 2 + 1) * 64 + lane];
        u32x4 fa = {ua[4 * t], ua[4 * t + 1], ua[4 * t + 2], ua[4 * t + 3]}, fb = {ub[4 * t], ub[4 * t + 1], ub[4 * t + 2], ub[4 * t + 3]};
        ar = mfma(fa, dr, ar); ai = mfma(fa, di, ai); br = mfma(fb, dr, br); bi = mfma(fb, di, bi);
      }
      u32x4 yr[4] = {packs<0>(ar), packs<1>(ar), packs<0>(br), packs<1>(br)};
      u32x4 yi[4] = {packs<0>(ai), packs<1>(ai), packs<0>(bi), packs<1>(bi)};
#pragma unroll
      for (int at = 0; at < 2; ++at)
#pragma unroll
        for (int t = 0; t < 4; ++t) {
          u32x4 C = F64[((at * 4 + t) * 2) * 64 + lane], S = F64[((at * 4 + t) * 2 + 1) * 64 + lane];
          zre[at] = mfma(C, yr[t], zre[at]); zim[at] = mfma(C, yi[t], zim[at]);
          zre[at] = mfma(S, yi[t] ^ 0x80008000u, zre[at]); zim[at] = mfma(S, yr[t], zim[at]);
        }
    } else {
      f32x16 ar = {}, ai = {};
#pragma unroll
      for (int t = 0; t < 8; ++t) {
        u32x4 dr = Dm[(t * 2) * 64 + lane], di = Dm[(t * 2 + 1) * 64 + lane];
        u32x4 fa = {ua[4 * t], ua[4 * t + 1], ua[4 * t + 2], ua[4 * t + 3]};
        ar = mfma(fa, dr, ar); ai = mfma(fa, di, ai);
      }
      u32x4 yr[2] = {packs<0>(ar), packs<1>(ar)}, yn[2] = {packsn<0>(ai), packsn<1>(ai)};
#pragma unroll
      for (int at = 0; at < 2; ++at)
#pragma unroll
        for (int t = 0; t < 2; ++t) {
          u32x4 C = F64[((at * 4 + t) * 2) * 64 + lane], S = F64[((at * 4 + t) * 2 + 1) * 64 + lane];
          if (t == 0 && h == 0) S[0] = (S[0] & 0xffff0000u) | ((l31 & 1) ? 0x3F80u : 0xBF80u);
          zre[at] = mfma(C, yr[t], zre[at]); zre[at] = mfma(S, yn[t], zre[at]);
        }
    }
    __syncthreads();
    {
      float* zp = zs + ((r ? 2 * r - 1 : 0) * 32 + l31) * ZS + 8 * h;
#pragma unroll
      for (int at = 0; at < 2; ++at)
#pragma unroll
        for (int qd = 0; qd < 4; ++qd) {
          const int off = at * 32 + 16 * (qd >> 1) + 4 * (qd & 1);
          *(f32x4*)(zp + off) = f32x4{zre[at][4 * qd], zre[at][4 * qd + 1], zre[at][4 * qd + 2], zre[at][4 * qd + 3]};
          if (r) *(f32x4*)(zp + 32 * ZS + off) = f32x4{zim[at][4 * qd], zim[at][4 * qd + 1], zim[at][4 * qd + 2], zim[at][4 * qd + 3]};
        }
    }
    __syncthreads();
    {
      const int o = tid & 7, row = tid >> 3;
      const float* zp = zs + row * ZS + 8 * o;
      f32x4 zl[7], zh[7];
#pragma unroll
      for (int c = 0; c < 7; ++c) { zl[c] = *(const f32x4*)(zp + c * 32 * ZS); zh[c] = *(const f32x4*)(zp + c * 32 * ZS + 4); }
      float y[8][7];
#pragma unroll
      for (int jp = 0; jp < 4; ++jp) {
        f32x2 z[7];
#pragma unroll
        for (int c = 0; c < 7; ++c) z[c] = jp < 2 ? f32x2{zl[c][2 * jp], zl[c][2 * jp + 1]} : f32x2{zh[c][2 * jp - 4], zh[c][2 * jp - 3]};
        f32x2 y0 = z[0] + z[1] + z[3] + z[5];
        f32x2 e1 = z[0] + z[1] * C71 + z[3] * C72 + z[5] * C73, o1 = z[2] * S71 + z[4] * S72 + z[6] * S73;
        f32x2 e2 = z[0] + z[1] * C72 + z[3] * C73 + z[5] * C71, o2 = z[2] * S72 - z[4] * S73 - z[6] * S71;
        f32x2 e3 = z[0] + z[1] * C73 + z[3] * C71 + z[5] * C72, o3 = z[2] * S73 - z[4] * S71 + z[6] * S72;
        f32x2 yy[7] = {y0, e1 - o1, e2 - o2, e3 - o3, e3 + o3, e2 + o2, e1 + o1};
#pragma unroll
        for (int b = 0; b < 7; ++b) { y[2 * jp][b] = yy[b][0]; y[2 * jp + 1][b] = yy[b][1]; }
      }
      u16* dst = out + (s2 + 32 * (32 * sh + row)) * 896 + 448 + 8 * o;
#pragma unroll
      for (int b = 0; b < 7; ++b) {
        u32x4 v;
#pragma unroll
        for (int jp = 0; jp < 4; ++jp) v[jp] = pk2(y[2 * jp][(b + 2 * jp) % 7], y[2 * jp + 1][(b + 2 * jp + 1) % 7]);
        int m = (b - o + 14) % 7;
        *(u32x4*)(dst + 64 * m) = v;
      }
    }
  }
}

extern "C" void kernel_launch(void* const* d_in, const int* in_sizes, int n_in, void* d_out, int out_size, void* d_ws, size_t ws_size,
                              hipStream_t stream) {
  const u16* ax = (const u16*)d_in[0];
  const u16* wqkv = (const u16*)d_in[1];
  const u16* wo = (const u16*)d_in[2];
  const u16* bx = (const u16*)d_in[3];
  const u16* wr = (const u16*)d_in[4];
  const u16* wi = (const u16*)d_in[5];
  char* ws = (char*)d_ws;
  u32x4* Qf = (u32x4*)(ws);
  u32x4* KVf = (u32x4*)(ws + (2u << 20));
  u32x4* Of = (u32x4*)(ws + (6u << 20));
  unsigned* T = (unsigned*)(ws + (8u << 20));
  unsigned* U = (unsigned*)(ws + (10u << 20));
  (void)hipFuncSetAttribute((const void*)k1_kernel, hipFuncAttributeMaxDynamicSharedMemorySize, K1_LDS);
  (void)hipFuncSetAttribute((const void*)k2_kernel, hipFuncAttributeMaxDynamicSharedMemorySize, K2_LDS);
  (void)hipFuncSetAttribute((const void*)k3_kernel, hipFuncAttributeMaxDynamicSharedMemorySize, K3_LDS);
  k1_kernel<<<256, 512, K1_LDS, stream>>>(ax, wqkv, bx, Qf, KVf, T);
  k2_kernel<<<256, 512, K2_LDS, stream>>>(Qf, KVf, Of, T, U, wr, wi);
  k3_kernel<<<288, 256, K3_LDS, stream>>>(Of, wo, U, (u16*)d_out);
}
```

```cpp
#include <hip/hip_runtime.h>
#include <hip/hip_bf16.h>

typedef __attribute__((ext_vector_type(8))) __bf16 bf16x8;
typedef __attribute__((ext_vector_type(4))) __bf16 bf16x4;
typedef __attribute__((ext_vector_type(2))) __bf16 bf16x2;
typedef __attribute__((ext_vector_type(4))) short s16x4;
typedef __attribute__((ext_vector_type(16))) float f32x16;
typedef __attribute__((ext_vector_type(4))) float f32x4;
typedef __attribute__((ext_vector_type(2))) float f32x2;
typedef __attribute__((ext_vector_type(4))) unsigned u32x4;
typedef __attribute__((ext_vector_type(2))) unsigned u32x2;
typedef unsigned short u16;
#define LDSP __attribute__((address_space(3)))
#define DEV __device__ __forceinline__

DEV unsigned pk2(float lo, float hi) {
  f32x2 v = {lo, hi};
  return __builtin_bit_cast(unsigned, __builtin_convertvector(v, bf16x2));
}
DEV f32x16 mfma(u32x4 a, u32x4 b, f32x16 c) {
  return __builtin_amdgcn_mfma_f32_32x32x16_bf16(__builtin_bit_cast(bf16x8, a), __builtin_bit_cast(bf16x8, b), c, 0, 0, 0);
}
template <int S> DEV u32x4 packs(const f32x16& v) {
  u32x4 r = {pk2(v[8 * S + 0], v[8 * S + 1]), pk2(v[8 * S + 2], v[8 * S + 3]), pk2(v[8 * S + 4], v[8 * S + 5]), pk2(v[8 * S + 6], v[8 * S + 7])};
  return r;
}
template <int S> DEV u32x4 packsn(const f32x16& v) {
  u32x4 r = {pk2(-v[8 * S + 0], -v[8 * S + 1]), pk2(-v[8 * S + 2], -v[8 * S + 3]), pk2(-v[8 * S + 4], -v[8 * S + 5]), pk2(-v[8 * S + 6], -v[8 * S + 7])};
  return r;
}
DEV int sw23(int r) { return (r & ~12) | ((r & 4) << 1) | ((r & 8) >> 1); }
DEV float cosr(int n, float inv) { return __builtin_amdgcn_cosf((float)n * inv); }
DEV float sinr(int n, float inv) { return __builtin_amdgcn_sinf((float)n * inv); }
DEV void glds16(const void* g, char* l) {
  __builtin_amdgcn_global_load_lds((const unsigned*)g, (LDSP unsigned*)l, 16, 0, 0);
}
DEV void dma16(const void* base, unsigned voff, unsigned ldsaddr) {
  asm volatile("s_mov_b32 m0, %2\n\ts_nop 0\n\tglobal_load_lds_dwordx4 %0, %1" ::"v"(voff), "s"(base), "s"(ldsaddr) : "memory");
}
DEV unsigned ldsaddr_of(const char* p) { return (unsigned)(unsigned long long)(const LDSP char*)p; }
DEV u32x4 ald128(const void* base, unsigned off) {
  u32x4 r; asm volatile("global_load_dwordx4 %0, %1, %2" : "=v"(r) : "v"(off), "s"(base) : "memory"); return r;
}
#define SB() __builtin_amdgcn_sched_barrier(0)
DEV u32x2 ldtr(const char* l) {
  return __builtin_bit_cast(u32x2, __builtin_amdgcn_ds_read_tr16_b64_v4i16((LDSP s16x4*)l));
}
DEV float bflo(unsigned u) { return __builtin_bit_cast(float, u << 16); }
DEV float bfhi(unsigned u) { return __builtin_bit_cast(float, u & 0xffff0000u); }
#define RAW_BARRIER() do { asm volatile("s_waitcnt lgkmcnt(0)" ::: "memory"); __builtin_amdgcn_s_barrier(); asm volatile("" ::: "memory"); } while (0)
#define VMCNT(n) asm volatile("s_waitcnt vmcnt(" #n ")" ::: "memory")

#define STAMP_INIT
#define STAMP(k, i)
constexpr float C71 = 0.62348980185873353f, C72 = -0.22252093395631440f, C73 = -0.90096886790241913f;
constexpr float S71 = 0.78183148246802981f, S72 = 0.97492791218182361f, S73 = 0.43388373911755812f;

DEV void gen_cs(int i, int k0, int mul, int add, int mask, float inv, u32x4& C, u32x4& S) {
  float c[8], s[8];
#pragma unroll
  for (int j = 0; j < 8; ++j) {
    int n = (i * (mul * (k0 + j) + add)) & mask;
    c[j] = cosr(n, inv); s[j] = sinr(n, inv);
  }
#pragma unroll
  for (int j = 0; j < 4; ++j) { C[j] = pk2(c[2 * j], c[2 * j + 1]); S[j] = pk2(s[2 * j], s[2 * j + 1]); }
}

constexpr double ccos64(int n) {
  n &= 63; if (n > 32) n = 64 - n;
  double sg = 1.0; if (n > 16) { n = 32 - n; sg = -1.0; }
  if (n == 16) return 0.0;
  double x = 6.283185307179586476925 * n / 64.0, x2 = x * x, term = 1.0, sum = 1.0;
  for (int i = 1; i < 14; ++i) { term *= -x2 / ((2 * i - 1) * (2 * i)); sum += term; }
  return sg * sum;
}
constexpr unsigned cbf(double v) { unsigned u = __builtin_bit_cast(unsigned, (float)v); return (u + 0x7fffu + ((u >> 16) & 1u)) >> 16; }
constexpr int csw(int r) { return (r & ~12) | ((r & 4) << 1) | ((r & 8) >> 1); }
struct alignas(16) Tabs { unsigned f64[16 * 256]; unsigned b2[14 * 256]; };
constexpr Tabs make_tabs() {
  Tabs t{};
  unsigned c[64] = {}, sn[64] = {};
  for (int n = 0; n < 64; ++n) { c[n] = cbf(ccos64(n)); sn[n] = cbf(ccos64(n - 16 + 64)); }
  for (int f = 0; f < 8; ++f)
    for (int l = 0; l < 64; ++l)
      for (int d = 0; d < 4; ++d) {
        int i = 32 * (f >> 2) + (l & 31), k = 16 * (f & 3) + 8 * (l >> 5) + 2 * d;
        t.f64[((f * 2) * 64 + l) * 4 + d] = c[(i * k) & 63] | (c[(i * (k + 1)) & 63] << 16);
        t.f64[((f * 2 + 1) * 64 + l) * 4 + d] = sn[(i * k) & 63] | (sn[(i * (k + 1)) & 63] << 16);
      }
  for (int l = 0; l < 64; ++l)
    for (int d = 0; d < 4; ++d) {
      for (int f = 0; f < 4; ++f) {
        int n = (2 * csw(l & 31) * (8 * f + 4 * (l >> 5) + d)) & 63;
        t.b2[(f * 64 + l) * 4 + d] = c[n] | (sn[n] << 16);
        t.b2[((4 + f) * 64 + l) * 4 + d] = (sn[n] ^ 0x8000u) | (c[n] << 16);
      }
      for (int f = 0; f < 2; ++f) {
        int k = 16 * f + 8 * (l >> 5) + 2 * d, n0 = (2 * (l & 31) * k) & 63, n1 = (2 * (l & 31) * (k + 1)) & 63;
        t.b2[((8 + f) * 64 + l) * 4 + d] = c[n0] | (c[n1] << 16);
        t.b2[((10 + f) * 64 + l) * 4 + d] = sn[n0] | (sn[n1] << 16);
        t.b2[((12 + f) * 64 + l) * 4 + d] = (sn[n0] | (sn[n1] << 16)) ^ 0x80008000u;
      }
    }
  return t;
}
__device__ const Tabs g_tabs = make_tabs();
DEV unsigned ald32(const void* base, unsigned off) {
  unsigned r; asm volatile("global_load_dword %0, %1, %2" : "=v"(r) : "v"(off), "s"(base) : "memory"); return r;
}
DEV void ast32(void* base, unsigned off, unsigned v) { asm volatile("global_store_dword %0, %1, %2" ::"v"(off), "v"(v), "s"(base) : "memory"); }
DEV int col_bin(int c) { int ct = c >> 5, k = 7 * ((ct ? ((ct + 1) & 1) * 32 : 0) + (c & 31)) + 64 * ((ct + 1) >> 1); return k >= 448 ? k - 448 : k; }

constexpr int K1_SLOT = 32 * 1024;
constexpr int K1_LDS = 152 * 1024;
struct QF { u32x4 x0[4], x1[4], w[4]; };

template <bool ISV>
DEV void qkv_loop(const char* lds, unsigned ld, const u16* xb, const u16* wb, int kn, unsigned xo, unsigned wo0, unsigned wo1,
                  int xoff, int woff, f32x16& a0, f32x16& a1) {
  auto piece = [&](int slot, int i) {
    const unsigned d = ld + slot * K1_SLOT;
    if (i == 0) dma16(xb + kn * 64, xo, d);
    if (i == 1) dma16(wb + kn * (64 * 1344), wo0, d + 16384);
    if (i == 2) dma16(xb + kn * 64 + 64 * 448, xo, d + 8192);
    if (i == 3) { dma16(wb + kn * (64 * 1344), wo1, d + 24576); kn = kn == 6 ? 0 : kn + 1; }
  };
  auto rd = [&](QF& f, int slot, int ks) {
    const char* sl = lds + slot * K1_SLOT;
    f.x0[ks] = *(const u32x4*)(sl + (xoff ^ (ks * 32)));
    f.x1[ks] = *(const u32x4*)(sl + (xoff ^ (ks * 32)) + 4096);
    u32x2 wa = ldtr(sl + woff + ks * 16 * 224), wc = ldtr(sl + woff + ks * 16 * 224 + 224);
    f.w[ks] = u32x4{wa[0], wa[1], wc[0], wc[1]};
  };
#pragma unroll
  for (int i = 0; i < 8; ++i) piece(i >> 2, i & 3);
  QF f[2];
#pragma unroll
  for (int kc = 0; kc < 8; ++kc) {
    if (kc < 7) { if (kc < 6) VMCNT(4); else VMCNT(0); __builtin_amdgcn_s_barrier(); asm volatile("" ::: "memory"); }
#pragma unroll
    for (int ks = 0; ks < 4; ++ks) {
      if (kc < 7) rd(f[kc & 1], kc & 3, ks);
      if (kc > 0) {
        QF& g = f[(kc - 1) & 1];
        if (ISV) { a0 = mfma(g.x0[ks], g.w[ks], a0); a1 = mfma(g.x1[ks], g.w[ks], a1); }
        else     { a0 = mfma(g.w[ks], g.x0[ks], a0); a1 = mfma(g.w[ks], g.x1[ks], a1); }
      }
      if (kc < 5) piece((kc + 2) & 3, ks);
      SB();
    }
  }
}

__global__ __launch_bounds__(512, 2) void k1_kernel(const u16* __restrict__ ax, const u16* __restrict__ wqkv, const u16* __restrict__ bx,
                                                    const u16* __restrict__ Wr, const u16* __restrict__ Wi,
                                                    u32x4* __restrict__ Qf, u32x4* __restrict__ KVf, u32x4* __restrict__ T, u32x4* __restrict__ Wf) {
  extern __shared__ __attribute__((aligned(16))) char lds[];
  const int tid = threadIdx.x, lane = tid & 63, w = __builtin_amdgcn_readfirstlane(tid >> 6);
  const int xcd = blockIdx.x & 7, idx = blockIdx.x >> 3;
  const int l31 = lane & 31, h = lane >> 5;
  const unsigned lb = ldsaddr_of(lds);
  STAMP_INIT; STAMP(0, 0); STAMP(0, 1);
  if (idx >= 8) {
    const int u = idx - 8, which = u >> 3, tm = (xcd & 1) * 8 + (u & 7), g = xcd >> 1;
    const int row = 8 * w + (lane >> 3);
    const unsigned xo = (row * 448 + ((lane & 7) ^ ((row >> 1) & 7)) * 8) * 2;
    unsigned wo2[2];
#pragma unroll
    for (int i = 0; i < 2; ++i) {
      int q = 64 * (w + 8 * i) + lane; if (q > 895) q = 895;
      int rho = q / 14, cc = q - rho * 14;
      wo2[i] = (((rho & ~7) + ((rho & 7) >> 1) + 4 * (rho & 1)) * 1344 + cc * 8) * 2;
    }
    const int hh = w & 3, th = w >> 2, r0 = th * 64 + l31;
    const int xoff = r0 * 128 + ((h ^ ((r0 >> 1) & 7)) * 16);
    const int woff = 16384 + (8 * h + 2 * ((lane >> 2) & 3)) * 224 + (hh * 28 + ((lane >> 4) & 1) * 16 + 4 * (lane & 3)) * 2;
    const int head = g * 4 + hh, tile = tm * 4 + th * 2, kn = ((u & 7) + 2 * which) % 7;
    f32x16 a0 = {}, a1 = {};
    u32x4 o0, o1, o2, o3;
    if (which == 2) {
      qkv_loop<true>(lds, lb + w * 1024, ax + tm * 128 * 448, wqkv + 896 + g * 112, kn, xo, wo2[0], wo2[1], xoff, woff, a0, a1);
      o0 = packs<0>(a0); o1 = packs<1>(a0); o2 = packs<0>(a1); o3 = packs<1>(a1);
      if (l31 >= 28) { unsigned f = (l31 == 28) ? 0x3F803F80u : 0u; o0 = u32x4{f, f, f, f}; o1 = o0; o2 = o0; o3 = o0; }
    } else {
      qkv_loop<false>(lds, lb + w * 1024, ax + tm * 128 * 448, wqkv + which * 448 + g * 112, kn, xo, wo2[0], wo2[1], xoff, woff, a0, a1);
      const float sc = which ? 1.0f : 0.18898223650461362f * 1.4426950408889634f;
      a0 *= sc; a1 *= sc;
      o0 = packs<0>(a0); o1 = packs<1>(a0); o2 = packs<0>(a1); o3 = packs<1>(a1);
      if (h) { o1[2] = which ? 0x3F80u : 0u; o1[3] = 0; o3[2] = o1[2]; o3[3] = 0; }
    }
    STAMP(0, 7);
    u32x4* o = (which == 0 ? Qf + ((head * 64 + tile) * 2) * 64 : KVf + ((head * 64 + tile) * 4 + (which & 2)) * 64) + lane;
    const int st = which == 0 ? 128 : 256;
    o[0] = o0; o[64] = o1; o[st] = o2; o[st + 64] = o3;
  } else {
    const int b1 = xcd * 8 + idx, s2 = b1 >> 1, hb = b1 & 1, jq = b1 >> 2, gq = b1 & 3;
    u32x4* F64 = (u32x4*)(lds + 56 * 1024);
    u32x4* Ap = (u32x4*)(lds + 72 * 1024);
    {
      const int row = 8 * w + (lane >> 3);
      const unsigned xo = ((s2 + 32 * ((row & 32) + sw23(row & 31))) * 448 + ((lane & 7) ^ ((row >> 1) & 7)) * 8) * 2;
#pragma unroll
      for (int m = 0; m < 7; ++m) dma16(bx + 64 * m, xo, lb + m * 8192 + w * 1024);
      dma16(g_tabs.f64 + w * 512, lane * 16, lb + 56 * 1024 + w * 2048); dma16(g_tabs.f64 + w * 512 + 256, lane * 16, lb + 57 * 1024 + w * 2048);
      const int gg = 4 * gq + (w & 3), jb = 4 * jq + 64 * ((gg & 7) + 8 * (w >> 2) + 16 * (gg >> 3));
#pragma unroll
      for (int i = 0; i < 4; ++i) {
        int j = jb + (i & 1) * 2 + h; if (i >> 1) j = (2048 - j) & 2047;
        unsigned off = ((j * 450) & ~15u) + l31 * 16;
        off = off > 921584u ? 921584u : off;
        dma16(Wr, off, lb + 88 * 1024 + w * 8192 + i * 1024); dma16(Wi, off, lb + 92 * 1024 + w * 8192 + i * 1024);
      }
    }
    SB();
    const int mt = w >> 2, o = 2 * (w & 3) + h;
    {
      u32x4 C, S;
      gen_cs(32 * mt + l31, 8 * o, 32, s2, 2047, 1.0f / 2048, C, S);
      Ap[(w * 2 + 0) * 64 + lane] = C; Ap[(w * 2 + 1) * 64 + lane] = S;
    }
    STAMP(0, 2);
    VMCNT(10);
    RAW_BARRIER();
    STAMP(0, 3);
    {
      const int row = 32 * mt + l31;
      char* xp = lds + row * 128 + ((o ^ ((row >> 1) & 7)) * 16);
      u32x4 xr[7], z[4];
#pragma unroll
      for (int b = 0; b < 7; ++b) { int m = b - o; m += m < 0 ? 7 : 0; m += m < 0 ? 7 : 0; xr[b] = *(const u32x4*)(xp + m * 8192); }
      const float ca = hb ? C72 : C71, cb = hb ? C73 : C72, cc = hb ? C71 : C73, sa = hb ? S72 : S71, sb = hb ? -S73 : S72, sc = hb ? -S71 : S73;
      const float da = hb ? C73 : 1.0f, db = hb ? C71 : 1.0f, dc = hb ? C72 : 1.0f, ta = hb ? S73 : 0.0f, tb = hb ? -S71 : 0.0f, tc = hb ? S72 : 0.0f;
#pragma unroll
      for (int jp = 0; jp < 4; ++jp) {
        f32x2 v[7];
#pragma unroll
        for (int b = 0; b < 7; ++b) {
          v[b][0] = bflo(xr[(b - 2 * jp + 14) % 7][jp]);
          v[b][1] = bfhi(xr[(b - 2 * jp - 1 + 14) % 7][jp]);
        }
        f32x2 p1 = v[1] + v[6], p2 = v[2] + v[5], p3 = v[3] + v[4];
        f32x2 m1 = v[6] - v[1], m2 = v[5] - v[2], m3 = v[4] - v[3];
        f32x2 ra = v[0] + p1 * ca + p2 * cb + p3 * cc, ia = m1 * sa + m2 * sb + m3 * sc;
        f32x2 rb = v[0] + p1 * da + p2 * db + p3 * dc, ib = m1 * ta + m2 * tb + m3 * tc;
        z[0][jp] = pk2(ra[0], ra[1]); z[1][jp] = pk2(ia[0], ia[1]); z[2][jp] = pk2(rb[0], rb[1]); z[3][jp] = pk2(ib[0], ib[1]);
      }
#pragma unroll
      for (int c = 0; c < 4; ++c) *(u32x4*)(xp + c * 8192) = z[c];
    }
    VMCNT(0);
    RAW_BARRIER();
    STAMP(0, 4);
    if (w < 4) {
      const int ct = hb ? 3 + w : (w == 2 ? 0 : w + 1), kt = w & 1;
      if (hb || w < 3) {
        f32x16 xre[2] = {}, xim[2] = {};
        const char* zp = lds + (w & 2) * 8192 + l31 * 128;
        const int zsw = (h ^ ((l31 >> 1) & 7)) * 16;
#pragma unroll
        for (int tt = 0; tt < 4; ++tt) {
          u32x4 C = F64[((kt * 4 + tt) * 2) * 64 + lane], S = F64[((kt * 4 + tt) * 2 + 1) * 64 + lane];
          u32x4 nS = S ^ 0x80008000u;
          if (ct == 0 && l31 == 0) nS = u32x4{0xBF803F80u, 0xBF803F80u, 0xBF803F80u, 0xBF803F80u};
#pragma unroll
          for (int m = 0; m < 2; ++m) {
            u32x4 zr = *(const u32x4*)(zp + m * 4096 + (zsw ^ (tt * 32))), zi = *(const u32x4*)(zp + 8192 + m * 4096 + (zsw ^ (tt * 32)));
            xre[m] = mfma(zr, C, xre[m]); xim[m] = mfma(zi, C, xim[m]);
            xre[m] = mfma(zi, S, xre[m]); xim[m] = mfma(zr, nS, xim[m]);
          }
        }
        STAMP(0, 5);
        u32x4 bre[4] = {packs<0>(xre[0]), packs<1>(xre[0]), packs<0>(xre[1]), packs<1>(xre[1])};
        u32x4 bim[4] = {packs<0>(xim[0]), packs<1>(xim[0]), packs<0>(xim[1]), packs<1>(xim[1])};
        u32x4 bnr[4] = {packsn<0>(xre[0]), packsn<1>(xre[0]), packsn<0>(xre[1]), packsn<1>(xre[1])};
        u32x4* tp = T + (s2 * 16 + h) * 224 + ct * 32 + l31;
#pragma unroll
        for (int jt = 0; jt < 2; ++jt) {
          f32x16 tre = {}, tim = {};
#pragma unroll
          for (int tt = 0; tt < 4; ++tt) {
            u32x4 C = Ap[((jt * 4 + tt) * 2) * 64 + lane], S = Ap[((jt * 4 + tt) * 2 + 1) * 64 + lane];
            tre = mfma(C, bre[tt], tre); tim = mfma(C, bim[tt], tim);
            tre = mfma(S, bim[tt], tre); tim = mfma(S, bnr[tt], tim);
          }
#pragma unroll
          for (int q = 0; q < 4; ++q)
            tp[(jt * 8 + 2 * q) * 224] = u32x4{pk2(tre[4 * q], tim[4 * q]), pk2(tre[4 * q + 1], tim[4 * q + 1]), pk2(tre[4 * q + 2], tim[4 * q + 2]), pk2(tre[4 * q + 3], tim[4 * q + 3])};
        }
      }
    } else {
      const int i = lane & 3, j1 = 4 * jq + i;
      const char* st = lds + 88 * 1024 + h * 32768 + i * 512;
      const int al0 = (j1 * 450) & 15, al1 = 2048 + ((-j1 * 450) & 15);
      u32x4* wp = Wf + (jq * 29 * 4 + gq) * 64 + lane;
#pragma unroll
      for (int n = 0; n < 7; ++n) {
        const int cg = (w & 3) * 7 + n, k = col_bin(cg * 8 + (l31 >> 2));
        const bool cj = k > 224;
        const char* a = st + (cj ? al1 + 2 * (448 - k) : al0 + 2 * k);
        u32x4 v;
#pragma unroll
        for (int gi = 0; gi < 4; ++gi) v[gi] = ((unsigned)*(const u16*)(a + gi * 8192) | ((unsigned)*(const u16*)(a + 4096 + gi * 8192) << 16)) ^ (cj ? 0x80000000u : 0u);
        wp[cg * 256] = v;
      }
      if (w == 7) {
        const char* a = st + al0 + (l31 & 4) * 112, *b = st + al1 + (l31 & 4) * 112;
        u32x4 v;
#pragma unroll
        for (int gi = 0; gi < 4; ++gi)
          v[gi] = pk2(0.25f * (bflo(*(const u16*)(a + gi * 8192)) + bflo(*(const u16*)(b + gi * 8192))),
                      0.25f * (bflo(*(const u16*)(a + 4096 + gi * 8192)) - bflo(*(const u16*)(b + 4096 + gi * 8192))));
        wp[28 * 256] = v;
      }
    }
  }
  STAMP(0, 12); STAMP(0, 15);
}

constexpr int K2_LDS = 128 * 1024 + 64;
DEV float xhalf_max(float v) {
  float a = v, b = v;
  asm volatile("s_nop 1\n\tv_permlane32_swap_b32 %0, %1\n\ts_nop 1" : "+v"(a), "+v"(b));
  return fmaxf(a, b);
}
DEV float ror4(float v) { return __builtin_bit_cast(float, __builtin_amdgcn_mov_dpp(__builtin_bit_cast(int, v), 0x124, 0xF, 0xF, true)); }
DEV float max16(const f32x16& s) {
  float m = fmaxf(fmaxf(s[0], s[1]), s[2]);
#pragma unroll
  for (int i = 3; i < 15; i += 2) m = fmaxf(fmaxf(m, s[i]), s[i + 1]);
  return fmaxf(m, s[15]);
}
#define PIN16(a) asm volatile("" : "+v"(a[0]), "+v"(a[1]), "+v"(a[2]), "+v"(a[3]), "+v"(a[4]), "+v"(a[5]), "+v"(a[6]), "+v"(a[7]), \
                                   "+v"(a[8]), "+v"(a[9]), "+v"(a[10]), "+v"(a[11]), "+v"(a[12]), "+v"(a[13]), "+v"(a[14]), "+v"(a[15]))
#define EX(s, i) s[i] = __builtin_amdgcn_exp2f(s[i])
#define TILE(sc, sn, PV, PK2, DMA0, DMA1)                                            \
  sn = mfma(k0, q0, f32x16{}); v0 = *(const u32x4*)(PV);                             \
  EX(sc, 0); EX(sc, 1); EX(sc, 2); EX(sc, 3); SB();                                  \
  o = mfma(v1, p1, o); k0 = *(const u32x4*)(PK2); DMA0;                              \
  EX(sc, 4); EX(sc, 5); EX(sc, 6); EX(sc, 7); SB();                                  \
  sn = mfma(k1, q1m, sn); v1 = *(const u32x4*)((PV) + 1024);                         \
  p0 = packs<0>(sc); EX(sc, 8); EX(sc, 9); EX(sc, 10); SB();                         \
  o = mfma(v0, p0, o); k1 = *(const u32x4*)((PK2) + 1024); DMA1;                     \
  EX(sc, 11); EX(sc, 12); EX(sc, 13); EX(sc, 14); EX(sc, 15); p1 = packs<1>(sc); SB();
#define CHUNK(WAIT, D0, D1, D2, D3)                                                  \
  { const char* cb = tb + (c & 3) * 32768; const char* nb = tb + ((c + 1) & 3) * 32768; \
    TILE(sa, sb, cb + 2048, cb + 8192, , )                                           \
    TILE(sb, sa, cb + 6144, cb + 12288, , )                                          \
    WAIT; __builtin_amdgcn_s_barrier(); asm volatile("" ::: "memory"); SB();         \
    TILE(sa, sb, cb + 10240, nb, D0, D1)                                             \
    TILE(sb, sa, cb + 14336, nb + 4096, D2, D3) }

__global__ __launch_bounds__(512, 2) void k2_kernel(const u32x4* __restrict__ Qf, const u32x4* __restrict__ KVf, u32x4* __restrict__ Of,
                                                    const unsigned* __restrict__ T, unsigned* __restrict__ U, const u32x4* __restrict__ Wf) {
  extern __shared__ __attribute__((aligned(16))) char lds[];
  const int tid = threadIdx.x, lane = tid & 63, w = __builtin_amdgcn_readfirstlane(tid >> 6);
  const int xcd = blockIdx.x & 7, idx = blockIdx.x >> 3;
  const int l31 = lane & 31, h = lane >> 5;
  STAMP_INIT; STAMP(1, 0); STAMP(1, 1);
  const int head = xcd * 2 + (idx >> 4), qt = (idx & 15) * 4 + (w & 3), grp = w >> 2;
  const char* kvb = (const char*)KVf + head * 262144 + w * 1024;
  const unsigned lb = ldsaddr_of(lds) + w * 1024, l16 = lane * 16;
#define KVDMA(c, i) dma16(kvb + (c) * 32768 + (i) * 8192, l16, lb + ((c) & 3) * 32768 + (i) * 8192)
  u32x4 q0, q1;
  const int tk = idx * 2 + w;
  if (w < 2 && tk < 58) {
    const int jq = xcd * 2 + (tk >= 29), cg = tk >= 29 ? tk - 29 : tk;
    const bool sp = cg == 28;
    unsigned ta[16], tb[16];
    u32x4 wf[4], mt[14];
    const unsigned toff = ((4 * h * 16 + jq) * 896 + (sp ? (lane & 3) : cg * 32 + l31)) * 4;
#pragma unroll
    for (int i = 0; i < 16; ++i) ta[i] = ald32(T + (8 * (i >> 2) + (i & 3)) * 14336, toff);
#pragma unroll
    for (int t = 0; t < 8; ++t) mt[t] = ald128(g_tabs.b2 + t * 256, l16);
#pragma unroll
    for (int t = 0; t < 4; ++t) wf[t] = ald128(Wf + ((jq * 29 + cg) * 4 + t) * 64, l16);
#pragma unroll
    for (int t = 8; t < 14; ++t) mt[t] = ald128(g_tabs.b2 + t * 256, l16);
    const int j1 = 4 * jq + (lane & 3);
    if (sp) {
      const int jm = (64 - j1) & 63;
#pragma unroll
      for (int i = 0; i < 16; ++i) tb[i] = ald32(T + (8 * (i >> 2) + (i & 3)) * 14336, ((4 * h * 16 + (jm >> 2)) * 896 + (jm & 3)) * 4);
    }
    q0 = ald128(Qf + (head * 64 + qt) * 128, l16); q1 = ald128(Qf + (head * 64 + qt) * 128 + 64, l16);
    KVDMA(0, 0); KVDMA(0, 2); KVDMA(0, 1); KVDMA(0, 3); KVDMA(1, 0); KVDMA(1, 1); KVDMA(1, 2); KVDMA(1, 3);
    asm volatile("s_waitcnt vmcnt(8)" : "+v"(mt[0]), "+v"(mt[1]), "+v"(mt[2]), "+v"(mt[3]), "+v"(mt[4]), "+v"(mt[5]), "+v"(mt[6]), "+v"(mt[7]), "+v"(mt[8]), "+v"(mt[9]),
                 "+v"(mt[10]), "+v"(mt[11]), "+v"(mt[12]), "+v"(mt[13]), "+v"(wf[0]), "+v"(wf[1]), "+v"(wf[2]), "+v"(wf[3]), "+v"(q0), "+v"(q1) :: "memory");
    PIN16(ta);
    STAMP(1, 4);
    if (__builtin_expect(sp, 0)) {
      PIN16(tb);
      const float sg = (l31 & 4) ? -0.5f : 0.5f;
#pragma unroll
      for (int i = 0; i < 16; ++i) {
        int s2 = j1 ? (8 * (i >> 2) + 4 * h + (i & 3)) : 0;
        float ec = cosr(s2, 1.0f / 32), es = -sinr(s2, 1.0f / 32);
        float br = bflo(tb[i]), bi = -bfhi(tb[i]);
        ta[i] = pk2(0.5f * bflo(ta[i]) + sg * (ec * br - es * bi), 0.5f * bfhi(ta[i]) + sg * (ec * bi + es * br));
      }
    }
    f32x16 xr = {}, xi = {};
#pragma unroll
    for (int t = 0; t < 4; ++t) {
      u32x4 b = {ta[4 * t], ta[4 * t + 1], ta[4 * t + 2], ta[4 * t + 3]};
      xr = mfma(mt[t], b, xr); xi = mfma(mt[4 + t], b, xi);
    }
    f32x16 yr, yi;
#pragma unroll
    for (int g = 0; g < 16; g += 2) {
      f32x2 wr = {bflo(wf[g >> 2][g & 3]), bflo(wf[g >> 2][(g & 3) + 1])}, wi = {bfhi(wf[g >> 2][g & 3]), bfhi(wf[g >> 2][(g & 3) + 1])};
      f32x2 a = {xr[g], xr[g + 1]}, b = {xi[g], xi[g + 1]};
      f32x2 r = a * wr - b * wi, i = a * wi + b * wr;
      yr[g] = r[0]; yr[g + 1] = r[1]; yi[g] = i[0]; yi[g + 1] = i[1];
    }
    u32x4 r0 = packs<0>(yr), r1 = packs<1>(yr), i0 = packs<0>(yi), i1 = packs<1>(yi);
    f32x16 ure = {}, uim = {};
    ure = mfma(mt[8], r0, ure); uim = mfma(mt[8], i0, uim);
    ure = mfma(mt[12], i0, ure); uim = mfma(mt[10], r0, uim);
    ure = mfma(mt[9], r1, ure); uim = mfma(mt[9], i1, uim);
    ure = mfma(mt[13], i1, ure); uim = mfma(mt[11], r1, uim);
    if (__builtin_expect(sp, 0)) {
#pragma unroll
      for (int g = 0; g < 16; ++g) { ure[g] += ror4(ure[g]); uim[g] += ror4(uim[g]); }
    }
    if (sp ? (l31 < 4) : (cg != 0 || l31 >= 4)) {
#pragma unroll
      for (int g = 0; g < 16; ++g) ast32(U + ((g & 3) + 8 * (g >> 2)) * 14336, toff, pk2(ure[g], uim[g]));
    }
    VMCNT(20);
  } else {
    q0 = ald128(Qf + (head * 64 + qt) * 128, l16); q1 = ald128(Qf + (head * 64 + qt) * 128 + 64, l16);
    KVDMA(0, 0); KVDMA(0, 2); KVDMA(0, 1); KVDMA(0, 3); KVDMA(1, 0); KVDMA(1, 1); KVDMA(1, 2); KVDMA(1, 3);
    asm volatile("s_waitcnt vmcnt(4)" : "+v"(q0), "+v"(q1) :: "memory");
  }
  STAMP(1, 2);
  RAW_BARRIER();
  STAMP(1, 3);
  const char* tb = lds + grp * 16384 + lane * 16;
  f32x16 o = {}, sa, sb;
  u32x4 k0, k1, v0, v1 = {}, p0, p1 = {}, q1m = q1;
  float m;
  {
    sa = mfma(*(const u32x4*)tb, q0, f32x16{}); sa = mfma(*(const u32x4*)(tb + 1024), q1, sa);
    k0 = *(const u32x4*)(tb + 4096); k1 = *(const u32x4*)(tb + 5120);
    KVDMA(2, 0); KVDMA(2, 1); KVDMA(2, 2); KVDMA(2, 3);
    const unsigned nm = pk2(-xhalf_max(max16(sa)), 0.0f);
    m = -bflo(nm);
    if (h) q1m[2] = nm;
    sa -= m;
  }
  int c = 0;
#pragma unroll 1
  for (; c < 5; ++c) CHUNK(VMCNT(4), KVDMA(c + 3, 0), KVDMA(c + 3, 1), KVDMA(c + 3, 2), KVDMA(c + 3, 3))
#pragma unroll 1
  for (; c < 8; ++c) CHUNK(VMCNT(0), , , , )
  o = mfma(v1, p1, o);
  STAMP(1, 11);
  f32x4* ex = (f32x4*)(lds + 32768 + w * 3072) + lane;
  const f32x4* ey = (const f32x4*)(lds + 32768 + (w ^ 4) * 3072) + lane;
  unsigned* flags = (unsigned*)(lds + 128 * 1024);
  flags[w] = __ballot(h && !(o[12] < 1.0e12f)) != 0ull;
#define K2_PUT()                                                                                                   \
  { float l_ = xhalf_max(h ? o[12] : -1.0f);                                                                       \
    ex[0] = grp ? f32x4{o[0], o[1], o[2], o[3]} : f32x4{o[8], o[9], o[10], o[11]};                                 \
    ex[64] = grp ? f32x4{o[4], o[5], o[6], o[7]} : f32x4{o[12], o[13], o[14], o[15]};                              \
    ex[128] = f32x4{m, l_, 0.f, 0.f}; lsum = l_; }
  float lsum;
  K2_PUT();
  RAW_BARRIER();
  {
    u32x4 fa = *(const u32x4*)flags, fb = *(const u32x4*)(flags + 4);
    const unsigned any = __builtin_amdgcn_readfirstlane(fa[0] | fa[1] | fa[2] | fa[3] | fb[0] | fb[1] | fb[2] | fb[3]);
    if (__builtin_expect(any != 0, 0)) {
      o = f32x16{}; m = -3.0e38f;
#pragma unroll 1
      for (int c2 = 0; c2 < 8; ++c2) {
        RAW_BARRIER();
#pragma unroll 1
        for (int i = 0; i < 4; ++i) dma16(kvb + c2 * 32768 + i * 8192, l16, lb + i * 8192);
        VMCNT(0);
        RAW_BARRIER();
#pragma unroll 1
        for (int tt = 0; tt < 4; ++tt) {
          const u32x4* p = (const u32x4*)(tb + tt * 4096);
          f32x16 s = mfma(p[0], q0, f32x16{}); s = mfma(p[64], q1, s);
          float mn = fmaxf(m, xhalf_max(max16(s)));
          float al = __builtin_amdgcn_exp2f(m - mn);
          m = mn;
#pragma unroll
          for (int i = 0; i < 16; ++i) { o[i] *= al; s[i] = __builtin_amdgcn_exp2f(s[i] - mn); }
          o = mfma(p[128], packs<0>(s), o); o = mfma(p[192], packs<1>(s), o);
        }
      }
      K2_PUT();
      RAW_BARRIER();
    }
  }
  {
    f32x4 ya = ey[0], yb = ey[64], yc = ey[128];
    float mm = fmaxf(m, yc[0]);
    float f1 = __builtin_amdgcn_exp2f(m - mm), f2 = __builtin_amdgcn_exp2f(yc[0] - mm);
    float inv = __builtin_amdgcn_rcpf(lsum * f1 + yc[1] * f2);
    f1 *= inv; f2 *= inv;
    f32x4 xa = grp ? f32x4{o[8], o[9], o[10], o[11]} : f32x4{o[0], o[1], o[2], o[3]};
    f32x4 xb = grp ? f32x4{o[12], o[13], o[14], o[15]} : f32x4{o[4], o[5], o[6], o[7]};
    xa = xa * f1 + ya * f2; xb = xb * f1 + yb * f2;
    if (grp && h) xb = f32x4{};
    Of[((qt * 16 + head) * 2 + grp) * 64 + lane] = u32x4{pk2(xa[0], xa[1]), pk2(xa[2], xa[3]), pk2(xb[0], xb[1]), pk2(xb[2], xb[3])};
  }
  STAMP(1, 12); STAMP(1, 15);
}

constexpr int ZS = 68;
constexpr int K3_LDS = 72 * 1024;

__global__ __launch_bounds__(256, 2) void k3_kernel(const u32x4* __restrict__ Of, const u16* __restrict__ wo, const u32x4* __restrict__ U,
                                                    u16* __restrict__ out) {
  extern __shared__ __attribute__((aligned(16))) char lds[];
  const int tid = threadIdx.x, lane = tid & 63, w = __builtin_amdgcn_readfirstlane(tid >> 6);
  const int xcd = blockIdx.x & 7, idx = blockIdx.x >> 3;
  const int l31 = lane & 31, h = lane >> 5;
  STAMP_INIT; STAMP(2, 0); STAMP(2, 1);
  if (idx >= 8) {
    const int u = idx - 8, tq = xcd * 4 + u / 7, tn = u % 7;
    const int qi = w & 1, kh = w >> 1;
    u32x4 of[16];
    {
      const int r8 = lane >> 3;
      const unsigned so = (r8 * 448 + ((lane & 7) ^ ((r8 & 2) << 1)) * 8) * 2, l16 = lane * 16;
      const u16* src = wo + (kh * 224 + qi * 8) * 448 + tn * 64;
      const unsigned d = ldsaddr_of(lds) + (kh * 224 + qi * 8) * 128;
      const u32x4* op = Of + (((tq * 2 + qi) * 16 + kh * 8) * 2) * 64;
#pragma unroll
      for (int st = 0; st < 2; ++st) {
#pragma unroll
        for (int i = 0; i < 7; ++i) dma16(src + (st * 7 + i) * 16 * 448, so, d + (st * 7 + i) * 2048);
#pragma unroll
        for (int i = 0; i < 8; ++i) of[st * 8 + i] = ald128(op + (st * 8 + i) * 64, l16);
      }
    }
    const int q = (lane >> 2) & 3, p = lane & 3, pc = ((p & 1) << 1) | (p >> 1);
    const int colb = ((lane >> 4) & 1) * 32 + pc * 8;
    const int base = (kh * 224 + q) * 128 + (colb ^ ((q >> 1) << 6));
    f32x16 a0 = {}, a1 = {};
    STAMP(2, 2);
#pragma unroll
    for (int st = 0; st < 2; ++st) {
      if (st == 0) asm volatile("s_waitcnt vmcnt(15)" : "+v"(of[0]), "+v"(of[1]), "+v"(of[2]), "+v"(of[3]), "+v"(of[4]), "+v"(of[5]), "+v"(of[6]), "+v"(of[7]) :: "memory");
      else asm volatile("s_waitcnt vmcnt(0)" : "+v"(of[8]), "+v"(of[9]), "+v"(of[10]), "+v"(of[11]), "+v"(of[12]), "+v"(of[13]), "+v"(of[14]), "+v"(of[15]) :: "memory");
      RAW_BARRIER();
      if (st == 0) STAMP(2, 3); else STAMP(2, 4);
#pragma unroll
      for (int i = st * 8; i < st * 8 + 8; ++i) {
        const int hd = i >> 1, s = i & 1;
        const char* pa = lds + (hd * 28 + 16 * s) * 128;
        const int ra = base + h * 512, rb = (s ? base : base + h * 512) + 1024;
        u32x2 x0 = ldtr(pa + ra), x1 = ldtr(pa + rb), y0 = ldtr(pa + (ra ^ 64)), y1 = ldtr(pa + (rb ^ 64));
        a0 = mfma(u32x4{x0[0], x0[1], x1[0], x1[1]}, of[i], a0);
        a1 = mfma(u32x4{y0[0], y0[1], y1[0], y1[1]}, of[i], a1);
      }
    }
    STAMP(2, 5);
    f32x4* ex = (f32x4*)(lds + 56 * 1024);
    f32x16 snd = kh ? a0 : a1, kp = kh ? a1 : a0;
#pragma unroll
    for (int i = 0; i < 4; ++i) ex[(w * 4 + i) * 64 + lane] = f32x4{snd[4 * i], snd[4 * i + 1], snd[4 * i + 2], snd[4 * i + 3]};
    RAW_BARRIER();
#pragma unroll
    for (int i = 0; i < 4; ++i) {
      f32x4 v = ex[((w ^ 2) * 4 + i) * 64 + lane];
      kp[4 * i] += v[0]; kp[4 * i + 1] += v[1]; kp[4 * i + 2] += v[2]; kp[4 * i + 3] += v[3];
    }
    u16* dst = out + ((tq * 2 + qi) * 32 + l31) * 896 + tn * 64 + kh * 32 + 8 * h;
    *(u32x4*)dst = packs<0>(kp); *(u32x4*)(dst + 16) = packs<1>(kp);
  } else {
    const int b3 = xcd * 8 + idx, s2 = b3 >> 1, sh = b3 & 1, r = w;
    u32x4* Dm = (u32x4*)lds;
    u32x4* F64 = (u32x4*)(lds + 16384);
    float* zs = (float*)lds;
    u32x4 ua[8], ub[8];
    {
      const unsigned pl = (h * 32 + sw23(l31)) * 16;
#pragma unroll
      for (int i = 0; i < 4; ++i) dma16(g_tabs.f64 + (w * 4 + i) * 256, pl, ldsaddr_of(lds) + 16384 + (w * 4 + i) * 1024);
      const u32x4* up = U + s2 * 16 * 224 + (r ? 2 * r - 1 : 0) * 32;
      const unsigned uo = (h * 224 + sw23(l31)) * 16;
#pragma unroll
      for (int i = 0; i < 8; ++i) { ua[i] = ald128(up + i * 448, uo); ub[i] = ald128(up + i * 448 + 32, uo); }
    }
    SB();
    {
      const int s = s2 + 32 * (32 * sh + l31);
      const float NRM2 = 2.0f / (2048.0f * 448.0f);
#pragma unroll
      for (int tt = 0; tt < 2; ++tt) {
        u32x4 dr, di;
#pragma unroll
        for (int i = 0; i < 4; ++i) {
          int n = ((8 * (2 * w + tt) + 4 * h + i) * s) & 2047;
          float c = cosr(n, 1.0f / 2048) * NRM2, sn = sinr(n, 1.0f / 2048) * NRM2;
          dr[i] = pk2(c, -sn); di[i] = pk2(sn, c);
        }
        Dm[((2 * w + tt) * 2) * 64 + lane] = dr; Dm[((2 * w + tt) * 2 + 1) * 64 + lane] = di;
      }
    }
    asm volatile("s_waitcnt vmcnt(0)" : "+v"(ua[0]), "+v"(ua[1]), "+v"(ua[2]), "+v"(ua[3]), "+v"(ua[4]), "+v"(ua[5]), "+v"(ua[6]), "+v"(ua[7]),
                 "+v"(ub[0]), "+v"(ub[1]), "+v"(ub[2]), "+v"(ub[3]), "+v"(ub[4]), "+v"(ub[5]), "+v"(ub[6]), "+v"(ub[7]) :: "memory");
    STAMP(2, 2);
    RAW_BARRIER();
    STAMP(2, 3);
    f32x16 zre[2] = {}, zim[2] = {};
    {
      f32x16 ar = {}, ai = {}, br = {}, bi = {};
#pragma unroll
      for (int t = 0; t < 8; ++t) {
        u32x4 dr = Dm[(t * 2) * 64 + lane], di = Dm[(t * 2 + 1) * 64 + lane];
        ar = mfma(ua[t], dr, ar); ai = mfma(ua[t], di, ai); br = mfma(ub[t], dr, br); bi = mfma(ub[t], di, bi);
      }
      STAMP(2, 4);
      u32x4 yr[4] = {packs<0>(ar), packs<1>(ar), packs<0>(br), packs<1>(br)};
      u32x4 yi[4] = {packs<0>(ai), packs<1>(ai), packs<0>(bi), packs<1>(bi)};
      u32x4 yn[4] = {packsn<0>(ai), packsn<1>(ai), packsn<0>(bi), packsn<1>(bi)};
      if (r == 0) { yr[2] = u32x4{}; yr[3] = u32x4{}; yn[2] = u32x4{}; yn[3] = u32x4{}; }
#pragma unroll
      for (int at = 0; at < 2; ++at)
#pragma unroll
        for (int t = 0; t < 4; ++t) {
          u32x4 C = F64[((at * 4 + t) * 2) * 64 + lane], S = F64[((at * 4 + t) * 2 + 1) * 64 + lane];
          if (t == 0 && r == 0 && h == 0) S[0] = (S[0] & 0xffff0000u) | ((l31 & 1) ? 0x3F80u : 0xBF80u);
          zre[at] = mfma(C, yr[t], zre[at]); zim[at] = mfma(C, yi[t], zim[at]);
          zre[at] = mfma(S, yn[t], zre[at]); zim[at] = mfma(S, yr[t], zim[at]);
        }
    }
    STAMP(2, 5);
    RAW_BARRIER();
    {
      float* zp = zs + ((r ? 2 * r - 1 : 0) * 32 + l31) * ZS + 8 * h;
#pragma unroll
      for (int at = 0; at < 2; ++at)
#pragma unroll
        for (int qd = 0; qd < 4; ++qd) {
          const int off = at * 32 + 16 * (qd >> 1) + 4 * (qd & 1);
          *(f32x4*)(zp + off) = f32x4{zre[at][4 * qd], zre[at][4 * qd + 1], zre[at][4 * qd + 2], zre[at][4 * qd + 3]};
          if (r) *(f32x4*)(zp + 32 * ZS + off) = f32x4{zim[at][4 * qd], zim[at][4 * qd + 1], zim[at][4 * qd + 2], zim[at][4 * qd + 3]};
        }
    }
    RAW_BARRIER();
    STAMP(2, 6);
    {
      const int o = tid & 7, row = tid >> 3;
      float* zp = zs + row * ZS + 8 * o;
      f32x4 zl[7], zh[7];
#pragma unroll
      for (int c = 0; c < 7; ++c) { zl[c] = *(const f32x4*)(zp + c * 32 * ZS); zh[c] = *(const f32x4*)(zp + c * 32 * ZS + 4); }
      float y[8][7];
#pragma unroll
      for (int jp = 0; jp < 4; ++jp) {
        f32x2 z[7];
#pragma unroll
        for (int c = 0; c < 7; ++c) z[c] = jp < 2 ? f32x2{zl[c][2 * jp], zl[c][2 * jp + 1]} : f32x2{zh[c][2 * jp - 4], zh[c][2 * jp - 3]};
        f32x2 y0 = z[0] + z[1] + z[3] + z[5];
        f32x2 e1 = z[0] + z[1] * C71 + z[3] * C72 + z[5] * C73, o1 = z[2] * S71 + z[4] * S72 + z[6] * S73;
        f32x2 e2 = z[0] + z[1] * C72 + z[3] * C73 + z[5] * C71, o2 = z[2] * S72 - z[4] * S73 - z[6] * S71;
        f32x2 e3 = z[0] + z[1] * C73 + z[3] * C71 + z[5] * C72, o3 = z[2] * S73 - z[4] * S71 + z[6] * S72;
        f32x2 yy[7] = {y0, e1 - o1, e2 - o2, e3 - o3, e3 + o3, e2 + o2, e1 + o1};
#pragma unroll
        for (int b = 0; b < 7; ++b) { y[2 * jp][b] = yy[b][0]; y[2 * jp + 1][b] = yy[b][1]; }
      }
#pragma unroll
      for (int b = 0; b < 7; ++b) {
        u32x4 v;
#pragma unroll
        for (int jp = 0; jp < 4; ++jp) v[jp] = pk2(y[2 * jp][(b + 2 * jp) % 7], y[2 * jp + 1][(b + 2 * jp + 1) % 7]);
        int m = b - o; m += m < 0 ? 7 : 0; m += m < 0 ? 7 : 0;
        *(u32x4*)(zp + m * 32 * ZS) = v;
      }
      u16* dst = out + (s2 + 32 * (32 * sh + row)) * 896 + 448 + 8 * o;
#pragma unroll
      for (int m = 0; m < 7; ++m) *(u32x4*)(dst + 64 * m) = *(const u32x4*)(zp + m * 32 * ZS);
    }
  }
  STAMP(2, 12); STAMP(2, 15);
}

extern "C" void kernel_launch(void* const* d_in, const int* in_sizes, int n_in, void* d_out, int out_size, void* d_ws, size_t ws_size,
                              hipStream_t stream) {
  const u16* ax = (const u16*)d_in[0];
  const u16* wqkv = (const u16*)d_in[1];
  const u16* wo = (const u16*)d_in[2];
  const u16* bx = (const u16*)d_in[3];
  const u16* wr = (const u16*)d_in[4];
  const u16* wi = (const u16*)d_in[5];
  char* ws = (char*)d_ws;
  u32x4* Qf = (u32x4*)(ws);
  u32x4* KVf = (u32x4*)(ws + (2u << 20));
  u32x4* Of = (u32x4*)(ws + (6u << 20));
  unsigned* T = (unsigned*)(ws + (8u << 20));
  unsigned* U = (unsigned*)(ws + (10u << 20));
  u32x4* Wf = (u32x4*)(ws + (12u << 20));
  (void)hipFuncSetAttribute((const void*)k1_kernel, hipFuncAttributeMaxDynamicSharedMemorySize, K1_LDS);
  (void)hipFuncSetAttribute((const void*)k2_kernel, hipFuncAttributeMaxDynamicSharedMemorySize, K2_LDS);
  (void)hipFuncSetAttribute((const void*)k3_kernel, hipFuncAttributeMaxDynamicSharedMemorySize, K3_LDS);
  k1_kernel<<<256, 512, K1_LDS, stream>>>(ax, wqkv, bx, wr, wi, Qf, KVf, (u32x4*)T, Wf);
  k2_kernel<<<256, 512, K2_LDS, stream>>>(Qf, KVf, Of, T, U, Wf);
  k3_kernel<<<288, 256, K3_LDS, stream>>>(Of, wo, (const u32x4*)U, (u16*)d_out);
}
```

```cpp
#include <hip/hip_runtime.h>
#include <hip/hip_bf16.h>

typedef __attribute__((ext_vector_type(8))) __bf16 bf16x8;
typedef __attribute__((ext_vector_type(4))) __bf16 bf16x4;
typedef __attribute__((ext_vector_type(2))) __bf16 bf16x2;
typedef __attribute__((ext_vector_type(4))) short s16x4;
typedef __attribute__((ext_vector_type(16))) float f32x16;
typedef __attribute__((ext_vector_type(4))) float f32x4;
typedef __attribute__((ext_vector_type(2))) float f32x2;
typedef __attribute__((ext_vector_type(4))) unsigned u32x4;
typedef __attribute__((ext_vector_type(2))) unsigned u32x2;
typedef unsigned short u16;
#define LDSP __attribute__((address_space(3)))
#define DEV __device__ __forceinline__

DEV unsigned pk2(float lo, float hi) {
  f32x2 v = {lo, hi};
  return __builtin_bit_cast(unsigned, __builtin_convertvector(v, bf16x2));
}
DEV f32x16 mfma(u32x4 a, u32x4 b, f32x16 c) {
  return __builtin_amdgcn_mfma_f32_32x32x16_bf16(__builtin_bit_cast(bf16x8, a), __builtin_bit_cast(bf16x8, b), c, 0, 0, 0);
}
template <int S> DEV u32x4 packs(const f32x16& v) {
  u32x4 r = {pk2(v[8 * S + 0], v[8 * S + 1]), pk2(v[8 * S + 2], v[8 * S + 3]), pk2(v[8 * S + 4], v[8 * S + 5]), pk2(v[8 * S + 6], v[8 * S + 7])};
  return r;
}
template <int S> DEV u32x4 packsn(const f32x16& v) {
  u32x4 r = {pk2(-v[8 * S + 0], -v[8 * S + 1]), pk2(-v[8 * S + 2], -v[8 * S + 3]), pk2(-v[8 * S + 4], -v[8 * S + 5]), pk2(-v[8 * S + 6], -v[8 * S + 7])};
  return r;
}
DEV int sw23(int r) { return (r & ~12) | ((r & 4) << 1) | ((r & 8) >> 1); }
DEV float cosr(int n, float inv) { return __builtin_amdgcn_cosf((float)n * inv); }
DEV float sinr(int n, float inv) { return __builtin_amdgcn_sinf((float)n * inv); }
DEV void glds16(const void* g, char* l) {
  __builtin_amdgcn_global_load_lds((const unsigned*)g, (LDSP unsigned*)l, 16, 0, 0);
}
DEV void dma16(const void* base, unsigned voff, unsigned ldsaddr) {
  asm volatile("s_mov_b32 m0, %2\n\ts_nop 0\n\tglobal_load_lds_dwordx4 %0, %1" ::"v"(voff), "s"(base), "s"(ldsaddr) : "memory");
}
DEV unsigned ldsaddr_of(const char* p) { return (unsigned)(unsigned long long)(const LDSP char*)p; }
DEV u32x4 ald128(const void* base, unsigned off) {
  u32x4 r; asm volatile("global_load_dwordx4 %0, %1, %2" : "=v"(r) : "v"(off), "s"(base) : "memory"); return r;
}
DEV void st128(void* p, u32x4 v) { asm volatile("global_store_dwordx4 %0, %1, off sc1\n\ts_nop 1" ::"v"(p), "v"(v) : "memory"); }
#define SB() __builtin_amdgcn_sched_barrier(0)
DEV u32x2 ldtr(const char* l) {
  return __builtin_bit_cast(u32x2, __builtin_amdgcn_ds_read_tr16_b64_v4i16((LDSP s16x4*)l));
}
DEV float bflo(unsigned u) { return __builtin_bit_cast(float, u << 16); }
DEV float bfhi(unsigned u) { return __builtin_bit_cast(float, u & 0xffff0000u); }
#define RAW_BARRIER() do { asm volatile("s_waitcnt lgkmcnt(0)" ::: "memory"); __builtin_amdgcn_s_barrier(); asm volatile("" ::: "memory"); } while (0)
#define VMCNT(n) asm volatile("s_waitcnt vmcnt(" #n ")" ::: "memory")

#define STAMP_INIT
#define STAMP(k, i)
constexpr float C71 = 0.62348980185873353f, C72 = -0.22252093395631440f, C73 = -0.90096886790241913f;
constexpr float S71 = 0.78183148246802981f, S72 = 0.97492791218182361f, S73 = 0.43388373911755812f;

DEV void gen_cs(int i, int k0, int mul, int add, int mask, float inv, u32x4& C, u32x4& S) {
  float c[8], s[8];
#pragma unroll
  for (int j = 0; j < 8; ++j) {
    int n = (i * (mul * (k0 + j) + add)) & mask;
    c[j] = cosr(n, inv); s[j] = sinr(n, inv);
  }
#pragma unroll
  for (int j = 0; j < 4; ++j) { C[j] = pk2(c[2 * j], c[2 * j + 1]); S[j] = pk2(s[2 * j], s[2 * j + 1]); }
}

constexpr double ccos64(int n) {
  n &= 63; if (n > 32) n = 64 - n;
  double sg = 1.0; if (n > 16) { n = 32 - n; sg = -1.0; }
  if (n == 16) return 0.0;
  double x = 6.283185307179586476925 * n / 64.0, x2 = x * x, term = 1.0, sum = 1.0;
  for (int i = 1; i < 14; ++i) { term *= -x2 / ((2 * i - 1) * (2 * i)); sum += term; }
  return sg * sum;
}
constexpr unsigned cbf(double v) { unsigned u = __builtin_bit_cast(unsigned, (float)v); return (u + 0x7fffu + ((u >> 16) & 1u)) >> 16; }
constexpr int csw(int r) { return (r & ~12) | ((r & 4) << 1) | ((r & 8) >> 1); }
struct alignas(16) Tabs { unsigned f64[16 * 256]; unsigned b2[14 * 256]; };
constexpr Tabs make_tabs() {
  Tabs t{};
  unsigned c[64] = {}, sn[64] = {};
  for (int n = 0; n < 64; ++n) { c[n] = cbf(ccos64(n)); sn[n] = cbf(ccos64(n - 16 + 64)); }
  for (int f = 0; f < 8; ++f)
    for (int l = 0; l < 64; ++l)
      for (int d = 0; d < 4; ++d) {
        int i = 32 * (f >> 2) + (l & 31), k = 16 * (f & 3) + 8 * (l >> 5) + 2 * d;
        t.f64[((f * 2) * 64 + l) * 4 + d] = c[(i * k) & 63] | (c[(i * (k + 1)) & 63] << 16);
        t.f64[((f * 2 + 1) * 64 + l) * 4 + d] = sn[(i * k) & 63] | (sn[(i * (k + 1)) & 63] << 16);
      }
  for (int l = 0; l < 64; ++l)
    for (int d = 0; d < 4; ++d) {
      for (int f = 0; f < 4; ++f) {
        int n = (2 * csw(l & 31) * (8 * f + 4 * (l >> 5) + d)) & 63;
        t.b2[(f * 64 + l) * 4 + d] = c[n] | (sn[n] << 16);
        t.b2[((4 + f) * 64 + l) * 4 + d] = (sn[n] ^ 0x8000u) | (c[n] << 16);
      }
      for (int f = 0; f < 2; ++f) {
        int k = 16 * f + 8 * (l >> 5) + 2 * d, n0 = (2 * (l & 31) * k) & 63, n1 = (2 * (l & 31) * (k + 1)) & 63;
        t.b2[((8 + f) * 64 + l) * 4 + d] = c[n0] | (c[n1] << 16);
        t.b2[((10 + f) * 64 + l) * 4 + d] = sn[n0] | (sn[n1] << 16);
        t.b2[((12 + f) * 64 + l) * 4 + d] = (sn[n0] | (sn[n1] << 16)) ^ 0x80008000u;
      }
    }
  return t;
}
__device__ const Tabs g_tabs = make_tabs();
DEV unsigned ald32(const void* base, unsigned off) {
  unsigned r; asm volatile("global_load_dword %0, %1, %2" : "=v"(r) : "v"(off), "s"(base) : "memory"); return r;
}
DEV void ast32(void* base, unsigned off, unsigned v) { asm volatile("global_store_dword %0, %1, %2" ::"v"(off), "v"(v), "s"(base) : "memory"); }
DEV int col_bin(int c) { int ct = c >> 5, k = 7 * ((ct ? ((ct + 1) & 1) * 32 : 0) + (c & 31)) + 64 * ((ct + 1) >> 1); return k >= 448 ? k - 448 : k; }

constexpr int K1_SLOT = 32 * 1024;
constexpr int K1_LDS = 152 * 1024;
struct QF { u32x4 x0[4], x1[4], w[4]; };

template <bool ISV>
DEV void qkv_loop(const char* lds, unsigned ld, const u16* xb, const u16* wb, int kn, unsigned xo, unsigned wo0, unsigned wo1,
                  int xoff, int woff, f32x16& a0, f32x16& a1) {
  auto piece = [&](int slot, int i) {
    const unsigned d = ld + slot * K1_SLOT;
    if (i == 0) dma16(xb + kn * 64, xo, d);
    if (i == 1) dma16(wb + kn * (64 * 1344), wo0, d + 16384);
    if (i == 2) dma16(xb + kn * 64 + 64 * 448, xo, d + 8192);
    if (i == 3) { dma16(wb + kn * (64 * 1344), wo1, d + 24576); kn = kn == 6 ? 0 : kn + 1; }
  };
  auto rd = [&](QF& f, int slot, int ks) {
    const char* sl = lds + slot * K1_SLOT;
    f.x0[ks] = *(const u32x4*)(sl + (xoff ^ (ks * 32)));
    f.x1[ks] = *(const u32x4*)(sl + (xoff ^ (ks * 32)) + 4096);
    u32x2 wa = ldtr(sl + woff + ks * 16 * 224), wc = ldtr(sl + woff + ks * 16 * 224 + 224);
    f.w[ks] = u32x4{wa[0], wa[1], wc[0], wc[1]};
  };
#pragma unroll
  for (int i = 0; i < 8; ++i) piece(i >> 2, i & 3);
  QF f[2];
#pragma unroll
  for (int kc = 0; kc < 8; ++kc) {
    if (kc < 7) { if (kc < 6) VMCNT(4); else VMCNT(0); __builtin_amdgcn_s_barrier(); asm volatile("" ::: "memory"); }
#pragma unroll
    for (int ks = 0; ks < 4; ++ks) {
      if (kc < 7) rd(f[kc & 1], kc & 3, ks);
      if (kc > 0) {
        QF& g = f[(kc - 1) & 1];
        if (ISV) { a0 = mfma(g.x0[ks], g.w[ks], a0); a1 = mfma(g.x1[ks], g.w[ks], a1); }
        else     { a0 = mfma(g.w[ks], g.x0[ks], a0); a1 = mfma(g.w[ks], g.x1[ks], a1); }
      }
      if (kc < 5) piece((kc + 2) & 3, ks);
      SB();
    }
  }
}

__global__ __launch_bounds__(512, 2) void k1_kernel(const u16* __restrict__ ax, const u16* __restrict__ wqkv, const u16* __restrict__ bx,
                                                    const u16* __restrict__ Wr, const u16* __restrict__ Wi,
                                                    u32x4* __restrict__ Qf, u32x4* __restrict__ KVf, u32x4* __restrict__ T, u32x4* __restrict__ Wf) {
  extern __shared__ __attribute__((aligned(16))) char lds[];
  const int tid = threadIdx.x, lane = tid & 63, w = __builtin_amdgcn_readfirstlane(tid >> 6);
  const int xcd = blockIdx.x & 7, idx = blockIdx.x >> 3;
  const int l31 = lane & 31, h = lane >> 5;
  const unsigned lb = ldsaddr_of(lds);
  STAMP_INIT; STAMP(0, 0); STAMP(0, 1);
  if (idx >= 8) {
    const int u = idx - 8, which = u >> 3, tm = (xcd & 1) * 8 + (u & 7), g = xcd >> 1;
    const int row = 8 * w + (lane >> 3);
    const unsigned xo = (row * 448 + ((lane & 7) ^ ((row >> 1) & 7)) * 8) * 2;
    unsigned wo2[2];
#pragma unroll
    for (int i = 0; i < 2; ++i) {
      int q = 64 * (w + 8 * i) + lane; if (q > 895) q = 895;
      int rho = q / 14, cc = q - rho * 14;
      wo2[i] = (((rho & ~7) + ((rho & 7) >> 1) + 4 * (rho & 1)) * 1344 + cc * 8) * 2;
    }
    const int hh = w & 3, th = w >> 2, r0 = th * 64 + l31;
    const int xoff = r0 * 128 + ((h ^ ((r0 >> 1) & 7)) * 16);
    const int woff = 16384 + (8 * h + 2 * ((lane >> 2) & 3)) * 224 + (hh * 28 + ((lane >> 4) & 1) * 16 + 4 * (lane & 3)) * 2;
    const int head = g * 4 + hh, tile = tm * 4 + th * 2, kn = ((u & 7) + 2 * which) % 7;
    f32x16 a0 = {}, a1 = {};
    u32x4 o0, o1, o2, o3;
    if (which == 2) {
      qkv_loop<true>(lds, lb + w * 1024, ax + tm * 128 * 448, wqkv + 896 + g * 112, kn, xo, wo2[0], wo2[1], xoff, woff, a0, a1);
      o0 = packs<0>(a0); o1 = packs<1>(a0); o2 = packs<0>(a1); o3 = packs<1>(a1);
      if (l31 >= 28) { unsigned f = (l31 == 28) ? 0x3F803F80u : 0u; o0 = u32x4{f, f, f, f}; o1 = o0; o2 = o0; o3 = o0; }
    } else {
      qkv_loop<false>(lds, lb + w * 1024, ax + tm * 128 * 448, wqkv + which * 448 + g * 112, kn, xo, wo2[0], wo2[1], xoff, woff, a0, a1);
      const float sc = which ? 1.0f : 0.18898223650461362f * 1.4426950408889634f;
      a0 *= sc; a1 *= sc;
      o0 = packs<0>(a0); o1 = packs<1>(a0); o2 = packs<0>(a1); o3 = packs<1>(a1);
      if (h) { o1[2] = which ? 0x3F80u : 0u; o1[3] = 0; o3[2] = o1[2]; o3[3] = 0; }
    }
    STAMP(0, 7);
    u32x4* o = (which == 0 ? Qf + ((head * 64 + tile) * 2) * 64 : KVf + ((head * 64 + tile) * 4 + (which & 2)) * 64) + lane;
    const int st = which == 0 ? 128 : 256;
    st128(o, o0); st128(o + 64, o1); st128(o + st, o2); st128(o + st + 64, o3);
  } else {
    const int b1 = xcd * 8 + idx, s2 = b1 >> 1, hb = b1 & 1, jq = b1 >> 2, gq = b1 & 3;
    u32x4* F64 = (u32x4*)(lds + 56 * 1024);
    u32x4* Ap = (u32x4*)(lds + 72 * 1024);
    {
      const int row = 8 * w + (lane >> 3);
      const unsigned xo = ((s2 + 32 * ((row & 32) + sw23(row & 31))) * 448 + ((lane & 7) ^ ((row >> 1) & 7)) * 8) * 2;
#pragma unroll
      for (int m = 0; m < 7; ++m) dma16(bx + 64 * m, xo, lb + m * 8192 + w * 1024);
    }
    SB();
    const int mt = w >> 2, o = 2 * (w & 3) + h;
    {
      u32x4 C, S;
      gen_cs(32 * mt + l31, 8 * o, 32, s2, 2047, 1.0f / 2048, C, S);
      Ap[(w * 2 + 0) * 64 + lane] = C; Ap[(w * 2 + 1) * 64 + lane] = S;
    }
    STAMP(0, 2);
    VMCNT(0);
    RAW_BARRIER();
    STAMP(0, 3);
    {
      const int row = 32 * mt + l31;
      char* xp = lds + row * 128 + ((o ^ ((row >> 1) & 7)) * 16);
      u32x4 xr[7], z[4];
#pragma unroll
      for (int b = 0; b < 7; ++b) { int m = b - o; m += m < 0 ? 7 : 0; m += m < 0 ? 7 : 0; xr[b] = *(const u32x4*)(xp + m * 8192); }
      const float ca = hb ? C72 : C71, cb = hb ? C73 : C72, cc = hb ? C71 : C73, sa = hb ? S72 : S71, sb = hb ? -S73 : S72, sc = hb ? -S71 : S73;
      const float da = hb ? C73 : 1.0f, db = hb ? C71 : 1.0f, dc = hb ? C72 : 1.0f, ta = hb ? S73 : 0.0f, tb = hb ? -S71 : 0.0f, tc = hb ? S72 : 0.0f;
      const int gg = 4 * gq + (w & 3), jb = 4 * jq + 64 * ((gg & 7) + 8 * (w >> 2) + 16 * (gg >> 3));
#pragma unroll
      for (int jp = 0; jp < 4; ++jp) {
        {
          int j = jb + (jp & 1) * 2 + h; if (jp >> 1) j = (2048 - j) & 2047;
          unsigned off = ((j * 450) & ~15u) + l31 * 16;
          off = off > 921584u ? 921584u : off;
          if (jp < 2) dma16(g_tabs.f64 + w * 512 + jp * 256, lane * 16, lb + 56 * 1024 + w * 2048 + jp * 1024);
          dma16(Wr, off, lb + 88 * 1024 + w * 8192 + jp * 1024); dma16(Wi, off, lb + 92 * 1024 + w * 8192 + jp * 1024);
          SB();
        }
        f32x2 v[7];
#pragma unroll
        for (int b = 0; b < 7; ++b) {
          v[b][0] = bflo(xr[(b - 2 * jp + 14) % 7][jp]);
          v[b][1] = bfhi(xr[(b - 2 * jp - 1 + 14) % 7][jp]);
        }
        f32x2 p1 = v[1] + v[6], p2 = v[2] + v[5], p3 = v[3] + v[4];
        f32x2 m1 = v[6] - v[1], m2 = v[5] - v[2], m3 = v[4] - v[3];
        f32x2 ra = v[0] + p1 * ca + p2 * cb + p3 * cc, ia = m1 * sa + m2 * sb + m3 * sc;
        f32x2 rb = v[0] + p1 * da + p2 * db + p3 * dc, ib = m1 * ta + m2 * tb + m3 * tc;
        z[0][jp] = pk2(ra[0], ra[1]); z[1][jp] = pk2(ia[0], ia[1]); z[2][jp] = pk2(rb[0], rb[1]); z[3][jp] = pk2(ib[0], ib[1]);
      }
#pragma unroll
      for (int c = 0; c < 4; ++c) *(u32x4*)(xp + c * 8192) = z[c];
    }
    VMCNT(0);
    RAW_BARRIER();
    STAMP(0, 4);
    if (w < 4) {
      const int ct = hb ? 3 + w : (w == 2 ? 0 : w + 1), kt = w & 1;
      if (hb || w < 3) {
        __builtin_amdgcn_s_setprio(2);
        f32x16 xre[2] = {}, xim[2] = {};
        const char* zp = lds + (w & 2) * 8192 + l31 * 128;
        const int zsw = (h ^ ((l31 >> 1) & 7)) * 16;
#pragma unroll
        for (int tt = 0; tt < 4; ++tt) {
          u32x4 C = F64[((kt * 4 + tt) * 2) * 64 + lane], S = F64[((kt * 4 + tt) * 2 + 1) * 64 + lane];
          u32x4 nS = S ^ 0x80008000u;
          if (ct == 0 && l31 == 0) nS = u32x4{0xBF803F80u, 0xBF803F80u, 0xBF803F80u, 0xBF803F80u};
#pragma unroll
          for (int m = 0; m < 2; ++m) {
            u32x4 zr = *(const u32x4*)(zp + m * 4096 + (zsw ^ (tt * 32))), zi = *(const u32x4*)(zp + 8192 + m * 4096 + (zsw ^ (tt * 32)));
            xre[m] = mfma(zr, C, xre[m]); xim[m] = mfma(zi, C, xim[m]);
            xre[m] = mfma(zi, S, xre[m]); xim[m] = mfma(zr, nS, xim[m]);
          }
        }
        STAMP(0, 5);
        u32x4 bre[4] = {packs<0>(xre[0]), packs<1>(xre[0]), packs<0>(xre[1]), packs<1>(xre[1])};
        u32x4 bim[4] = {packs<0>(xim[0]), packs<1>(xim[0]), packs<0>(xim[1]), packs<1>(xim[1])};
        u32x4 bnr[4] = {packsn<0>(xre[0]), packsn<1>(xre[0]), packsn<0>(xre[1]), packsn<1>(xre[1])};
        u32x4* tp = T + (s2 * 16 + h) * 224 + ct * 32 + l31;
#pragma unroll
        for (int jt = 0; jt < 2; ++jt) {
          f32x16 tre = {}, tim = {};
#pragma unroll
          for (int tt = 0; tt < 4; ++tt) {
            u32x4 C = Ap[((jt * 4 + tt) * 2) * 64 + lane], S = Ap[((jt * 4 + tt) * 2 + 1) * 64 + lane];
            tre = mfma(C, bre[tt], tre); tim = mfma(C, bim[tt], tim);
            tre = mfma(S, bim[tt], tre); tim = mfma(S, bnr[tt], tim);
          }
#pragma unroll
          for (int q = 0; q < 4; ++q)
            st128(tp + (jt * 8 + 2 * q) * 224, u32x4{pk2(tre[4 * q], tim[4 * q]), pk2(tre[4 * q + 1], tim[4 * q + 1]), pk2(tre[4 * q + 2], tim[4 * q + 2]), pk2(tre[4 * q + 3], tim[4 * q + 3])});
        }
      }
    } else {
      const int i = lane & 3, j1 = 4 * jq + i;
      const char* st = lds + 88 * 1024 + h * 32768 + i * 512;
      const int al0 = (j1 * 450) & 15, al1 = 2048 + ((-j1 * 450) & 15);
      u32x4* wp = Wf + (jq * 29 * 4 + gq) * 64 + lane;
#pragma unroll
      for (int n = 0; n < 7; ++n) {
        const int cg = (w & 3) * 7 + n, k = col_bin(cg * 8 + (l31 >> 2));
        const bool cj = k > 224;
        const char* a = st + (cj ? al1 + 2 * (448 - k) : al0 + 2 * k);
        u32x4 v;
#pragma unroll
        for (int gi = 0; gi < 4; ++gi) v[gi] = ((unsigned)*(const u16*)(a + gi * 8192) | ((unsigned)*(const u16*)(a + 4096 + gi * 8192) << 16)) ^ (cj ? 0x80000000u : 0u);
        st128(wp + cg * 256, v);
      }
      if (w == 7) {
        const char* a = st + al0 + (l31 & 4) * 112, *b = st + al1 + (l31 & 4) * 112;
        u32x4 v;
#pragma unroll
        for (int gi = 0; gi < 4; ++gi)
          v[gi] = pk2(0.25f * (bflo(*(const u16*)(a + gi * 8192)) + bflo(*(const u16*)(b + gi * 8192))),
                      0.25f * (bflo(*(const u16*)(a + 4096 + gi * 8192)) - bflo(*(const u16*)(b + 4096 + gi * 8192))));
        st128(wp + 28 * 256, v);
      }
    }
  }
  STAMP(0, 12); STAMP(0, 15);
}

constexpr int K2_LDS = 128 * 1024;
DEV float xhalf_max(float v) {
  float a = v, b = v;
  asm volatile("s_nop 1\n\tv_permlane32_swap_b32 %0, %1\n\ts_nop 1" : "+v"(a), "+v"(b));
  return fmaxf(a, b);
}
DEV float ror4(float v) { return __builtin_bit_cast(float, __builtin_amdgcn_mov_dpp(__builtin_bit_cast(int, v), 0x124, 0xF, 0xF, true)); }
DEV float max16(const f32x16& s) {
  float m = fmaxf(fmaxf(s[0], s[1]), s[2]);
#pragma unroll
  for (int i = 3; i < 15; i += 2) m = fmaxf(fmaxf(m, s[i]), s[i + 1]);
  return fmaxf(m, s[15]);
}
#define PIN16(a) asm volatile("" : "+v"(a[0]), "+v"(a[1]), "+v"(a[2]), "+v"(a[3]), "+v"(a[4]), "+v"(a[5]), "+v"(a[6]), "+v"(a[7]), \
                                   "+v"(a[8]), "+v"(a[9]), "+v"(a[10]), "+v"(a[11]), "+v"(a[12]), "+v"(a[13]), "+v"(a[14]), "+v"(a[15]))
#define EX(s, i) s[i] = __builtin_amdgcn_exp2f(s[i])
#define TILE(sc, sn, PV, PK2, DMA)                                                   \
  sn = mfma(k0, q0, f32x16{}); v0 = *(const u32x4*)(PV);                             \
  EX(sc, 0); EX(sc, 1); EX(sc, 2); EX(sc, 3); SB();                                  \
  o = mfma(v1, p1, o); k0 = *(const u32x4*)(PK2);                                    \
  EX(sc, 4); EX(sc, 5); EX(sc, 6); EX(sc, 7); SB();                                  \
  sn = mfma(k1, q1m, sn); v1 = *(const u32x4*)((PV) + 1024);                         \
  p0 = packs<0>(sc); EX(sc, 8); EX(sc, 9); EX(sc, 10); SB();                         \
  o = mfma(v0, p0, o); k1 = *(const u32x4*)((PK2) + 1024); DMA;                      \
  EX(sc, 11); EX(sc, 12); EX(sc, 13); EX(sc, 14); EX(sc, 15); p1 = packs<1>(sc); SB();
#define BAR() do { __builtin_amdgcn_s_barrier(); asm volatile("" ::: "memory"); SB(); } while (0)
#define CHUNK(D0, D1, SYNC, D2, D3)                                                  \
  { const char* cb = tb + (c & 3) * 32768; const char* nb = tb + ((c + 1) & 3) * 32768; \
    TILE(sa, sb, cb + 2048, cb + 8192, D0)                                           \
    TILE(sb, sa, cb + 6144, cb + 12288, D1)                                          \
    SYNC;                                                                            \
    TILE(sa, sb, cb + 10240, nb, D2)                                                 \
    TILE(sb, sa, cb + 14336, nb + 4096, D3) }

__global__ __launch_bounds__(512, 2) void k2_kernel(const u32x4* __restrict__ Qf, const u32x4* __restrict__ KVf, u32x4* __restrict__ Of,
                                                    const unsigned* __restrict__ T, unsigned* __restrict__ U, const u32x4* __restrict__ Wf) {
  extern __shared__ __attribute__((aligned(16))) char lds[];
  const int tid = threadIdx.x, lane = tid & 63, w = __builtin_amdgcn_readfirstlane(tid >> 6);
  const int xcd = blockIdx.x & 7, idx = blockIdx.x >> 3;
  const int l31 = lane & 31, h = lane >> 5;
  STAMP_INIT; STAMP(1, 0); STAMP(1, 1);
  const int head = xcd * 2 + (idx >> 4), qt = (idx & 15) * 4 + (w & 3), grp = w >> 2;
  const char* kvb = (const char*)KVf + head * 262144 + w * 1024;
  const unsigned lb = ldsaddr_of(lds) + w * 1024, l16 = lane * 16;
#define KVDMA(c, i) dma16(kvb + (c) * 32768 + (i) * 8192, l16, lb + ((c) & 3) * 32768 + (i) * 8192)
  u32x4 q0 = ald128(Qf + (head * 64 + qt) * 128, l16), q1 = ald128(Qf + (head * 64 + qt) * 128 + 64, l16);
  KVDMA(0, 0); KVDMA(0, 2); KVDMA(0, 1); KVDMA(0, 3);
  asm volatile("s_waitcnt vmcnt(0)" : "+v"(q0), "+v"(q1) :: "memory");
  STAMP(1, 2);
  RAW_BARRIER();
  STAMP(1, 3);
  const char* tb = lds + grp * 16384 + lane * 16;
  f32x16 o = {}, sa, sb;
  u32x4 k0, k1, v0, v1 = {}, p0, p1 = {}, q1m = q1;
  float m;
  {
    sa = mfma(*(const u32x4*)tb, q0, f32x16{}); sa = mfma(*(const u32x4*)(tb + 1024), q1, sa);
    k0 = *(const u32x4*)(tb + 4096); k1 = *(const u32x4*)(tb + 5120);
    KVDMA(1, 0); KVDMA(1, 2); KVDMA(1, 1); KVDMA(1, 3);
    const unsigned nm = pk2(-xhalf_max(max16(sa)), 0.0f);
    m = -bflo(nm);
    if (h) q1m[2] = nm;
    sa -= m;
  }
  int c = 0;
#pragma unroll 1
  for (; c < 6; ++c) CHUNK(KVDMA(c + 2, 0), KVDMA(c + 2, 1), VMCNT(2); BAR(), KVDMA(c + 2, 2), KVDMA(c + 2, 3))
  const int tk = idx * 2 + w;
  const bool b2 = w < 2 && tk < 58;
  const int jq = xcd * 2 + (tk >= 29), cg = tk >= 29 ? tk - 29 : tk, j1 = 4 * jq + (lane & 3);
  const bool sp = cg == 28;
  const unsigned toff = ((4 * h * 16 + jq) * 896 + (sp ? (lane & 3) : cg * 32 + l31)) * 4;
  unsigned ta[16], tc[16];
  u32x4 wf[4], mt[14];
  CHUNK(, , VMCNT(0); BAR(), , )
  if (b2) {
    const int jm = (64 - j1) & 63;
#pragma unroll
    for (int i = 0; i < 16; ++i) ta[i] = ald32(T + (8 * (i >> 2) + (i & 3)) * 14336, toff);
#pragma unroll
    for (int t = 0; t < 4; ++t) wf[t] = ald128(Wf + ((jq * 29 + cg) * 4 + t) * 64, l16);
#pragma unroll
    for (int t = 0; t < 14; ++t) mt[t] = ald128(g_tabs.b2 + t * 256, l16);
    if (__builtin_expect(sp, 0)) {
#pragma unroll
      for (int i = 0; i < 16; ++i) tc[i] = ald32(T + (8 * (i >> 2) + (i & 3)) * 14336, ((4 * h * 16 + (jm >> 2)) * 896 + (jm & 3)) * 4);
    }
  }
  c = 7;
  CHUNK(, , , , )
  o = mfma(v1, p1, o);
  STAMP(1, 11);
  if (__builtin_expect(__ballot(h && !(o[12] < 1.0e12f)) != 0ull, 0)) {
    VMCNT(0);
    o = f32x16{}; m = -3.0e38f;
#pragma unroll 1
    for (int t = 0; t < 32; ++t) {
      const u32x4* p = KVf + (head * 64 + (t >> 2) * 8 + grp * 4 + (t & 3)) * 256 + lane;
      f32x16 s = mfma(p[0], q0, f32x16{}); s = mfma(p[64], q1, s);
      float mn = fmaxf(m, xhalf_max(max16(s)));
      float al = __builtin_amdgcn_exp2f(m - mn);
      m = mn;
#pragma unroll
      for (int i = 0; i < 16; ++i) { o[i] *= al; s[i] = __builtin_amdgcn_exp2f(s[i] - mn); }
      o = mfma(p[128], packs<0>(s), o); o = mfma(p[192], packs<1>(s), o);
    }
  }
  f32x4* ex = (f32x4*)(lds + 32768 + w * 3072) + lane;
  const f32x4* ey = (const f32x4*)(lds + 32768 + (w ^ 4) * 3072) + lane;
  const float lsum = xhalf_max(h ? o[12] : -1.0f);
  f32x4 xa, xb;
  if (grp) {
    ex[0] = f32x4{o[0], o[1], o[2], o[3]}; ex[64] = f32x4{o[4], o[5], o[6], o[7]};
    xa = f32x4{o[8], o[9], o[10], o[11]}; xb = f32x4{o[12], o[13], o[14], o[15]};
  } else {
    ex[0] = f32x4{o[8], o[9], o[10], o[11]}; ex[64] = f32x4{o[12], o[13], o[14], o[15]};
    xa = f32x4{o[0], o[1], o[2], o[3]}; xb = f32x4{o[4], o[5], o[6], o[7]};
  }
  ex[128] = f32x4{m, lsum, 0.f, 0.f};
  RAW_BARRIER();
  STAMP(1, 12);
  {
    f32x4 ya = ey[0], yb = ey[64], yc = ey[128];
    float mm = fmaxf(m, yc[0]);
    float f1 = __builtin_amdgcn_exp2f(m - mm), f2 = __builtin_amdgcn_exp2f(yc[0] - mm);
    float inv = __builtin_amdgcn_rcpf(lsum * f1 + yc[1] * f2);
    f1 *= inv; f2 *= inv;
    xa = xa * f1 + ya * f2; xb = xb * f1 + yb * f2;
    if (grp && h) xb = f32x4{};
    if (b2) VMCNT(0);
    st128(Of + ((qt * 16 + head) * 2 + grp) * 64 + lane, u32x4{pk2(xa[0], xa[1]), pk2(xa[2], xa[3]), pk2(xb[0], xb[1]), pk2(xb[2], xb[3])});
  }
  STAMP(1, 13);
  if (b2) {
    asm volatile("" : "+v"(mt[0]), "+v"(mt[1]), "+v"(mt[2]), "+v"(mt[3]), "+v"(mt[4]), "+v"(mt[5]), "+v"(mt[6]), "+v"(mt[7]), "+v"(mt[8]), "+v"(mt[9]),
                 "+v"(mt[10]), "+v"(mt[11]), "+v"(mt[12]), "+v"(mt[13]), "+v"(wf[0]), "+v"(wf[1]), "+v"(wf[2]), "+v"(wf[3]));
    PIN16(ta);
    if (__builtin_expect(sp, 0)) {
      PIN16(tc);
      const float sg = (l31 & 4) ? -0.5f : 0.5f;
#pragma unroll
      for (int i = 0; i < 16; ++i) {
        int s2 = j1 ? (8 * (i >> 2) + 4 * h + (i & 3)) : 0;
        float ec = cosr(s2, 1.0f / 32), es = -sinr(s2, 1.0f / 32);
        float br = bflo(tc[i]), bi = -bfhi(tc[i]);
        ta[i] = pk2(0.5f * bflo(ta[i]) + sg * (ec * br - es * bi), 0.5f * bfhi(ta[i]) + sg * (ec * bi + es * br));
      }
    }
    f32x16 xr = {}, xi = {};
#pragma unroll
    for (int t = 0; t < 4; ++t) {
      u32x4 b = {ta[4 * t], ta[4 * t + 1], ta[4 * t + 2], ta[4 * t + 3]};
      xr = mfma(mt[t], b, xr); xi = mfma(mt[4 + t], b, xi);
    }
    f32x16 yr, yi;
#pragma unroll
    for (int g = 0; g < 16; g += 2) {
      f32x2 wr = {bflo(wf[g >> 2][g & 3]), bflo(wf[g >> 2][(g & 3) + 1])}, wi = {bfhi(wf[g >> 2][g & 3]), bfhi(wf[g >> 2][(g & 3) + 1])};
      f32x2 a = {xr[g], xr[g + 1]}, b = {xi[g], xi[g + 1]};
      f32x2 r = a * wr - b * wi, i = a * wi + b * wr;
      yr[g] = r[0]; yr[g + 1] = r[1]; yi[g] = i[0]; yi[g + 1] = i[1];
    }
    u32x4 r0 = packs<0>(yr), r1 = packs<1>(yr), i0 = packs<0>(yi), i1 = packs<1>(yi);
    f32x16 ure = {}, uim = {};
    ure = mfma(mt[8], r0, ure); uim = mfma(mt[8], i0, uim);
    ure = mfma(mt[12], i0, ure); uim = mfma(mt[10], r0, uim);
    ure = mfma(mt[9], r1, ure); uim = mfma(mt[9], i1, uim);
    ure = mfma(mt[13], i1, ure); uim = mfma(mt[11], r1, uim);
    if (__builtin_expect(sp, 0)) {
#pragma unroll
      for (int g = 0; g < 16; ++g) { ure[g] += ror4(ure[g]); uim[g] += ror4(uim[g]); }
    }
    if (sp ? (l31 < 4) : (cg != 0 || l31 >= 4)) {
#pragma unroll
      for (int g = 0; g < 16; ++g) ast32(U + ((g & 3) + 8 * (g >> 2)) * 14336, toff, pk2(ure[g], uim[g]));
    }
  }
  STAMP(1, 14); STAMP(1, 15);
}

constexpr int ZS = 68;
constexpr int K3_LDS = 77 * 1024;

__global__ __launch_bounds__(256, 2) void k3_kernel(const u32x4* __restrict__ Of, const u16* __restrict__ wo, const u32x4* __restrict__ U,
                                                    u16* __restrict__ out) {
  extern __shared__ __attribute__((aligned(16))) char lds[];
  const int tid = threadIdx.x, lane = tid & 63, w = __builtin_amdgcn_readfirstlane(tid >> 6);
  const int xcd = blockIdx.x & 7, idx = blockIdx.x >> 3;
  const int l31 = lane & 31, h = lane >> 5;
  STAMP_INIT; STAMP(2, 0); STAMP(2, 1);
  if (idx >= 8) {
    const int u = idx - 8, tq = xcd * 4 + u / 7, tn = u % 7;
    const int qi = w & 1, kh = w >> 1;
    u32x4 of[16];
    {
      const int r8 = lane >> 3;
      const unsigned so = (r8 * 448 + ((lane & 7) ^ ((r8 & 2) << 1)) * 8) * 2, l16 = lane * 16;
      const u16* src = wo + (kh * 224 + qi * 8) * 448 + tn * 64;
      const unsigned d = ldsaddr_of(lds) + (kh * 224 + qi * 8) * 128;
      const u32x4* op = Of + (((tq * 2 + qi) * 16 + kh * 8) * 2) * 64;
#pragma unroll
      for (int st = 0; st < 2; ++st) {
#pragma unroll
        for (int i = 0; i < 7; ++i) dma16(src + (st * 7 + i) * 16 * 448, so, d + (st * 7 + i) * 2048);
#pragma unroll
        for (int i = 0; i < 8; ++i) of[st * 8 + i] = ald128(op + (st * 8 + i) * 64, l16);
      }
    }
    const int q = (lane >> 2) & 3, p = lane & 3, pc = ((p & 1) << 1) | (p >> 1);
    const int colb = ((lane >> 4) & 1) * 32 + pc * 8;
    const int base = (kh * 224 + q) * 128 + (colb ^ (((q >> 1) ^ kh) << 6));
    f32x16 a0 = {}, a1 = {};
    STAMP(2, 2);
#pragma unroll
    for (int st = 0; st < 2; ++st) {
      if (st == 0) asm volatile("s_waitcnt vmcnt(15)" : "+v"(of[0]), "+v"(of[1]), "+v"(of[2]), "+v"(of[3]), "+v"(of[4]), "+v"(of[5]), "+v"(of[6]), "+v"(of[7]) :: "memory");
      else asm volatile("s_waitcnt vmcnt(0)" : "+v"(of[8]), "+v"(of[9]), "+v"(of[10]), "+v"(of[11]), "+v"(of[12]), "+v"(of[13]), "+v"(of[14]), "+v"(of[15]) :: "memory");
      RAW_BARRIER();
      if (st == 0) STAMP(2, 3); else STAMP(2, 4);
#pragma unroll
      for (int i = st * 8; i < st * 8 + 8; ++i) {
        const int hd = i >> 1, s = i & 1;
        const char* pa = lds + (hd * 28 + 16 * s) * 128;
        const int ra = base + h * 512, rb = (s ? base : base + h * 512) + 1024;
        u32x2 x0 = ldtr(pa + ra), x1 = ldtr(pa + rb), y0 = ldtr(pa + (ra ^ 64)), y1 = ldtr(pa + (rb ^ 64));
        a0 = mfma(u32x4{x0[0], x0[1], x1[0], x1[1]}, of[i], a0);
        a1 = mfma(u32x4{y0[0], y0[1], y1[0], y1[1]}, of[i], a1);
      }
    }
    STAMP(2, 5);
    f32x4* ex = (f32x4*)(lds + 56 * 1024);
    f32x16 snd = a1, kp = a0;
#pragma unroll
    for (int i = 0; i < 4; ++i) ex[(w * 4 + i) * 64 + lane] = f32x4{snd[4 * i], snd[4 * i + 1], snd[4 * i + 2], snd[4 * i + 3]};
    RAW_BARRIER();
#pragma unroll
    for (int i = 0; i < 4; ++i) {
      f32x4 v = ex[((w ^ 2) * 4 + i) * 64 + lane];
      kp[4 * i] += v[0]; kp[4 * i + 1] += v[1]; kp[4 * i + 2] += v[2]; kp[4 * i + 3] += v[3];
    }
    u16* dst = out + ((tq * 2 + qi) * 32 + l31) * 896 + tn * 64 + kh * 32 + 8 * h;
    *(u32x4*)dst = packs<0>(kp); *(u32x4*)(dst + 16) = packs<1>(kp);
  } else {
    const int b3 = xcd * 8 + idx, s2 = b3 >> 1, sh = b3 & 1, r = w;
    u32x4* Dm = (u32x4*)lds;
    u32x4* F64 = (u32x4*)(lds + 61 * 1024);
    float* zs = (float*)lds;
    u32x4 ua[8], ub[8];
    {
      const u32x4* up = U + s2 * 16 * 224 + (r ? 2 * r - 1 : 0) * 32;
      const unsigned uo = (h * 224 + sw23(l31)) * 16, uo2 = r ? uo + 512 : 0;
#pragma unroll
      for (int i = 0; i < 8; ++i) { ua[i] = ald128(up + i * 448, uo); ub[i] = ald128(up + i * 448, uo2); }
      const unsigned pl = (h * 32 + sw23(l31)) * 16;
#pragma unroll
      for (int i = 0; i < 4; ++i) dma16(g_tabs.f64 + (w * 4 + i) * 256, pl, ldsaddr_of(lds) + 61 * 1024 + (w * 4 + i) * 1024);
    }
    SB();
    {
      const int s = s2 + 32 * (32 * sh + l31);
      const float NRM2 = 2.0f / (2048.0f * 448.0f);
#pragma unroll
      for (int tt = 0; tt < 2; ++tt) {
        u32x4 dr, di;
#pragma unroll
        for (int i = 0; i < 4; ++i) {
          int n = ((8 * (2 * w + tt) + 4 * h + i) * s) & 2047;
          float c = cosr(n, 1.0f / 2048) * NRM2, sn = sinr(n, 1.0f / 2048) * NRM2;
          dr[i] = pk2(c, -sn); di[i] = pk2(sn, c);
        }
        Dm[((2 * w + tt) * 2) * 64 + lane] = dr; Dm[((2 * w + tt) * 2 + 1) * 64 + lane] = di;
      }
    }
    STAMP(2, 2);
    RAW_BARRIER();
    STAMP(2, 3);
    f32x16 zre[2] = {}, zim[2] = {};
    {
      f32x16 ar = {}, ai = {}, br = {}, bi = {};
#pragma unroll
      for (int t = 0; t < 8; ++t) {
        u32x4 dr = Dm[(t * 2) * 64 + lane], di = Dm[(t * 2 + 1) * 64 + lane];
        asm volatile("s_waitcnt vmcnt(%2)" : "+v"(ua[t]), "+v"(ub[t]) : "n"(18 - 2 * t) : "memory");
        ar = mfma(ua[t], dr, ar); ai = mfma(ua[t], di, ai); br = mfma(ub[t], dr, br); bi = mfma(ub[t], di, bi);
      }
      STAMP(2, 4);
      VMCNT(0);
      RAW_BARRIER();
      u32x4 yr[4] = {packs<0>(ar), packs<1>(ar), packs<0>(br), packs<1>(br)};
      u32x4 yi[4] = {packs<0>(ai), packs<1>(ai), packs<0>(bi), packs<1>(bi)};
      u32x4 yn[4] = {packsn<0>(ai), packsn<1>(ai), packsn<0>(bi), packsn<1>(bi)};
      if (r == 0) { yr[2] = u32x4{}; yr[3] = u32x4{}; yn[2] = u32x4{}; yn[3] = u32x4{}; }
      float* zp = zs + ((r ? 2 * r - 1 : 0) * 32 + l31) * ZS + 8 * h;
#pragma unroll
      for (int at = 0; at < 2; ++at) {
#pragma unroll
        for (int t = 0; t < 4; ++t) {
          u32x4 C = F64[((at * 4 + t) * 2) * 64 + lane], S = F64[((at * 4 + t) * 2 + 1) * 64 + lane];
          if (t == 0 && r == 0 && h == 0) S[0] = (S[0] & 0xffff0000u) | ((l31 & 1) ? 0x3F80u : 0xBF80u);
          zre[at] = mfma(C, yr[t], zre[at]); zim[at] = mfma(C, yi[t], zim[at]);
          zre[at] = mfma(S, yn[t], zre[at]); zim[at] = mfma(S, yr[t], zim[at]);
          if (at == 1) {
            const int off = 16 * (t >> 1) + 4 * (t & 1);
            *(f32x4*)(zp + off) = f32x4{zre[0][4 * t], zre[0][4 * t + 1], zre[0][4 * t + 2], zre[0][4 * t + 3]};
            if (r) *(f32x4*)(zp + 32 * ZS + off) = f32x4{zim[0][4 * t], zim[0][4 * t + 1], zim[0][4 * t + 2], zim[0][4 * t + 3]};
          }
        }
      }
      STAMP(2, 5);
#pragma unroll
      for (int qd = 0; qd < 4; ++qd) {
        const int off = 32 + 16 * (qd >> 1) + 4 * (qd & 1);
        *(f32x4*)(zp + off) = f32x4{zre[1][4 * qd], zre[1][4 * qd + 1], zre[1][4 * qd + 2], zre[1][4 * qd + 3]};
        if (r) *(f32x4*)(zp + 32 * ZS + off) = f32x4{zim[1][4 * qd], zim[1][4 * qd + 1], zim[1][4 * qd + 2], zim[1][4 * qd + 3]};
      }
    }
    RAW_BARRIER();
    STAMP(2, 6);
    {
      const int o = tid & 7, row = tid >> 3;
      float* zp = zs + row * ZS + 8 * o;
      f32x4 zl[7], zh[7];
#pragma unroll
      for (int c = 0; c < 7; ++c) { zl[c] = *(const f32x4*)(zp + c * 32 * ZS); zh[c] = *(const f32x4*)(zp + c * 32 * ZS + 4); }
      float y[8][7];
#pragma unroll
      for (int jp = 0; jp < 4; ++jp) {
        f32x2 z[7];
#pragma unroll
        for (int c = 0; c < 7; ++c) z[c] = jp < 2 ? f32x2{zl[c][2 * jp], zl[c][2 * jp + 1]} : f32x2{zh[c][2 * jp - 4], zh[c][2 * jp - 3]};
        f32x2 y0 = z[0] + z[1] + z[3] + z[5];
        f32x2 e1 = z[0] + z[1] * C71 + z[3] * C72 + z[5] * C73, o1 = z[2] * S71 + z[4] * S72 + z[6] * S73;
        f32x2 e2 = z[0] + z[1] * C72 + z[3] * C73 + z[5] * C71, o2 = z[2] * S72 - z[4] * S73 - z[6] * S71;
        f32x2 e3 = z[0] + z[1] * C73 + z[3] * C71 + z[5] * C72, o3 = z[2] * S73 - z[4] * S71 + z[6] * S72;
        f32x2 yy[7] = {y0, e1 - o1, e2 - o2, e3 - o3, e3 + o3, e2 + o2, e1 + o1};
#pragma unroll
        for (int b = 0; b < 7; ++b) { y[2 * jp][b] = yy[b][0]; y[2 * jp + 1][b] = yy[b][1]; }
      }
#pragma unroll
      for (int b = 0; b < 7; ++b) {
        u32x4 v;
#pragma unroll
        for (int jp = 0; jp < 4; ++jp) v[jp] = pk2(y[2 * jp][(b + 2 * jp) % 7], y[2 * jp + 1][(b + 2 * jp + 1) % 7]);
        int m = b - o; m += m < 0 ? 7 : 0; m += m < 0 ? 7 : 0;
        *(u32x4*)(zp + m * 32 * ZS) = v;
      }
      u16* dst = out + (s2 + 32 * (32 * sh + row)) * 896 + 448 + 8 * o;
#pragma unroll
      for (int m = 0; m < 7; ++m) *(u32x4*)(dst + 64 * m) = *(const u32x4*)(zp + m * 32 * ZS);
    }
  }
  STAMP(2, 12); STAMP(2, 15);
}

extern "C" void kernel_launch(void* const* d_in, const int* in_sizes, int n_in, void* d_out, int out_size, void* d_ws, size_t ws_size,
                              hipStream_t stream) {
  const u16* ax = (const u16*)d_in[0];
  const u16* wqkv = (const u16*)d_in[1];
  const u16* wo = (const u16*)d_in[2];
  const u16* bx = (const u16*)d_in[3];
  const u16* wr = (const u16*)d_in[4];
  const u16* wi = (const u16*)d_in[5];
  char* ws = (char*)d_ws;
  u32x4* Qf = (u32x4*)(ws);
  u32x4* KVf = (u32x4*)(ws + (2u << 20));
  u32x4* Of = (u32x4*)(ws + (6u << 20));
  unsigned* T = (unsigned*)(ws + (8u << 20));
  unsigned* U = (unsigned*)(ws + (10u << 20));
  u32x4* Wf = (u32x4*)(ws + (12u << 20));
  (void)hipFuncSetAttribute((const void*)k1_kernel, hipFuncAttributeMaxDynamicSharedMemorySize, K1_LDS);
  (void)hipFuncSetAttribute((const void*)k2_kernel, hipFuncAttributeMaxDynamicSharedMemorySize, K2_LDS);
  (void)hipFuncSetAttribute((const void*)k3_kernel, hipFuncAttributeMaxDynamicSharedMemorySize, K3_LDS);
  k1_kernel<<<256, 512, K1_LDS, stream>>>(ax, wqkv, bx, wr, wi, Qf, KVf, (u32x4*)T, Wf);
  k2_kernel<<<256, 512, K2_LDS, stream>>>(Qf, KVf, Of, T, U, Wf);
  k3_kernel<<<288, 256, K3_LDS, stream>>>(Of, wo, (const u32x4*)U, (u16*)d_out);
}
```

```cpp
#include <hip/hip_runtime.h>
#include <hip/hip_bf16.h>

typedef __attribute__((ext_vector_type(8))) __bf16 bf16x8;
typedef __attribute__((ext_vector_type(4))) __bf16 bf16x4;
typedef __attribute__((ext_vector_type(2))) __bf16 bf16x2;
typedef __attribute__((ext_vector_type(4))) short s16x4;
typedef __attribute__((ext_vector_type(16))) float f32x16;
typedef __attribute__((ext_vector_type(4))) float f32x4;
typedef __attribute__((ext_vector_type(2))) float f32x2;
typedef __attribute__((ext_vector_type(4))) unsigned u32x4;
typedef __attribute__((ext_vector_type(2))) unsigned u32x2;
typedef unsigned short u16;
#define LDSP __attribute__((address_space(3)))
#define DEV __device__ __forceinline__

DEV unsigned pk2(float lo, float hi) {
  f32x2 v = {lo, hi};
  return __builtin_bit_cast(unsigned, __builtin_convertvector(v, bf16x2));
}
DEV f32x16 mfma(u32x4 a, u32x4 b, f32x16 c) {
  return __builtin_amdgcn_mfma_f32_32x32x16_bf16(__builtin_bit_cast(bf16x8, a), __builtin_bit_cast(bf16x8, b), c, 0, 0, 0);
}
template <int S> DEV u32x4 packs(const f32x16& v) {
  u32x4 r = {pk2(v[8 * S + 0], v[8 * S + 1]), pk2(v[8 * S + 2], v[8 * S + 3]), pk2(v[8 * S + 4], v[8 * S + 5]), pk2(v[8 * S + 6], v[8 * S + 7])};
  return r;
}
template <int S> DEV u32x4 packsn(const f32x16& v) {
  u32x4 r = {pk2(-v[8 * S + 0], -v[8 * S + 1]), pk2(-v[8 * S + 2], -v[8 * S + 3]), pk2(-v[8 * S + 4], -v[8 * S + 5]), pk2(-v[8 * S + 6], -v[8 * S + 7])};
  return r;
}
DEV int sw23(int r) { return (r & ~12) | ((r & 4) << 1) | ((r & 8) >> 1); }
DEV float cosr(int n, float inv) { return __builtin_amdgcn_cosf((float)n * inv); }
DEV float sinr(int n, float inv) { return __builtin_amdgcn_sinf((float)n * inv); }
DEV void glds16(const void* g, char* l) {
  __builtin_amdgcn_global_load_lds((const unsigned*)g, (LDSP unsigned*)l, 16, 0, 0);
}
DEV void dma16(const void* base, unsigned voff, unsigned ldsaddr) {
  asm volatile("s_mov_b32 m0, %2\n\ts_nop 0\n\tglobal_load_lds_dwordx4 %0, %1" ::"v"(voff), "s"(base), "s"(ldsaddr) : "memory");
}
DEV unsigned ldsaddr_of(const char* p) { return (unsigned)(unsigned long long)(const LDSP char*)p; }
DEV u32x4 ald128(const void* base, unsigned off) {
  u32x4 r; asm volatile("global_load_dwordx4 %0, %1, %2" : "=v"(r) : "v"(off), "s"(base) : "memory"); return r;
}
DEV void st128(void* p, u32x4 v) { asm volatile("global_store_dwordx4 %0, %1, off sc1\n\ts_nop 1" ::"v"(p), "v"(v) : "memory"); }
#define SB() __builtin_amdgcn_sched_barrier(0)
DEV u32x2 ldtr(const char* l) {
  return __builtin_bit_cast(u32x2, __builtin_amdgcn_ds_read_tr16_b64_v4i16((LDSP s16x4*)l));
}
DEV float bflo(unsigned u) { return __builtin_bit_cast(float, u << 16); }
DEV float bfhi(unsigned u) { return __builtin_bit_cast(float, u & 0xffff0000u); }
#define RAW_BARRIER() do { asm volatile("s_waitcnt lgkmcnt(0)" ::: "memory"); __builtin_amdgcn_s_barrier(); asm volatile("" ::: "memory"); } while (0)
#define VMCNT(n) asm volatile("s_waitcnt vmcnt(" #n ")" ::: "memory")

#define STAMP_INIT
#define STAMP(k, i)
constexpr float C71 = 0.62348980185873353f, C72 = -0.22252093395631440f, C73 = -0.90096886790241913f;
constexpr float S71 = 0.78183148246802981f, S72 = 0.97492791218182361f, S73 = 0.43388373911755812f;

DEV void gen_cs(int i, int k0, int mul, int add, int mask, float inv, u32x4& C, u32x4& S) {
  float c[8], s[8];
#pragma unroll
  for (int j = 0; j < 8; ++j) {
    int n = (__mul24(i, mul * k0 + add) + __mul24(i, mul) * j) & mask;
    c[j] = cosr(n, inv); s[j] = sinr(n, inv);
  }
#pragma unroll
  for (int j = 0; j < 4; ++j) { C[j] = pk2(c[2 * j], c[2 * j + 1]); S[j] = pk2(s[2 * j], s[2 * j + 1]); }
}

constexpr double ccos64(int n) {
  n &= 63; if (n > 32) n = 64 - n;
  double sg = 1.0; if (n > 16) { n = 32 - n; sg = -1.0; }
  if (n == 16) return 0.0;
  double x = 6.283185307179586476925 * n / 64.0, x2 = x * x, term = 1.0, sum = 1.0;
  for (int i = 1; i < 14; ++i) { term *= -x2 / ((2 * i - 1) * (2 * i)); sum += term; }
  return sg * sum;
}
constexpr unsigned cbf(double v) { unsigned u = __builtin_bit_cast(unsigned, (float)v); return (u + 0x7fffu + ((u >> 16) & 1u)) >> 16; }
constexpr int csw(int r) { return (r & ~12) | ((r & 4) << 1) | ((r & 8) >> 1); }
struct alignas(16) Tabs { unsigned f64[16 * 256]; unsigned b2[14 * 256]; };
constexpr Tabs make_tabs() {
  Tabs t{};
  unsigned c[64] = {}, sn[64] = {};
  for (int n = 0; n < 64; ++n) { c[n] = cbf(ccos64(n)); sn[n] = cbf(ccos64(n - 16 + 64)); }
  for (int f = 0; f < 8; ++f)
    for (int l = 0; l < 64; ++l)
      for (int d = 0; d < 4; ++d) {
        int i = 32 * (f >> 2) + (l & 31), k = 16 * (f & 3) + 8 * (l >> 5) + 2 * d;
        t.f64[((f * 2) * 64 + l) * 4 + d] = c[(i * k) & 63] | (c[(i * (k + 1)) & 63] << 16);
        t.f64[((f * 2 + 1) * 64 + l) * 4 + d] = sn[(i * k) & 63] | (sn[(i * (k + 1)) & 63] << 16);
      }
  for (int l = 0; l < 64; ++l)
    for (int d = 0; d < 4; ++d) {
      for (int f = 0; f < 4; ++f) {
        int n = (2 * csw(l & 31) * (8 * f + 4 * (l >> 5) + d)) & 63;
        t.b2[(f * 64 + l) * 4 + d] = c[n] | (sn[n] << 16);
        t.b2[((4 + f) * 64 + l) * 4 + d] = (sn[n] ^ 0x8000u) | (c[n] << 16);
      }
      for (int f = 0; f < 2; ++f) {
        int k = 16 * f + 8 * (l >> 5) + 2 * d, n0 = (2 * (l & 31) * k) & 63, n1 = (2 * (l & 31) * (k + 1)) & 63;
        t.b2[((8 + f) * 64 + l) * 4 + d] = c[n0] | (c[n1] << 16);
        t.b2[((10 + f) * 64 + l) * 4 + d] = sn[n0] | (sn[n1] << 16);
        t.b2[((12 + f) * 64 + l) * 4 + d] = (sn[n0] | (sn[n1] << 16)) ^ 0x80008000u;
      }
    }
  return t;
}
__device__ const Tabs g_tabs = make_tabs();
DEV unsigned ald32(const void* base, unsigned off) {
  unsigned r; asm volatile("global_load_dword %0, %1, %2" : "=v"(r) : "v"(off), "s"(base) : "memory"); return r;
}
DEV unsigned ald16(const void* base, unsigned off) {
  unsigned r; asm volatile("global_load_ushort %0, %1, %2" : "=v"(r) : "v"(off), "s"(base) : "memory"); return r;
}
DEV void ast32(void* base, unsigned off, unsigned v) { asm volatile("global_store_dword %0, %1, %2 sc1" ::"v"(off), "v"(v), "s"(base) : "memory"); }
DEV int col_bin(int c) { int ct = c >> 5, k = ct == 7 ? 7 * (c - 192) : 7 * ((ct ? ((ct + 1) & 1) * 32 : 0) + (c & 31)) + 64 * ((ct + 1) >> 1); return k >= 448 ? k - 448 : k; }

constexpr int K1_SLOT = 32 * 1024;
constexpr int K1_LDS = 128 * 1024;
struct QF { u32x4 x0[4], x1[4], w[4]; };

template <bool ISV>
DEV void qkv_loop(const char* lds, unsigned ld, const u16* xb, const u16* wb, int kn, unsigned xo, unsigned wo0, unsigned wo1,
                  int xoff, int woff, f32x16& a0, f32x16& a1) {
  auto piece = [&](int slot, int i) {
    const unsigned d = ld + slot * K1_SLOT;
    if (i == 0) dma16(xb + kn * 64, xo, d);
    if (i == 1) dma16(wb + kn * (64 * 1344), wo0, d + 16384);
    if (i == 2) dma16(xb + kn * 64 + 64 * 448, xo, d + 8192);
    if (i == 3) { dma16(wb + kn * (64 * 1344), wo1, d + 24576); kn = kn == 6 ? 0 : kn + 1; }
  };
  auto rd = [&](QF& f, int slot, int ks) {
    const char* sl = lds + slot * K1_SLOT;
    f.x0[ks] = *(const u32x4*)(sl + (xoff ^ (ks * 32)));
    f.x1[ks] = *(const u32x4*)(sl + (xoff ^ (ks * 32)) + 4096);
    u32x2 wa = ldtr(sl + woff + ks * 16 * 224), wc = ldtr(sl + woff + ks * 16 * 224 + 224);
    f.w[ks] = u32x4{wa[0], wa[1], wc[0], wc[1]};
  };
#pragma unroll
  for (int i = 0; i < 8; ++i) piece(i >> 2, i & 3);
  QF f[2];
#pragma unroll
  for (int kc = 0; kc < 8; ++kc) {
    if (kc < 7) { if (kc < 6) VMCNT(4); else VMCNT(0); __builtin_amdgcn_s_barrier(); asm volatile("" ::: "memory"); }
#pragma unroll
    for (int ks = 0; ks < 4; ++ks) {
      if (kc < 7) rd(f[kc & 1], kc & 3, ks);
      if (kc > 0) {
        QF& g = f[(kc - 1) & 1];
        if (ISV) { a0 = mfma(g.x0[ks], g.w[ks], a0); a1 = mfma(g.x1[ks], g.w[ks], a1); }
        else     { a0 = mfma(g.w[ks], g.x0[ks], a0); a1 = mfma(g.w[ks], g.x1[ks], a1); }
      }
      if (kc < 5) piece((kc + 2) & 3, ks);
      SB();
    }
  }
}

__global__ __launch_bounds__(512, 2) void k1_kernel(const u16* __restrict__ ax, const u16* __restrict__ wqkv, const u16* __restrict__ bx,
                                                    u32x4* __restrict__ Qf, u32x4* __restrict__ KVf, u32x4* __restrict__ T) {
  extern __shared__ __attribute__((aligned(16))) char lds[];
  const int tid = threadIdx.x, lane = tid & 63, w = __builtin_amdgcn_readfirstlane(tid >> 6);
  const int xcd = blockIdx.x & 7, idx = blockIdx.x >> 3;
  const int l31 = lane & 31, h = lane >> 5;
  const unsigned lb = ldsaddr_of(lds);
  STAMP_INIT; STAMP(0, 0); STAMP(0, 1);
  if (idx >= 8) {
    const int u = idx - 8, which = u >> 3, tm = (xcd & 1) * 8 + (u & 7), g = xcd >> 1;
    const int row = 8 * w + (lane >> 3);
    const unsigned xo = (row * 448 + ((lane & 7) ^ ((row >> 1) & 7)) * 8) * 2;
    unsigned wo2[2];
#pragma unroll
    for (int i = 0; i < 2; ++i) {
      int q = 64 * (w + 8 * i) + lane; if (q > 895) q = 895;
      int rho = q / 14, cc = q - rho * 14;
      wo2[i] = (((rho & ~7) + ((rho & 7) >> 1) + 4 * (rho & 1)) * 1344 + cc * 8) * 2;
    }
    const int hh = w & 3, th = w >> 2, r0 = th * 64 + l31;
    const int xoff = r0 * 128 + ((h ^ ((r0 >> 1) & 7)) * 16);
    const int woff = 16384 + (8 * h + 2 * ((lane >> 2) & 3)) * 224 + (hh * 28 + ((lane >> 4) & 1) * 16 + 4 * (lane & 3)) * 2;
    const int head = g * 4 + hh, tile = tm * 4 + th * 2, kn = ((u & 7) + 2 * which) % 7;
    f32x16 a0 = {}, a1 = {};
    u32x4 o0, o1, o2, o3;
    if (which == 2) {
      qkv_loop<true>(lds, lb + w * 1024, ax + tm * 128 * 448, wqkv + 896 + g * 112, kn, xo, wo2[0], wo2[1], xoff, woff, a0, a1);
      o0 = packs<0>(a0); o1 = packs<1>(a0); o2 = packs<0>(a1); o3 = packs<1>(a1);
      if (l31 >= 28) { unsigned f = (l31 == 28) ? 0x3F803F80u : 0u; o0 = u32x4{f, f, f, f}; o1 = o0; o2 = o0; o3 = o0; }
    } else {
      qkv_loop<false>(lds, lb + w * 1024, ax + tm * 128 * 448, wqkv + which * 448 + g * 112, kn, xo, wo2[0], wo2[1], xoff, woff, a0, a1);
      const float sc = which ? 1.0f : 0.18898223650461362f * 1.4426950408889634f;
      a0 *= sc; a1 *= sc;
      o0 = packs<0>(a0); o1 = packs<1>(a0); o2 = packs<0>(a1); o3 = packs<1>(a1);
      if (h) { o1[2] = which ? 0x3F80u : 0u; o1[3] = 0; o3[2] = o1[2]; o3[3] = 0; }
    }
    STAMP(0, 7);
    u32x4* o = (which == 0 ? Qf + ((head * 64 + tile) * 2) * 64 : KVf + ((head * 64 + tile) * 4 + (which & 2)) * 64) + lane;
    const int st = which == 0 ? 128 : 256;
    st128(o, o0); st128(o + 64, o1); st128(o + st, o2); st128(o + st + 64, o3);
  } else {
    const int b1 = xcd * 8 + idx, s2 = b1 >> 1, hb = b1 & 1;
    u32x4* F64 = (u32x4*)(lds + 56 * 1024);
    u32x4* Ap = (u32x4*)(lds + 72 * 1024);
    u32x4* Xf = (u32x4*)(lds + 88 * 1024);
    {
      const int row = 8 * w + (lane >> 3);
      const unsigned xo = ((s2 + 32 * ((row & 32) + sw23(row & 31))) * 448 + ((lane & 7) ^ ((row >> 1) & 7)) * 8) * 2;
#pragma unroll
      for (int m = 0; m < 7; ++m) dma16(bx + 64 * m, xo, lb + m * 8192 + w * 1024);
      dma16(g_tabs.f64 + w * 512, lane * 16, lb + 56 * 1024 + w * 2048); dma16(g_tabs.f64 + w * 512 + 256, lane * 16, lb + 57 * 1024 + w * 2048);
    }
    SB();
    const int mt = w >> 2, o = 2 * (w & 3) + h;
    {
      u32x4 C, S;
      gen_cs(32 * mt + l31, 8 * o, 32, s2, 2047, 1.0f / 2048, C, S);
      Ap[(w * 2 + 0) * 64 + lane] = C; Ap[(w * 2 + 1) * 64 + lane] = S;
    }
    char* xp = lds + (32 * mt + l31) * 128 + ((o ^ ((l31 >> 1) & 7)) * 16);
    int xm[7];
#pragma unroll
    for (int b = 0; b < 7; ++b) { int m = b - o; m += m < 0 ? 7 : 0; m += m < 0 ? 7 : 0; xm[b] = m * 8192; asm volatile("" : "+v"(xm[b])); }
    STAMP(0, 2);
    VMCNT(2);
    RAW_BARRIER();
    STAMP(0, 3);
    {
      u32x4 xr[7], z[4];
#pragma unroll
      for (int b = 0; b < 7; ++b) xr[b] = *(const u32x4*)(xp + xm[b]);
      const float ca = hb ? C72 : C71, cb = hb ? C73 : C72, cc = hb ? C71 : C73, sa = hb ? S72 : S71, sb = hb ? -S73 : S72, sc = hb ? -S71 : S73;
      const float da = hb ? C73 : 1.0f, db = hb ? C71 : 1.0f, dc = hb ? C72 : 1.0f, ta = hb ? S73 : 0.0f, tb = hb ? -S71 : 0.0f, tc = hb ? S72 : 0.0f;
#pragma unroll
      for (int jp = 0; jp < 4; ++jp) {
        f32x2 v[7];
#pragma unroll
        for (int b = 0; b < 7; ++b) {
          v[b][0] = bflo(xr[(b - 2 * jp + 14) % 7][jp]);
          v[b][1] = bfhi(xr[(b - 2 * jp - 1 + 14) % 7][jp]);
        }
        f32x2 p1 = v[1] + v[6], p2 = v[2] + v[5], p3 = v[3] + v[4];
        f32x2 m1 = v[6] - v[1], m2 = v[5] - v[2], m3 = v[4] - v[3];
        f32x2 ra = v[0] + p1 * ca + p2 * cb + p3 * cc, ia = m1 * sa + m2 * sb + m3 * sc;
        f32x2 rb = v[0] + p1 * da + p2 * db + p3 * dc, ib = m1 * ta + m2 * tb + m3 * tc;
        z[0][jp] = pk2(ra[0], ra[1]); z[1][jp] = pk2(ia[0], ia[1]); z[2][jp] = pk2(rb[0], rb[1]); z[3][jp] = pk2(ib[0], ib[1]);
      }
#pragma unroll
      for (int c = 0; c < 4; ++c) *(u32x4*)(xp + c * 8192) = z[c];
    }
    VMCNT(0);
    RAW_BARRIER();
    STAMP(0, 4);
    const int wc = w & 3, ct = hb ? 3 + wc : (wc == 2 ? 0 : (wc == 3 ? 7 : wc + 1)), kt = w & 1;
    {
      f32x16 xre = {}, xim = {};
      const char* zp = lds + (w & 2) * 8192 + mt * 4096 + l31 * 128;
      const int zsw = (h ^ ((l31 >> 1) & 7)) * 16;
#pragma unroll
      for (int tt = 0; tt < 4; ++tt) {
        u32x4 C = F64[((kt * 4 + tt) * 2) * 64 + lane], S = F64[((kt * 4 + tt) * 2 + 1) * 64 + lane];
        u32x4 nS = S ^ 0x80008000u;
        u32x4 zr = *(const u32x4*)(zp + (zsw ^ (tt * 32))), zi = *(const u32x4*)(zp + 8192 + (zsw ^ (tt * 32)));
        xre = mfma(zr, C, xre); xim = mfma(zi, C, xim);
        xre = mfma(zi, S, xre); xim = mfma(zr, nS, xim);
      }
      u32x4* xq = Xf + (wc * 4 + mt * 2) * 128 + lane;
      xq[0] = packs<0>(xre); xq[64] = packs<0>(xim); xq[128] = packs<1>(xre); xq[192] = packs<1>(xim);
    }
    RAW_BARRIER();
    STAMP(0, 5);
    {
      f32x16 tre = {}, tim = {};
#pragma unroll
      for (int tt = 0; tt < 4; ++tt) {
        u32x4 C = Ap[((mt * 4 + tt) * 2) * 64 + lane], S = Ap[((mt * 4 + tt) * 2 + 1) * 64 + lane];
        u32x4 bre = Xf[(wc * 4 + tt) * 128 + lane], bim = Xf[(wc * 4 + tt) * 128 + 64 + lane];
        tre = mfma(C, bre, tre); tim = mfma(C, bim, tim);
        tre = mfma(S, bim, tre); tim = mfma(S ^ 0x80008000u, bre, tim);
      }
      u32x4* tp = T + (s2 * 16 + mt * 8 + h) * 232 + ct * 32 + l31;
      if (ct < 7 || l31 < 8)
#pragma unroll
      for (int q = 0; q < 4; ++q)
        st128(tp + 2 * q * 232, u32x4{pk2(tre[4 * q], tim[4 * q]), pk2(tre[4 * q + 1], tim[4 * q + 1]), pk2(tre[4 * q + 2], tim[4 * q + 2]), pk2(tre[4 * q + 3], tim[4 * q + 3])});
    }
  }
  STAMP(0, 12); STAMP(0, 15);
}

constexpr int K2_LDS = 84 * 1024;
DEV float xhalf_max(float v) {
  float a = v, b = v;
  asm volatile("s_nop 1\n\tv_permlane32_swap_b32 %0, %1\n\ts_nop 1" : "+v"(a), "+v"(b));
  return fmaxf(a, b);
}
DEV float max16(const f32x16& s) {
  float m = fmaxf(fmaxf(s[0], s[1]), s[2]);
#pragma unroll
  for (int i = 3; i < 15; i += 2) m = fmaxf(fmaxf(m, s[i]), s[i + 1]);
  return fmaxf(m, s[15]);
}
#define PIN16(a) asm volatile("" : "+v"(a[0]), "+v"(a[1]), "+v"(a[2]), "+v"(a[3]), "+v"(a[4]), "+v"(a[5]), "+v"(a[6]), "+v"(a[7]), \
                                   "+v"(a[8]), "+v"(a[9]), "+v"(a[10]), "+v"(a[11]), "+v"(a[12]), "+v"(a[13]), "+v"(a[14]), "+v"(a[15]))
#define EX(s, i) s[i] = __builtin_amdgcn_exp2f(s[i])
#define EX4(s, i, P, j)                                                                                          \
  { const float t0_ = __builtin_amdgcn_exp2f(s[i]); SB(); P[j] = pk2(e0, e1); SB();                               \
    const float t1_ = __builtin_amdgcn_exp2f(s[i + 1]); SB(); P[j + 1] = pk2(e2, e3); SB();                       \
    e0 = t0_; e1 = t1_; e2 = __builtin_amdgcn_exp2f(s[i + 2]); SB(); e3 = __builtin_amdgcn_exp2f(s[i + 3]); SB(); }
#define TILE(sc, sn, K0, K1, Q0, Q1, oc, V0, op, V1, L0, L1, L2, L3)                 \
  sn = mfma(K0, Q0, f32x16{}); SB(); L0; EX4(sc, 0, p1, 2)                           \
  op = mfma(V1, p1, op); SB(); L1; EX4(sc, 4, p0, 0)                                 \
  sn = mfma(K1, Q1, sn); SB(); L2; EX4(sc, 8, p0, 2)                                 \
  oc = mfma(V0, p0, oc); SB(); L3; EX4(sc, 12, p1, 0)
#define KTILE(X, Y, P1, P2)                                                                                      \
  TILE(sa, sb, X##k0, X##k1, q0[1], q1[1], o[0], X##v0, o[3], Y##v1, , Y##v1 = (P1)[192], , )                     \
  TILE(sb, sa, X##k0, X##k1, q0[2], q1[2], o[1], X##v0, o[0], X##v1, , , , )                                      \
  TILE(sa, sb, X##k0, X##k1, q0[3], q1[3], o[2], X##v0, o[1], X##v1, X##k0 = (P2)[0], , X##k1 = (P2)[64], )       \
  TILE(sb, sa, Y##k0, Y##k1, q0[0], q1[0], o[3], X##v0, o[2], X##v1, , , , X##v0 = (P2)[128])

__global__ __launch_bounds__(384, 2) void k2_kernel(const u32x4* __restrict__ Qf, const u32x4* __restrict__ KVf, u32x4* __restrict__ Of,
                                                    const unsigned* __restrict__ T, unsigned* __restrict__ U,
                                                    const u16* __restrict__ Wr, const u16* __restrict__ Wi, const u16* __restrict__ wo) {
  extern __shared__ __attribute__((aligned(16))) char lds[];
  const int tid = threadIdx.x, lane = tid & 63, w = __builtin_amdgcn_readfirstlane(tid >> 6);
  const int xcd = blockIdx.x & 7, idx = blockIdx.x >> 3;
  const int l31 = lane & 31, h = lane >> 5;
  STAMP_INIT; STAMP(1, 0); STAMP(1, 1);
  if (w < 4) {
    __builtin_amdgcn_s_setprio(2);
    const int head = xcd * 2 + (idx >> 4), qt = (idx & 15) * 4;
    const u32x4* kp = KVf + (head * 64 + w * 16) * 256 + lane;
    u32x4 q0[4], q1[4];
    u32x4 ek0 = kp[0], ek1 = kp[64];
#pragma unroll
    for (int i = 0; i < 4; ++i) { const u32x4* qp = Qf + (head * 64 + qt + ((w + i) & 3)) * 128 + lane; q0[i] = qp[0]; q1[i] = qp[64]; }
    u32x4 ev0 = kp[128], ev1 = kp[192], dk0 = kp[256], dk1 = kp[320], dv0 = kp[384], dv1 = {}, p0, p1 = {};
    f32x16 o[4] = {}, sa, sb;
    float m[4], e0 = 0.f, e1 = 0.f, e2 = 0.f, e3 = 0.f;
    {
      f32x16 s4[4];
#pragma unroll
      for (int i = 0; i < 4; ++i) { s4[i] = mfma(ek0, q0[i], f32x16{}); s4[i] = mfma(ek1, q1[i], s4[i]); }
#pragma unroll
      for (int i = 0; i < 4; ++i) {
        const unsigned nm = pk2(-xhalf_max(max16(s4[i])), 0.0f);
        m[i] = -bflo(nm);
        if (h) q1[i][2] = nm;
      }
      sa = s4[0] - m[0];
    }
    STAMP(1, 3);
#pragma unroll 1
    for (int it = 0; it < 8; ++it) {
      const u32x4* kn = kp + ((2 * it + 2) & 15) * 256;
      KTILE(e, d, kp + (2 * it + 1) * 256, kn)
      KTILE(d, e, kn, kn + 256)
    }
    p1[2] = pk2(e0, e1); p1[3] = pk2(e2, e3);
    o[3] = mfma(dv1, p1, o[3]);
    STAMP(1, 11);
    if (__builtin_expect(__ballot(h && !(o[0][12] + o[1][12] + o[2][12] + o[3][12] < 1.0e12f)) != 0ull, 0)) {
#pragma unroll
      for (int u = 0; u < 4; ++u) {
        f32x16 oo = {}; float mm = -3.0e38f;
        if (h) q1[u][2] = 0;
#pragma unroll 1
        for (int t = 0; t < 16; ++t) {
          const u32x4* p = kp + t * 256;
          f32x16 s = mfma(p[0], q0[u], f32x16{}); s = mfma(p[64], q1[u], s);
          float mn = fmaxf(mm, xhalf_max(max16(s)));
          float al = __builtin_amdgcn_exp2f(mm - mn);
          mm = mn;
#pragma unroll
          for (int i = 0; i < 16; ++i) { oo[i] *= al; s[i] = __builtin_amdgcn_exp2f(s[i] - mn); }
          oo = mfma(p[128], packs<0>(s), oo); oo = mfma(p[192], packs<1>(s), oo);
        }
        o[u] = oo; m[u] = mm;
      }
    }
#pragma unroll
    for (int u = 1; u < 4; ++u) {
      if (h) o[u][13] = m[u];
      f32x4* ex = (f32x4*)(lds + (((w + u) & 3) * 3 + u - 1) * 4096) + lane;
#pragma unroll
      for (int i = 0; i < 4; ++i) ex[i * 64] = f32x4{o[u][4 * i], o[u][4 * i + 1], o[u][4 * i + 2], o[u][4 * i + 3]};
    }
    const float l0 = xhalf_max(h ? o[0][12] : -1.0f);
    RAW_BARRIER();
    STAMP(1, 12);
    const char* ey = lds + w * 12288;
    f32x2 st[3];
#pragma unroll
    for (int u = 0; u < 3; ++u) st[u] = *(const f32x2*)(ey + u * 4096 + 3584 + l31 * 16);
    f32x4 y[12];
#pragma unroll
    for (int i = 0; i < 12; ++i) y[i] = *(const f32x4*)(ey + i * 1024 + lane * 16);
    SB();
    const float mm = fmaxf(fmaxf(st[0][1], st[1][1]), fmaxf(st[2][1], m[0]));
    float f0 = __builtin_amdgcn_exp2f(m[0] - mm), f[3];
#pragma unroll
    for (int u = 0; u < 3; ++u) f[u] = __builtin_amdgcn_exp2f(st[u][1] - mm);
    const float inv = __builtin_amdgcn_rcpf(l0 * f0 + st[0][0] * f[0] + st[1][0] * f[1] + st[2][0] * f[2]);
    f0 *= inv;
    f32x16 r = o[0] * f0;
    asm volatile("" : "+v"(r) : "v"(f0));
#pragma unroll
    for (int u = 0; u < 3; ++u) {
      const float fu = f[u] * inv;
#pragma unroll
      for (int i = 0; i < 4; ++i) {
        const f32x4 v = y[u * 4 + i];
        r[4 * i] += v[0] * fu; r[4 * i + 1] += v[1] * fu; r[4 * i + 2] += v[2] * fu; r[4 * i + 3] += v[3] * fu;
      }
    }
    u32x4 r1 = packs<1>(r);
    if (h) { r1[2] = 0; r1[3] = 0; }
    u32x4* op = Of + (((qt + w) * 16 + head) * 2) * 64 + lane;
    st128(op, packs<0>(r)); st128(op + 64, r1);
  } else if (idx < 29) {
    const int jq = xcd * 2 + w - 4, col = idx * 8 + (l31 >> 2), kb = col_bin(col), jr = 4 * jq + (lane & 3) + 512 * h;
    const unsigned toff = (4 * h * 16 + jq) * 928 + idx * 32 + l31;
    const bool cj = kb > 224;
    const int of = jr * 450 + 2 * kb, om = (2048 - jr) * 450 + 2 * (448 - kb), om0 = jr ? om : om - 921600;
    const float sr = col > 224 ? 0.0f : ((col == 0 || col == 224) ? 0.5f : 1.0f), si = cj ? -sr : sr;
    u32x4 mt[14];
    unsigned ta[16], ur[16], ui[16]; float wr[16], wi[16];
#pragma unroll
    for (int i = 0; i < 16; ++i) ta[i] = ald32(T + (8 * (i >> 2) + (i & 3)) * 14848, toff * 4);
#pragma unroll
    for (int g = 0; g < 16; ++g) {
      const unsigned o_ = g ? (cj ? om : of) + ((g & 7) + 16 * (g >> 3)) * (cj ? -28800 : 28800) : (cj ? om0 : of);
      ur[g] = ald16(Wr, o_); ui[g] = ald16(Wi, o_);
    }
    const unsigned* tabp = g_tabs.b2;
    asm volatile("" : "+s"(tabp));
#pragma unroll
    for (int t = 0; t < 14; ++t) mt[t] = ald128(tabp + t * 256, lane * 16);
    asm volatile("s_waitcnt vmcnt(0)" : "+v"(mt[0]), "+v"(mt[1]), "+v"(mt[2]), "+v"(mt[3]), "+v"(mt[4]), "+v"(mt[5]), "+v"(mt[6]), "+v"(mt[7]), "+v"(mt[8]),
                 "+v"(mt[9]), "+v"(mt[10]), "+v"(mt[11]), "+v"(mt[12]), "+v"(mt[13]) :: "memory");
    PIN16(ta); PIN16(ur); PIN16(ui);
#pragma unroll
    for (int g = 0; g < 16; ++g) { wr[g] = bflo(ur[g]); wi[g] = bflo(ui[g]); }
    SB();
    f32x16 xr = {}, xi = {};
#pragma unroll
    for (int t = 0; t < 4; ++t) {
      u32x4 b = {ta[4 * t], ta[4 * t + 1], ta[4 * t + 2], ta[4 * t + 3]};
      xr = mfma(mt[t], b, xr); xi = mfma(mt[4 + t], b, xi);
    }
    f32x16 yr, yi;
#pragma unroll
    for (int g = 0; g < 16; g += 2) {
      f32x2 c = {wr[g], wr[g + 1]}, d = {wi[g], wi[g + 1]};
      asm volatile("" : "+v"(c), "+v"(d));
      c *= sr; d *= si;
      const f32x2 a = {xr[g], xr[g + 1]}, b = {xi[g], xi[g + 1]}, r = a * c - b * d, i = a * d + b * c;
      yr[g] = r[0]; yr[g + 1] = r[1]; yi[g] = i[0]; yi[g + 1] = i[1];
    }
    u32x4 r0 = packs<0>(yr), r1 = packs<1>(yr), i0 = packs<0>(yi), i1 = packs<1>(yi);
    f32x16 ure = {}, uim = {};
    ure = mfma(mt[8], r0, ure); uim = mfma(mt[8], i0, uim);
    ure = mfma(mt[12], i0, ure); uim = mfma(mt[10], r0, uim);
    ure = mfma(mt[9], r1, ure); uim = mfma(mt[9], i1, uim);
    ure = mfma(mt[13], i1, ure); uim = mfma(mt[11], r1, uim);
#pragma unroll
    for (int g = 0; g < 16; ++g) ast32(U + ((g & 3) + 8 * (g >> 2)) * 14848, toff * 4, pk2(ure[g], uim[g]));
    {
      const unsigned off = ((idx * 2 + w - 4) * 64 + lane) * 128;
      if (off < 401408u) { unsigned pf = *(const unsigned*)((const char*)wo + off); asm volatile("" ::"v"(pf)); }
    }
  }
  STAMP(1, 14); STAMP(1, 15);
}

constexpr int ZS = 68;
constexpr int K3_LDS = 77 * 1024;

__global__ __launch_bounds__(512, 4) void k3_kernel(const u32x4* __restrict__ Of, const u16* __restrict__ wo, const u32x4* __restrict__ U,
                                                    u16* __restrict__ out) {
  extern __shared__ __attribute__((aligned(16))) char lds[];
  const int tid = threadIdx.x, lane = tid & 63, w = __builtin_amdgcn_readfirstlane(tid >> 6);
  const int xcd = blockIdx.x & 7, idx = blockIdx.x >> 3;
  const int l31 = lane & 31, h = lane >> 5;
  STAMP_INIT; STAMP(2, 0); STAMP(2, 1);
  if (idx < 4 || idx >= 12) {
    if (w >= 4) return;
    const int u = idx < 4 ? idx : idx - 8, tq = xcd * 4 + u / 7, tn = u % 7;
    const int qi = w & 1, kh = w >> 1;
    u32x4 of[16];
    {
      const int r8 = lane >> 3;
      const unsigned so = (r8 * 448 + ((lane & 7) ^ ((r8 & 2) << 1)) * 8) * 2, l16 = lane * 16;
      const u16* src = wo + (kh * 224 + qi * 8) * 448 + tn * 64;
      const unsigned d = ldsaddr_of(lds) + (kh * 224 + qi * 8) * 128;
      const u32x4* op = Of + (((tq * 2 + qi) * 16 + kh * 8) * 2) * 64;
#pragma unroll
      for (int st = 0; st < 2; ++st) {
#pragma unroll
        for (int i = 0; i < 7; ++i) dma16(src + (st * 7 + i) * 16 * 448, so, d + (st * 7 + i) * 2048);
#pragma unroll
        for (int i = 0; i < 8; ++i) of[st * 8 + i] = ald128(op + (st * 8 + i) * 64, l16);
      }
    }
    const int q = (lane >> 2) & 3, p = lane & 3, pc = ((p & 1) << 1) | (p >> 1);
    const int colb = ((lane >> 4) & 1) * 32 + pc * 8;
    const int base = (kh * 224 + q) * 128 + (colb ^ (((q >> 1) ^ kh) << 6));
    f32x16 a0 = {}, a1 = {};
    STAMP(2, 2);
#pragma unroll
    for (int st = 0; st < 2; ++st) {
      if (st == 0) asm volatile("s_waitcnt vmcnt(15)" : "+v"(of[0]), "+v"(of[1]), "+v"(of[2]), "+v"(of[3]), "+v"(of[4]), "+v"(of[5]), "+v"(of[6]), "+v"(of[7]) :: "memory");
      else asm volatile("s_waitcnt vmcnt(0)" : "+v"(of[8]), "+v"(of[9]), "+v"(of[10]), "+v"(of[11]), "+v"(of[12]), "+v"(of[13]), "+v"(of[14]), "+v"(of[15]) :: "memory");
      RAW_BARRIER();
      if (st == 0) STAMP(2, 3); else STAMP(2, 4);
      u32x4 fx[3], fy[3];
#pragma unroll
      for (int n = 0; n < 10; ++n) {
        if (n < 8) {
          const int i = st * 8 + n, hd = i >> 1, s = i & 1;
          const char* pa = lds + (hd * 28 + 16 * s) * 128;
          const int ra = base + h * 512, rb = (s ? base : base + h * 512) + 1024;
          u32x2 x0 = ldtr(pa + ra), x1 = ldtr(pa + rb), y0 = ldtr(pa + (ra ^ 64)), y1 = ldtr(pa + (rb ^ 64));
          fx[n % 3] = u32x4{x0[0], x0[1], x1[0], x1[1]}; fy[n % 3] = u32x4{y0[0], y0[1], y1[0], y1[1]};
        }
        SB();
        if (n >= 2) { a0 = mfma(fx[(n - 2) % 3], of[st * 8 + n - 2], a0); a1 = mfma(fy[(n - 2) % 3], of[st * 8 + n - 2], a1); }
        SB();
      }
    }
    STAMP(2, 5);
    f32x4* ex = (f32x4*)(lds + 56 * 1024);
    f32x16 snd = a1, kp = a0;
#pragma unroll
    for (int i = 0; i < 4; ++i) ex[(w * 4 + i) * 64 + lane] = f32x4{snd[4 * i], snd[4 * i + 1], snd[4 * i + 2], snd[4 * i + 3]};
    RAW_BARRIER();
#pragma unroll
    for (int i = 0; i < 4; ++i) {
      f32x4 v = ex[((w ^ 2) * 4 + i) * 64 + lane];
      kp[4 * i] += v[0]; kp[4 * i + 1] += v[1]; kp[4 * i + 2] += v[2]; kp[4 * i + 3] += v[3];
    }
    u16* dst = out + ((tq * 2 + qi) * 32 + l31) * 896 + tn * 64 + kh * 32 + 8 * h;
    *(u32x4*)dst = packs<0>(kp); *(u32x4*)(dst + 16) = packs<1>(kp);
  } else {
    const int b3 = xcd * 8 + idx - 4, s2 = b3 >> 1, sh = b3 & 1, r = w;
    u32x4* Dm = (u32x4*)lds;
    u32x4* F64 = (u32x4*)(lds + 61 * 1024);
    float* zs = (float*)lds;
    u32x4 ua[8], ub[8];
    if (w < 4) {
      const u32x4* up = U + s2 * 16 * 232 + (r ? 2 * r - 1 : 0) * 32;
      const unsigned uo = (h * 232 + sw23(l31)) * 16, uo2 = r ? uo + 512 : (h * 232 + 224 + (sw23(l31) & 7)) * 16;
#pragma unroll
      for (int i = 0; i < 8; ++i) { ua[i] = ald128(up + i * 464, uo); ub[i] = ald128(up + i * 464, uo2); }
    } else {
      const unsigned pl = (h * 32 + sw23(l31)) * 16;
#pragma unroll
      for (int i = 0; i < 4; ++i) dma16(g_tabs.f64 + ((w & 3) * 4 + i) * 256, pl, ldsaddr_of(lds) + 61 * 1024 + ((w & 3) * 4 + i) * 1024);
    }
    SB();
    {
      const int s = s2 + 32 * (32 * sh + l31);
      const float NRM2 = 2.0f / (2048.0f * 448.0f);
      u32x4 dr, di;
#pragma unroll
      for (int i = 0; i < 4; ++i) {
        int n = __mul24(8 * w + 4 * h + i, s) & 2047;
        float c = cosr(n, 1.0f / 2048) * NRM2, sn = sinr(n, 1.0f / 2048) * NRM2;
        dr[i] = pk2(c, -sn); di[i] = pk2(sn, c);
      }
      Dm[(w * 2) * 64 + lane] = dr; Dm[(w * 2 + 1) * 64 + lane] = di;
    }
    STAMP(2, 2);
    RAW_BARRIER();
    STAMP(2, 3);
    if (w < 4) {
      f32x16 ar = {}, ai = {}, br = {}, bi = {};
#pragma unroll
      for (int t = 0; t < 8; ++t) {
        u32x4 dr = Dm[(t * 2) * 64 + lane], di = Dm[(t * 2 + 1) * 64 + lane];
        asm volatile("s_waitcnt vmcnt(%2)" : "+v"(ua[t]), "+v"(ub[t]) : "n"(14 - 2 * t) : "memory");
        ar = mfma(ua[t], dr, ar); ai = mfma(ua[t], di, ai); br = mfma(ub[t], dr, br); bi = mfma(ub[t], di, bi);
      }
      STAMP(2, 4);
      RAW_BARRIER();
      u32x4 yr[4] = {packs<0>(ar), packs<1>(ar), packs<0>(br), packs<1>(br)};
      u32x4 yi[4] = {packs<0>(ai), packs<1>(ai), packs<0>(bi), packs<1>(bi)};
      const unsigned k2 = r ? ~0u : 0u, k1 = (r || !h) ? ~0u : 0u;
      yr[2] &= k1; yr[3] &= k2; yi[2] &= k2; yi[3] &= k2;
      float* zp = zs + ((r ? 2 * r - 1 : 0) * 32 + l31) * ZS + 8 * h;
      float* zq = zp + (r ? 32 * ZS : 0);
      f32x16 zre[2] = {}, zim[2] = {};
      u32x4 C = F64[lane], S = F64[64 + lane];
#pragma unroll
      for (int n = 0; n < 12; ++n) {
        const int at = n >> 2, t = n & 3;
        if (n < 8) {
          const u32x4 Cn = F64[((n + 1) & 7) * 128 + lane], Sn = F64[((n + 1) & 7) * 128 + 64 + lane];
          SB();
          zim[at] = mfma(C, yi[t], zim[at]); zre[at] = mfma(C, yr[t], zre[at]);
          zim[at] = mfma(S, yr[t], zim[at]); zre[at] = mfma(S ^ 0x80008000u, yi[t], zre[at]);
          C = Cn; S = Sn;
          SB();
        }
        if (n >= 4) {
          const int off = (at - 1) * 32 + 16 * (t >> 1) + 4 * (t & 1);
          *(f32x4*)(zq + off) = f32x4{zim[at - 1][4 * t], zim[at - 1][4 * t + 1], zim[at - 1][4 * t + 2], zim[at - 1][4 * t + 3]};
          *(f32x4*)(zp + off) = f32x4{zre[at - 1][4 * t], zre[at - 1][4 * t + 1], zre[at - 1][4 * t + 2], zre[at - 1][4 * t + 3]};
        }
      }
      STAMP(2, 5);
    } else {
      VMCNT(0);
      RAW_BARRIER();
    }
    RAW_BARRIER();
    STAMP(2, 6);
    {
      const int o = tid & 15, row = tid >> 4, rot = ((o >> 1) + 4 * (o & 1)) % 7;
      float* zp = zs + row * ZS + 4 * o;
      f32x4 zl[7];
#pragma unroll
      for (int c = 0; c < 7; ++c) zl[c] = *(const f32x4*)(zp + c * 32 * ZS);
      float y[4][7];
#pragma unroll
      for (int jp = 0; jp < 2; ++jp) {
        f32x2 z[7];
#pragma unroll
        for (int c = 0; c < 7; ++c) z[c] = f32x2{zl[c][2 * jp], zl[c][2 * jp + 1]};
        f32x2 y0 = z[0] + z[1] + z[3] + z[5];
        f32x2 e1 = z[0] + z[1] * C71 + z[3] * C72 + z[5] * C73, o1 = z[2] * S71 + z[4] * S72 + z[6] * S73;
        f32x2 e2 = z[0] + z[1] * C72 + z[3] * C73 + z[5] * C71, o2 = z[2] * S72 - z[4] * S73 - z[6] * S71;
        f32x2 e3 = z[0] + z[1] * C73 + z[3] * C71 + z[5] * C72, o3 = z[2] * S73 - z[4] * S71 + z[6] * S72;
        f32x2 yy[7] = {y0, e1 - o1, e2 - o2, e3 - o3, e3 + o3, e2 + o2, e1 + o1};
#pragma unroll
        for (int b = 0; b < 7; ++b) { y[2 * jp][b] = yy[b][0]; y[2 * jp + 1][b] = yy[b][1]; }
      }
#pragma unroll
      for (int b = 0; b < 7; ++b) {
        int m = b - rot; m += m < 0 ? 7 : 0;
        *(u32x2*)(zp + m * 32 * ZS) = u32x2{pk2(y[0][b], y[1][(b + 1) % 7]), pk2(y[2][(b + 2) % 7], y[3][(b + 3) % 7])};
      }
      u16* dst = out + (s2 + 32 * (32 * sh + row)) * 896 + 448 + 4 * o;
#pragma unroll
      for (int m = 0; m < 7; ++m) *(u32x2*)(dst + 64 * m) = *(const u32x2*)(zp + m * 32 * ZS);
    }
  }
  STAMP(2, 12); STAMP(2, 15);
}

extern "C" void kernel_launch(void* const* d_in, const int* in_sizes, int n_in, void* d_out, int out_size, void* d_ws, size_t ws_size,
                              hipStream_t stream) {
  const u16* ax = (const u16*)d_in[0];
  const u16* wqkv = (const u16*)d_in[1];
  const u16* wo = (const u16*)d_in[2];
  const u16* bx = (const u16*)d_in[3];
  const u16* wr = (const u16*)d_in[4];
  const u16* wi = (const u16*)d_in[5];
  char* ws = (char*)d_ws;
  u32x4* Qf = (u32x4*)(ws);
  u32x4* KVf = (u32x4*)(ws + (2u << 20));
  u32x4* Of = (u32x4*)(ws + (6u << 20));
  unsigned* T = (unsigned*)(ws + (8u << 20));
  unsigned* U = (unsigned*)(ws + (10u << 20));
  (void)hipFuncSetAttribute((const void*)k1_kernel, hipFuncAttributeMaxDynamicSharedMemorySize, K1_LDS);
  (void)hipFuncSetAttribute((const void*)k2_kernel, hipFuncAttributeMaxDynamicSharedMemorySize, K2_LDS);
  (void)hipFuncSetAttribute((const void*)k3_kernel, hipFuncAttributeMaxDynamicSharedMemorySize, K3_LDS);
  k1_kernel<<<256, 512, K1_LDS, stream>>>(ax, wqkv, bx, Qf, KVf, (u32x4*)T);
  k2_kernel<<<256, 384, K2_LDS, stream>>>(Qf, KVf, Of, T, U, wr, wi, wo);
  k3_kernel<<<288, 512, K3_LDS, stream>>>(Of, wo, (const u32x4*)U, (u16*)d_out);
}
```

```cpp
#include <hip/hip_runtime.h>
#include <hip/hip_bf16.h>

typedef __attribute__((ext_vector_type(8))) __bf16 bf16x8;
typedef __attribute__((ext_vector_type(4))) __bf16 bf16x4;
typedef __attribute__((ext_vector_type(2))) __bf16 bf16x2;
typedef __attribute__((ext_vector_type(4))) short s16x4;
typedef __attribute__((ext_vector_type(16))) float f32x16;
typedef __attribute__((ext_vector_type(4))) float f32x4;
typedef __attribute__((ext_vector_type(2))) float f32x2;
typedef __attribute__((ext_vector_type(4))) unsigned u32x4;
typedef __attribute__((ext_vector_type(2))) unsigned u32x2;
typedef unsigned short u16;
#define LDSP __attribute__((address_space(3)))
#define DEV __device__ __forceinline__

DEV unsigned pk2(float lo, float hi) {
  f32x2 v = {lo, hi};
  return __builtin_bit_cast(unsigned, __builtin_convertvector(v, bf16x2));
}
DEV f32x16 mfma(u32x4 a, u32x4 b, f32x16 c) {
  return __builtin_amdgcn_mfma_f32_32x32x16_bf16(__builtin_bit_cast(bf16x8, a), __builtin_bit_cast(bf16x8, b), c, 0, 0, 0);
}
template <int S> DEV u32x4 packs(const f32x16& v) {
  u32x4 r = {pk2(v[8 * S + 0], v[8 * S + 1]), pk2(v[8 * S + 2], v[8 * S + 3]), pk2(v[8 * S + 4], v[8 * S + 5]), pk2(v[8 * S + 6], v[8 * S + 7])};
  return r;
}
template <int S> DEV u32x4 packsn(const f32x16& v) {
  u32x4 r = {pk2(-v[8 * S + 0], -v[8 * S + 1]), pk2(-v[8 * S + 2], -v[8 * S + 3]), pk2(-v[8 * S + 4], -v[8 * S + 5]), pk2(-v[8 * S + 6], -v[8 * S + 7])};
  return r;
}
DEV int sw23(int r) { return (r & ~12) | ((r & 4) << 1) | ((r & 8) >> 1); }
DEV float cosr(int n, float inv) { return __builtin_amdgcn_cosf((float)n * inv); }
DEV float sinr(int n, float inv) { return __builtin_amdgcn_sinf((float)n * inv); }
DEV void glds16(const void* g, char* l) {
  __builtin_amdgcn_global_load_lds((const unsigned*)g, (LDSP unsigned*)l, 16, 0, 0);
}
DEV void dma16(const void* base, unsigned voff, unsigned ldsaddr) {
  asm volatile("s_mov_b32 m0, %2\n\ts_nop 0\n\tglobal_load_lds_dwordx4 %0, %1" ::"v"(voff), "s"(base), "s"(ldsaddr) : "memory");
}
DEV unsigned ldsaddr_of(const char* p) { return (unsigned)(unsigned long long)(const LDSP char*)p; }
DEV u32x4 ald128(const void* base, unsigned off) {
  u32x4 r; asm volatile("global_load_dwordx4 %0, %1, %2" : "=v"(r) : "v"(off), "s"(base) : "memory"); return r;
}
DEV void st128(void* p, u32x4 v) { asm volatile("global_store_dwordx4 %0, %1, off sc1\n\ts_nop 1" ::"v"(p), "v"(v) : "memory"); }
#define SB() __builtin_amdgcn_sched_barrier(0)
DEV u32x2 ldtr(const char* l) {
  return __builtin_bit_cast(u32x2, __builtin_amdgcn_ds_read_tr16_b64_v4i16((LDSP s16x4*)l));
}
DEV float bflo(unsigned u) { return __builtin_bit_cast(float, u << 16); }
DEV float bfhi(unsigned u) { return __builtin_bit_cast(float, u & 0xffff0000u); }
#define RAW_BARRIER() do { asm volatile("s_waitcnt lgkmcnt(0)" ::: "memory"); __builtin_amdgcn_s_barrier(); asm volatile("" ::: "memory"); } while (0)
#define VMCNT(n) asm volatile("s_waitcnt vmcnt(" #n ")" ::: "memory")

#define STAMP_INIT
#define STAMP(k, i)
constexpr float C71 = 0.62348980185873353f, C72 = -0.22252093395631440f, C73 = -0.90096886790241913f;
constexpr float S71 = 0.78183148246802981f, S72 = 0.97492791218182361f, S73 = 0.43388373911755812f;

DEV void gen_cs(int i, int k0, int mul, int add, int mask, float inv, u32x4& C, u32x4& S) {
  float c[8], s[8];
#pragma unroll
  for (int j = 0; j < 8; ++j) {
    int n = (__mul24(i, mul * k0 + add) + __mul24(i, mul) * j) & mask;
    c[j] = cosr(n, inv); s[j] = sinr(n, inv);
  }
#pragma unroll
  for (int j = 0; j < 4; ++j) { C[j] = pk2(c[2 * j], c[2 * j + 1]); S[j] = pk2(s[2 * j], s[2 * j + 1]); }
}

constexpr double ccos64(int n) {
  n &= 63; if (n > 32) n = 64 - n;
  double sg = 1.0; if (n > 16) { n = 32 - n; sg = -1.0; }
  if (n == 16) return 0.0;
  double x = 6.283185307179586476925 * n / 64.0, x2 = x * x, term = 1.0, sum = 1.0;
  for (int i = 1; i < 14; ++i) { term *= -x2 / ((2 * i - 1) * (2 * i)); sum += term; }
  return sg * sum;
}
constexpr unsigned cbf(double v) { unsigned u = __builtin_bit_cast(unsigned, (float)v); return (u + 0x7fffu + ((u >> 16) & 1u)) >> 16; }
constexpr int csw(int r) { return (r & ~12) | ((r & 4) << 1) | ((r & 8) >> 1); }
struct alignas(16) Tabs { unsigned f64[16 * 256]; unsigned b2[14 * 256]; };
constexpr Tabs make_tabs() {
  Tabs t{};
  unsigned c[64] = {}, sn[64] = {};
  for (int n = 0; n < 64; ++n) { c[n] = cbf(ccos64(n)); sn[n] = cbf(ccos64(n - 16 + 64)); }
  for (int f = 0; f < 8; ++f)
    for (int l = 0; l < 64; ++l)
      for (int d = 0; d < 4; ++d) {
        int i = 32 * (f >> 2) + (l & 31), k = 16 * (f & 3) + 8 * (l >> 5) + 2 * d;
        t.f64[((f * 2) * 64 + l) * 4 + d] = c[(i * k) & 63] | (c[(i * (k + 1)) & 63] << 16);
        t.f64[((f * 2 + 1) * 64 + l) * 4 + d] = sn[(i * k) & 63] | (sn[(i * (k + 1)) & 63] << 16);
      }
  for (int l = 0; l < 64; ++l)
    for (int d = 0; d < 4; ++d) {
      for (int f = 0; f < 4; ++f) {
        int n = (2 * csw(l & 31) * (8 * f + 4 * (l >> 5) + d)) & 63;
        t.b2[(f * 64 + l) * 4 + d] = c[n] | (sn[n] << 16);
        t.b2[((4 + f) * 64 + l) * 4 + d] = (sn[n] ^ 0x8000u) | (c[n] << 16);
      }
      for (int f = 0; f < 2; ++f) {
        int k = 16 * f + 8 * (l >> 5) + 2 * d, n0 = (2 * (l & 31) * k) & 63, n1 = (2 * (l & 31) * (k + 1)) & 63;
        t.b2[((8 + f) * 64 + l) * 4 + d] = c[n0] | (c[n1] << 16);
        t.b2[((10 + f) * 64 + l) * 4 + d] = sn[n0] | (sn[n1] << 16);
        t.b2[((12 + f) * 64 + l) * 4 + d] = (sn[n0] | (sn[n1] << 16)) ^ 0x80008000u;
      }
    }
  return t;
}
__device__ const Tabs g_tabs = make_tabs();
DEV unsigned ald32(const void* base, unsigned off) {
  unsigned r; asm volatile("global_load_dword %0, %1, %2" : "=v"(r) : "v"(off), "s"(base) : "memory"); return r;
}
DEV unsigned ald16(const void* base, unsigned off) {
  unsigned r; asm volatile("global_load_ushort %0, %1, %2" : "=v"(r) : "v"(off), "s"(base) : "memory"); return r;
}
DEV void ast32(void* base, unsigned off, unsigned v) { asm volatile("global_store_dword %0, %1, %2 sc1" ::"v"(off), "v"(v), "s"(base) : "memory"); }
DEV int col_bin(int c) { int ct = c >> 5, k = ct == 7 ? 7 * (c - 192) : 7 * ((ct ? ((ct + 1) & 1) * 32 : 0) + (c & 31)) + 64 * ((ct + 1) >> 1); return k >= 448 ? k - 448 : k; }

constexpr int K1_SLOT = 32 * 1024;
constexpr int K1_LDS = 128 * 1024;
constexpr unsigned CODE_BYTES = 26000;
struct QF { u32x4 x0[4], x1[4], w[4]; };

template <bool ISV>
DEV void qkv_loop(const char* lds, unsigned ld, const u16* xb, const u16* wb, int kn, unsigned xo, unsigned wo0, unsigned wo1,
                  int xoff, int woff, f32x16& a0, f32x16& a1) {
  auto piece = [&](int slot, int i) {
    const unsigned d = ld + slot * K1_SLOT;
    if (i == 0) dma16(xb + kn * 64, xo, d);
    if (i == 1) dma16(wb + kn * (64 * 1344), wo0, d + 16384);
    if (i == 2) dma16(xb + kn * 64 + 64 * 448, xo, d + 8192);
    if (i == 3) { dma16(wb + kn * (64 * 1344), wo1, d + 24576); kn = kn == 6 ? 0 : kn + 1; }
  };
  auto rd = [&](QF& f, int slot, int ks) {
    const char* sl = lds + slot * K1_SLOT;
    f.x0[ks] = *(const u32x4*)(sl + (xoff ^ (ks * 32)));
    f.x1[ks] = *(const u32x4*)(sl + (xoff ^ (ks * 32)) + 4096);
    u32x2 wa = ldtr(sl + woff + ks * 16 * 224), wc = ldtr(sl + woff + ks * 16 * 224 + 224);
    f.w[ks] = u32x4{wa[0], wa[1], wc[0], wc[1]};
  };
#pragma unroll
  for (int i = 0; i < 8; ++i) piece(i >> 2, i & 3);
  QF f[2];
#pragma unroll
  for (int kc = 0; kc < 8; ++kc) {
    if (kc < 7) { if (kc < 6) VMCNT(4); else VMCNT(0); __builtin_amdgcn_s_barrier(); asm volatile("" ::: "memory"); }
#pragma unroll
    for (int ks = 0; ks < 4; ++ks) {
      if (kc < 7) rd(f[kc & 1], kc & 3, ks);
      if (kc > 0) {
        QF& g = f[(kc - 1) & 1];
        if (ISV) { a0 = mfma(g.x0[ks], g.w[ks], a0); a1 = mfma(g.x1[ks], g.w[ks], a1); }
        else     { a0 = mfma(g.w[ks], g.x0[ks], a0); a1 = mfma(g.w[ks], g.x1[ks], a1); }
      }
      if (kc < 5) piece((kc + 2) & 3, ks);
      SB();
    }
  }
}

__global__ __launch_bounds__(512, 2) void k1_kernel(const u16* __restrict__ ax, const u16* __restrict__ wqkv, const u16* __restrict__ bx,
                                                    u32x4* __restrict__ Qf, u32x4* __restrict__ KVf, u32x4* __restrict__ T) {
  extern __shared__ __attribute__((aligned(16))) char lds[];
  const int tid = threadIdx.x, lane = tid & 63, w = __builtin_amdgcn_readfirstlane(tid >> 6);
  const int xcd = blockIdx.x & 7, idx = blockIdx.x >> 3;
  const int l31 = lane & 31, h = lane >> 5;
  const unsigned lb = ldsaddr_of(lds);
  unsigned pf = 0;
  if (idx == 0) {
    const char* pc = (const char*)(__builtin_amdgcn_s_getpc() & ~127ull);
    const unsigned off = (tid & 255) * 128, lim = w < 4 ? CODE_BYTES : 14336u;
    pf = ald32(w < 4 ? pc : (const char*)g_tabs.b2, off < lim ? off : 0u);
  }
  STAMP_INIT; STAMP(0, 0); STAMP(0, 1);
  if (idx >= 8) {
    const int u = idx - 8, which = u >> 3, tm = (xcd & 1) * 8 + (u & 7), g = xcd >> 1;
    const int row = 8 * w + (lane >> 3);
    const unsigned xo = (row * 448 + ((lane & 7) ^ ((row >> 1) & 7)) * 8) * 2;
    unsigned wo2[2];
#pragma unroll
    for (int i = 0; i < 2; ++i) {
      int q = 64 * (w + 8 * i) + lane; if (q > 895) q = 895;
      int rho = q / 14, cc = q - rho * 14;
      wo2[i] = (((rho & ~7) + ((rho & 7) >> 1) + 4 * (rho & 1)) * 1344 + cc * 8) * 2;
    }
    const int hh = w & 3, th = w >> 2, r0 = th * 64 + l31;
    const int xoff = r0 * 128 + ((h ^ ((r0 >> 1) & 7)) * 16);
    const int woff = 16384 + (8 * h + 2 * ((lane >> 2) & 3)) * 224 + (hh * 28 + ((lane >> 4) & 1) * 16 + 4 * (lane & 3)) * 2;
    const int head = g * 4 + hh, tile = tm * 4 + th * 2, kn = ((u & 7) + 2 * which) % 7;
    f32x16 a0 = {}, a1 = {};
    u32x4 o0, o1, o2, o3;
    if (which == 2) {
      qkv_loop<true>(lds, lb + w * 1024, ax + tm * 128 * 448, wqkv + 896 + g * 112, kn, xo, wo2[0], wo2[1], xoff, woff, a0, a1);
      o0 = packs<0>(a0); o1 = packs<1>(a0); o2 = packs<0>(a1); o3 = packs<1>(a1);
      if (l31 >= 28) { unsigned f = (l31 == 28) ? 0x3F803F80u : 0u; o0 = u32x4{f, f, f, f}; o1 = o0; o2 = o0; o3 = o0; }
    } else {
      qkv_loop<false>(lds, lb + w * 1024, ax + tm * 128 * 448, wqkv + which * 448 + g * 112, kn, xo, wo2[0], wo2[1], xoff, woff, a0, a1);
      const float sc = which ? 1.0f : 0.18898223650461362f * 1.4426950408889634f;
      a0 *= sc; a1 *= sc;
      o0 = packs<0>(a0); o1 = packs<1>(a0); o2 = packs<0>(a1); o3 = packs<1>(a1);
      if (h) { o1[2] = which ? 0x3F80u : 0u; o1[3] = 0; o3[2] = o1[2]; o3[3] = 0; }
    }
    STAMP(0, 7);
    u32x4* o = (which == 0 ? Qf + ((head * 64 + tile) * 2) * 64 : KVf + ((head * 64 + tile) * 4 + (which & 2)) * 64) + lane;
    const int st = which == 0 ? 128 : 256;
    st128(o, o0); st128(o + 64, o1); st128(o + st, o2); st128(o + st + 64, o3);
  } else {
    const int b1 = xcd * 8 + idx, s2 = b1 >> 1, hb = b1 & 1;
    u32x4* F64 = (u32x4*)(lds + 56 * 1024);
    u32x4* Ap = (u32x4*)(lds + 72 * 1024);
    u32x4* Xf = (u32x4*)(lds + 88 * 1024);
    {
      const int row = 8 * w + (lane >> 3);
      const unsigned xo = ((s2 + 32 * ((row & 32) + sw23(row & 31))) * 448 + ((lane & 7) ^ ((row >> 1) & 7)) * 8) * 2;
#pragma unroll
      for (int m = 0; m < 7; ++m) dma16(bx + 64 * m, xo, lb + m * 8192 + w * 1024);
      dma16(g_tabs.f64 + w * 512, lane * 16, lb + 56 * 1024 + w * 2048); dma16(g_tabs.f64 + w * 512 + 256, lane * 16, lb + 57 * 1024 + w * 2048);
    }
    SB();
    const int mt = w >> 2, o = 2 * (w & 3) + h;
    {
      u32x4 C, S;
      gen_cs(32 * mt + l31, 8 * o, 32, s2, 2047, 1.0f / 2048, C, S);
      Ap[(w * 2 + 0) * 64 + lane] = C; Ap[(w * 2 + 1) * 64 + lane] = S;
    }
    char* xp = lds + (32 * mt + l31) * 128 + ((o ^ ((l31 >> 1) & 7)) * 16);
    int xm[7];
#pragma unroll
    for (int b = 0; b < 7; ++b) { int m = b - o; m += m < 0 ? 7 : 0; m += m < 0 ? 7 : 0; xm[b] = m * 8192; asm volatile("" : "+v"(xm[b])); }
    STAMP(0, 2);
    asm volatile("s_waitcnt vmcnt(2)" : "+v"(pf) :: "memory");
    RAW_BARRIER();
    STAMP(0, 3);
    {
      u32x4 xr[7], z[4];
#pragma unroll
      for (int b = 0; b < 7; ++b) xr[b] = *(const u32x4*)(xp + xm[b]);
      const float ca = hb ? C72 : C71, cb = hb ? C73 : C72, cc = hb ? C71 : C73, sa = hb ? S72 : S71, sb = hb ? -S73 : S72, sc = hb ? -S71 : S73;
      const float da = hb ? C73 : 1.0f, db = hb ? C71 : 1.0f, dc = hb ? C72 : 1.0f, ta = hb ? S73 : 0.0f, tb = hb ? -S71 : 0.0f, tc = hb ? S72 : 0.0f;
#pragma unroll
      for (int jp = 0; jp < 4; ++jp) {
        f32x2 v[7];
#pragma unroll
        for (int b = 0; b < 7; ++b) {
          v[b][0] = bflo(xr[(b - 2 * jp + 14) % 7][jp]);
          v[b][1] = bfhi(xr[(b - 2 * jp - 1 + 14) % 7][jp]);
        }
        f32x2 p1 = v[1] + v[6], p2 = v[2] + v[5], p3 = v[3] + v[4];
        f32x2 m1 = v[6] - v[1], m2 = v[5] - v[2], m3 = v[4] - v[3];
        f32x2 ra = v[0] + p1 * ca + p2 * cb + p3 * cc, ia = m1 * sa + m2 * sb + m3 * sc;
        f32x2 rb = v[0] + p1 * da + p2 * db + p3 * dc, ib = m1 * ta + m2 * tb + m3 * tc;
        z[0][jp] = pk2(ra[0], ra[1]); z[1][jp] = pk2(ia[0], ia[1]); z[2][jp] = pk2(rb[0], rb[1]); z[3][jp] = pk2(ib[0], ib[1]);
      }
#pragma unroll
      for (int c = 0; c < 4; ++c) *(u32x4*)(xp + c * 8192) = z[c];
    }
    VMCNT(0);
    RAW_BARRIER();
    STAMP(0, 4);
    const int wc = w & 3, ct = hb ? 3 + wc : (wc == 2 ? 0 : (wc == 3 ? 7 : wc + 1)), kt = w & 1;
    {
      f32x16 xre = {}, xim = {};
      const char* zp = lds + (w & 2) * 8192 + mt * 4096 + l31 * 128;
      const int zsw = (h ^ ((l31 >> 1) & 7)) * 16;
#pragma unroll
      for (int tt = 0; tt < 4; ++tt) {
        u32x4 C = F64[((kt * 4 + tt) * 2) * 64 + lane], S = F64[((kt * 4 + tt) * 2 + 1) * 64 + lane];
        u32x4 nS = S ^ 0x80008000u;
        u32x4 zr = *(const u32x4*)(zp + (zsw ^ (tt * 32))), zi = *(const u32x4*)(zp + 8192 + (zsw ^ (tt * 32)));
        xre = mfma(zr, C, xre); xim = mfma(zi, C, xim);
        xre = mfma(zi, S, xre); xim = mfma(zr, nS, xim);
      }
      u32x4* xq = Xf + (wc * 4 + mt * 2) * 128 + lane;
      xq[0] = packs<0>(xre); xq[64] = packs<0>(xim); xq[128] = packs<1>(xre); xq[192] = packs<1>(xim);
    }
    RAW_BARRIER();
    STAMP(0, 5);
    {
      f32x16 tre = {}, tim = {};
#pragma unroll
      for (int tt = 0; tt < 4; ++tt) {
        u32x4 C = Ap[((mt * 4 + tt) * 2) * 64 + lane], S = Ap[((mt * 4 + tt) * 2 + 1) * 64 + lane];
        u32x4 bre = Xf[(wc * 4 + tt) * 128 + lane], bim = Xf[(wc * 4 + tt) * 128 + 64 + lane];
        tre = mfma(C, bre, tre); tim = mfma(C, bim, tim);
        tre = mfma(S, bim, tre); tim = mfma(S ^ 0x80008000u, bre, tim);
      }
      u32x4* tp = T + (s2 * 16 + mt * 8 + h) * 232 + ct * 32 + l31;
      if (ct < 7 || l31 < 8)
#pragma unroll
      for (int q = 0; q < 4; ++q)
        st128(tp + 2 * q * 232, u32x4{pk2(tre[4 * q], tim[4 * q]), pk2(tre[4 * q + 1], tim[4 * q + 1]), pk2(tre[4 * q + 2], tim[4 * q + 2]), pk2(tre[4 * q + 3], tim[4 * q + 3])});
    }
  }
  STAMP(0, 12); STAMP(0, 15);
}

constexpr int K2_LDS = 84 * 1024;
DEV float xhalf_max(float v) {
  float a = v, b = v;
  asm volatile("s_nop 1\n\tv_permlane32_swap_b32 %0, %1\n\ts_nop 1" : "+v"(a), "+v"(b));
  return fmaxf(a, b);
}
DEV float max16(const f32x16& s) {
  float m = fmaxf(fmaxf(s[0], s[1]), s[2]);
#pragma unroll
  for (int i = 3; i < 15; i += 2) m = fmaxf(fmaxf(m, s[i]), s[i + 1]);
  return fmaxf(m, s[15]);
}
#define PIN16(a) asm volatile("" : "+v"(a[0]), "+v"(a[1]), "+v"(a[2]), "+v"(a[3]), "+v"(a[4]), "+v"(a[5]), "+v"(a[6]), "+v"(a[7]), \
                                   "+v"(a[8]), "+v"(a[9]), "+v"(a[10]), "+v"(a[11]), "+v"(a[12]), "+v"(a[13]), "+v"(a[14]), "+v"(a[15]))
#define EX(s, i) s[i] = __builtin_amdgcn_exp2f(s[i])
#define EX4(s, i, P, j)                                                                                          \
  { const float t0_ = __builtin_amdgcn_exp2f(s[i]); SB(); P[j] = pk2(e0, e1); SB();                               \
    const float t1_ = __builtin_amdgcn_exp2f(s[i + 1]); SB(); P[j + 1] = pk2(e2, e3); SB();                       \
    e0 = t0_; e1 = t1_; e2 = __builtin_amdgcn_exp2f(s[i + 2]); SB(); e3 = __builtin_amdgcn_exp2f(s[i + 3]); SB(); }
#define TILE(sc, sn, K0, K1, Q0, Q1, oc, V0, op, V1, L0, L1, L2, L3)                 \
  sn = mfma(K0, Q0, f32x16{}); SB(); L0; EX4(sc, 0, p1, 2)                           \
  op = mfma(V1, p1, op); SB(); L1; EX4(sc, 4, p0, 0)                                 \
  sn = mfma(K1, Q1, sn); SB(); L2; EX4(sc, 8, p0, 2)                                 \
  oc = mfma(V0, p0, oc); SB(); L3; EX4(sc, 12, p1, 0)
#define KTILE(X, Y, P1, P2)                                                                                      \
  TILE(sa, sb, X##k0, X##k1, q0[1], q1[1], o[0], X##v0, o[3], Y##v1, , Y##v1 = (P1)[192], , )                     \
  TILE(sb, sa, X##k0, X##k1, q0[2], q1[2], o[1], X##v0, o[0], X##v1, , , , )                                      \
  TILE(sa, sb, X##k0, X##k1, q0[3], q1[3], o[2], X##v0, o[1], X##v1, X##k0 = (P2)[0], , X##k1 = (P2)[64], )       \
  TILE(sb, sa, Y##k0, Y##k1, q0[0], q1[0], o[3], X##v0, o[2], X##v1, , , , X##v0 = (P2)[128])

__global__ __launch_bounds__(384, 2) void k2_kernel(const u32x4* __restrict__ Qf, const u32x4* __restrict__ KVf, u32x4* __restrict__ Of,
                                                    const unsigned* __restrict__ T, unsigned* __restrict__ U,
                                                    const u16* __restrict__ Wr, const u16* __restrict__ Wi, const u16* __restrict__ wo) {
  extern __shared__ __attribute__((aligned(16))) char lds[];
  const int tid = threadIdx.x, lane = tid & 63, w = __builtin_amdgcn_readfirstlane(tid >> 6);
  const int xcd = blockIdx.x & 7, idx = blockIdx.x >> 3;
  const int l31 = lane & 31, h = lane >> 5;
  STAMP_INIT; STAMP(1, 0); STAMP(1, 1);
  if (w < 4) {
    __builtin_amdgcn_s_setprio(2);
    const int head = xcd * 2 + (idx >> 4), qt = (idx & 15) * 4;
    const u32x4* kp = KVf + (head * 64 + w * 16) * 256 + lane;
    u32x4 q0[4], q1[4];
    u32x4 ek0 = kp[0], ek1 = kp[64];
#pragma unroll
    for (int i = 0; i < 4; ++i) { const u32x4* qp = Qf + (head * 64 + qt + ((w + i) & 3)) * 128 + lane; q0[i] = qp[0]; q1[i] = qp[64]; }
    u32x4 ev0 = kp[128], ev1 = kp[192], dk0 = kp[256], dk1 = kp[320], dv0 = kp[384], dv1 = {}, p0, p1 = {};
    f32x16 o[4] = {}, sa, sb;
    float m[4], e0 = 0.f, e1 = 0.f, e2 = 0.f, e3 = 0.f;
    {
      f32x16 s4[4];
#pragma unroll
      for (int i = 0; i < 4; ++i) { s4[i] = mfma(ek0, q0[i], f32x16{}); s4[i] = mfma(ek1, q1[i], s4[i]); }
#pragma unroll
      for (int i = 0; i < 4; ++i) {
        const unsigned nm = pk2(-xhalf_max(max16(s4[i])), 0.0f);
        m[i] = -bflo(nm);
        if (h) q1[i][2] = nm;
      }
      sa = s4[0] - m[0];
    }
    STAMP(1, 3);
#pragma unroll 1
    for (int it = 0; it < 8; ++it) {
      const u32x4* kn = kp + ((2 * it + 2) & 15) * 256;
      KTILE(e, d, kp + (2 * it + 1) * 256, kn)
      KTILE(d, e, kn, kn + 256)
    }
    p1[2] = pk2(e0, e1); p1[3] = pk2(e2, e3);
    o[3] = mfma(dv1, p1, o[3]);
    STAMP(1, 11);
    if (__builtin_expect(__ballot(h && !(o[0][12] + o[1][12] + o[2][12] + o[3][12] < 1.0e12f)) != 0ull, 0)) {
#pragma unroll
      for (int u = 0; u < 4; ++u) {
        f32x16 oo = {}; float mm = -3.0e38f;
        if (h) q1[u][2] = 0;
#pragma unroll 1
        for (int t = 0; t < 16; ++t) {
          const u32x4* p = kp + t * 256;
          f32x16 s = mfma(p[0], q0[u], f32x16{}); s = mfma(p[64], q1[u], s);
          float mn = fmaxf(mm, xhalf_max(max16(s)));
          float al = __builtin_amdgcn_exp2f(mm - mn);
          mm = mn;
#pragma unroll
          for (int i = 0; i < 16; ++i) { oo[i] *= al; s[i] = __builtin_amdgcn_exp2f(s[i] - mn); }
          oo = mfma(p[128], packs<0>(s), oo); oo = mfma(p[192], packs<1>(s), oo);
        }
        o[u] = oo; m[u] = mm;
      }
    }
#pragma unroll
    for (int u = 1; u < 4; ++u) {
      if (h) o[u][13] = m[u];
      f32x4* ex = (f32x4*)(lds + (((w + u) & 3) * 3 + u - 1) * 4096) + lane;
#pragma unroll
      for (int i = 0; i < 4; ++i) ex[i * 64] = f32x4{o[u][4 * i], o[u][4 * i + 1], o[u][4 * i + 2], o[u][4 * i + 3]};
    }
    const float l0 = xhalf_max(h ? o[0][12] : -1.0f);
    RAW_BARRIER();
    STAMP(1, 12);
    const char* ey = lds + w * 12288;
    f32x2 st[3];
#pragma unroll
    for (int u = 0; u < 3; ++u) st[u] = *(const f32x2*)(ey + u * 4096 + 3584 + l31 * 16);
    f32x4 y[12];
#pragma unroll
    for (int i = 0; i < 12; ++i) y[i] = *(const f32x4*)(ey + i * 1024 + lane * 16);
    SB();
    const float mm = fmaxf(fmaxf(st[0][1], st[1][1]), fmaxf(st[2][1], m[0]));
    float f0 = __builtin_amdgcn_exp2f(m[0] - mm), f[3];
#pragma unroll
    for (int u = 0; u < 3; ++u) f[u] = __builtin_amdgcn_exp2f(st[u][1] - mm);
    const float inv = __builtin_amdgcn_rcpf(l0 * f0 + st[0][0] * f[0] + st[1][0] * f[1] + st[2][0] * f[2]);
    f0 *= inv;
    f32x16 r = o[0] * f0;
    asm volatile("" : "+v"(r) : "v"(f0));
#pragma unroll
    for (int u = 0; u < 3; ++u) {
      const float fu = f[u] * inv;
#pragma unroll
      for (int i = 0; i < 4; ++i) {
        const f32x4 v = y[u * 4 + i];
        r[4 * i] += v[0] * fu; r[4 * i + 1] += v[1] * fu; r[4 * i + 2] += v[2] * fu; r[4 * i + 3] += v[3] * fu;
      }
    }
    u32x4 r1 = packs<1>(r);
    if (h) { r1[2] = 0; r1[3] = 0; }
    u32x4* op = Of + (((qt + w) * 16 + head) * 2) * 64 + lane;
    st128(op, packs<0>(r)); st128(op + 64, r1);
  } else if (idx < 29) {
    const int jq = xcd * 2 + w - 4, col = idx * 8 + (l31 >> 2), kb = col_bin(col), jr = 4 * jq + (lane & 3) + 512 * h;
    const unsigned toff = (4 * h * 16 + jq) * 928 + idx * 32 + l31;
    const bool cj = kb > 224;
    const int of = jr * 450 + 2 * kb, om = (2048 - jr) * 450 + 2 * (448 - kb), om0 = jr ? om : om - 921600;
    const float sr = col > 224 ? 0.0f : ((col == 0 || col == 224) ? 0.5f : 1.0f), si = cj ? -sr : sr;
    u32x4 mt[14];
    unsigned ta[16], ur[16], ui[16]; float wr[16], wi[16];
#pragma unroll
    for (int i = 0; i < 16; ++i) ta[i] = ald32(T + (8 * (i >> 2) + (i & 3)) * 14848, toff * 4);
#pragma unroll
    for (int g = 0; g < 16; ++g) {
      const unsigned o_ = g ? (cj ? om : of) + ((g & 7) + 16 * (g >> 3)) * (cj ? -28800 : 28800) : (cj ? om0 : of);
      ur[g] = ald16(Wr, o_); ui[g] = ald16(Wi, o_);
    }
    const unsigned* tabp = g_tabs.b2;
    asm volatile("" : "+s"(tabp));
#pragma unroll
    for (int t = 0; t < 14; ++t) mt[t] = ald128(tabp + t * 256, lane * 16);
    asm volatile("s_waitcnt vmcnt(0)" : "+v"(mt[0]), "+v"(mt[1]), "+v"(mt[2]), "+v"(mt[3]), "+v"(mt[4]), "+v"(mt[5]), "+v"(mt[6]), "+v"(mt[7]), "+v"(mt[8]),
                 "+v"(mt[9]), "+v"(mt[10]), "+v"(mt[11]), "+v"(mt[12]), "+v"(mt[13]) :: "memory");
    PIN16(ta); PIN16(ur); PIN16(ui);
#pragma unroll
    for (int g = 0; g < 16; ++g) { wr[g] = bflo(ur[g]); wi[g] = bflo(ui[g]); }
    SB();
    f32x16 xr = {}, xi = {};
#pragma unroll
    for (int t = 0; t < 4; ++t) {
      u32x4 b = {ta[4 * t], ta[4 * t + 1], ta[4 * t + 2], ta[4 * t + 3]};
      xr = mfma(mt[t], b, xr); xi = mfma(mt[4 + t], b, xi);
    }
    f32x16 yr, yi;
#pragma unroll
    for (int g = 0; g < 16; g += 2) {
      f32x2 c = {wr[g], wr[g + 1]}, d = {wi[g], wi[g + 1]};
      asm volatile("" : "+v"(c), "+v"(d));
      c *= sr; d *= si;
      const f32x2 a = {xr[g], xr[g + 1]}, b = {xi[g], xi[g + 1]}, r = a * c - b * d, i = a * d + b * c;
      yr[g] = r[0]; yr[g + 1] = r[1]; yi[g] = i[0]; yi[g + 1] = i[1];
    }
    u32x4 r0 = packs<0>(yr), r1 = packs<1>(yr), i0 = packs<0>(yi), i1 = packs<1>(yi);
    f32x16 ure = {}, uim = {};
    ure = mfma(mt[8], r0, ure); uim = mfma(mt[8], i0, uim);
    ure = mfma(mt[12], i0, ure); uim = mfma(mt[10], r0, uim);
    ure = mfma(mt[9], r1, ure); uim = mfma(mt[9], i1, uim);
    ure = mfma(mt[13], i1, ure); uim = mfma(mt[11], r1, uim);
#pragma unroll
    for (int g = 0; g < 16; ++g) ast32(U + ((g & 3) + 8 * (g >> 2)) * 14848, toff * 4, pk2(ure[g], uim[g]));
    {
      const unsigned off = ((idx * 2 + w - 4) * 64 + lane) * 128;
      if (off < 401408u) { unsigned pf = *(const unsigned*)((const char*)wo + off); asm volatile("" ::"v"(pf)); }
    }
  }
  STAMP(1, 14); STAMP(1, 15);
}

constexpr int ZS = 68;
constexpr int K3_LDS = 77 * 1024;

__global__ __launch_bounds__(512, 4) void k3_kernel(const u32x4* __restrict__ Of, const u16* __restrict__ wo, const u32x4* __restrict__ U,
                                                    u16* __restrict__ out) {
  extern __shared__ __attribute__((aligned(16))) char lds[];
  const int tid = threadIdx.x, lane = tid & 63, w = __builtin_amdgcn_readfirstlane(tid >> 6);
  const int xcd = blockIdx.x & 7, idx = blockIdx.x >> 3;
  const int l31 = lane & 31, h = lane >> 5;
  STAMP_INIT; STAMP(2, 0); STAMP(2, 1);
  if (idx < 4 || idx >= 12) {
    if (w >= 4) return;
    const int u = idx < 4 ? idx : idx - 8, tq = xcd * 4 + u / 7, tn = u % 7;
    const int qi = w & 1, kh = w >> 1;
    u32x4 of[16];
    {
      const int r8 = lane >> 3;
      const unsigned so = (r8 * 448 + ((lane & 7) ^ ((r8 & 2) << 1)) * 8) * 2, l16 = lane * 16;
      const u16* src = wo + (kh * 224 + qi * 8) * 448 + tn * 64;
      const unsigned d = ldsaddr_of(lds) + (kh * 224 + qi * 8) * 128;
      const u32x4* op = Of + (((tq * 2 + qi) * 16 + kh * 8) * 2) * 64;
#pragma unroll
      for (int st = 0; st < 2; ++st) {
#pragma unroll
        for (int i = 0; i < 7; ++i) dma16(src + (st * 7 + i) * 16 * 448, so, d + (st * 7 + i) * 2048);
#pragma unroll
        for (int i = 0; i < 8; ++i) of[st * 8 + i] = ald128(op + (st * 8 + i) * 64, l16);
      }
    }
    const int q = (lane >> 2) & 3, p = lane & 3, pc = ((p & 1) << 1) | (p >> 1);
    const int colb = ((lane >> 4) & 1) * 32 + pc * 8;
    const int base = (kh * 224 + q) * 128 + (colb ^ (((q >> 1) ^ kh) << 6));
    f32x16 a0 = {}, a1 = {};
    STAMP(2, 2);
#pragma unroll
    for (int st = 0; st < 2; ++st) {
      if (st == 0) asm volatile("s_waitcnt vmcnt(15)" : "+v"(of[0]), "+v"(of[1]), "+v"(of[2]), "+v"(of[3]), "+v"(of[4]), "+v"(of[5]), "+v"(of[6]), "+v"(of[7]) :: "memory");
      else asm volatile("s_waitcnt vmcnt(0)" : "+v"(of[8]), "+v"(of[9]), "+v"(of[10]), "+v"(of[11]), "+v"(of[12]), "+v"(of[13]), "+v"(of[14]), "+v"(of[15]) :: "memory");
      RAW_BARRIER();
      if (st == 0) STAMP(2, 3); else STAMP(2, 4);
      u32x4 fx[3], fy[3];
#pragma unroll
      for (int n = 0; n < 10; ++n) {
        if (n < 8) {
          const int i = st * 8 + n, hd = i >> 1, s = i & 1;
          const char* pa = lds + (hd * 28 + 16 * s) * 128;
          const int ra = base + h * 512, rb = (s ? base : base + h * 512) + 1024;
          u32x2 x0 = ldtr(pa + ra), x1 = ldtr(pa + rb), y0 = ldtr(pa + (ra ^ 64)), y1 = ldtr(pa + (rb ^ 64));
          fx[n % 3] = u32x4{x0[0], x0[1], x1[0], x1[1]}; fy[n % 3] = u32x4{y0[0], y0[1], y1[0], y1[1]};
        }
        SB();
        if (n >= 2) { a0 = mfma(fx[(n - 2) % 3], of[st * 8 + n - 2], a0); a1 = mfma(fy[(n - 2) % 3], of[st * 8 + n - 2], a1); }
        SB();
      }
    }
    STAMP(2, 5);
    f32x4* ex = (f32x4*)(lds + 56 * 1024);
    f32x16 snd = a1, kp = a0;
#pragma unroll
    for (int i = 0; i < 4; ++i) ex[(w * 4 + i) * 64 + lane] = f32x4{snd[4 * i], snd[4 * i + 1], snd[4 * i + 2], snd[4 * i + 3]};
    RAW_BARRIER();
#pragma unroll
    for (int i = 0; i < 4; ++i) {
      f32x4 v = ex[((w ^ 2) * 4 + i) * 64 + lane];
      kp[4 * i] += v[0]; kp[4 * i + 1] += v[1]; kp[4 * i + 2] += v[2]; kp[4 * i + 3] += v[3];
    }
    u16* dst = out + ((tq * 2 + qi) * 32 + l31) * 896 + tn * 64 + kh * 32 + 8 * h;
    *(u32x4*)dst = packs<0>(kp); *(u32x4*)(dst + 16) = packs<1>(kp);
  } else {
    const int b3 = xcd * 8 + idx - 4, s2 = b3 >> 1, sh = b3 & 1, r = w;
    u32x4* Dm = (u32x4*)lds;
    u32x4* F64 = (u32x4*)(lds + 61 * 1024);
    float* zs = (float*)lds;
    u32x4 ua[8], ub[8];
    if (w < 4) {
      const u32x4* up = U + s2 * 16 * 232 + (r ? 2 * r - 1 : 0) * 32;
      const unsigned uo = (h * 232 + sw23(l31)) * 16, uo2 = r ? uo + 512 : (h * 232 + 224 + (sw23(l31) & 7)) * 16;
#pragma unroll
      for (int i = 0; i < 8; ++i) { ua[i] = ald128(up + i * 464, uo); ub[i] = ald128(up + i * 464, uo2); }
    } else {
      const unsigned pl = (h * 32 + sw23(l31)) * 16;
#pragma unroll
      for (int i = 0; i < 4; ++i) dma16(g_tabs.f64 + ((w & 3) * 4 + i) * 256, pl, ldsaddr_of(lds) + 61 * 1024 + ((w & 3) * 4 + i) * 1024);
    }
    SB();
    {
      const int s = s2 + 32 * (32 * sh + l31);
      const float NRM2 = 2.0f / (2048.0f * 448.0f);
      u32x4 dr, di;
#pragma unroll
      for (int i = 0; i < 4; ++i) {
        int n = __mul24(8 * w + 4 * h + i, s) & 2047;
        float c = cosr(n, 1.0f / 2048) * NRM2, sn = sinr(n, 1.0f / 2048) * NRM2;
        dr[i] = pk2(c, -sn); di[i] = pk2(sn, c);
      }
      Dm[(w * 2) * 64 + lane] = dr; Dm[(w * 2 + 1) * 64 + lane] = di;
    }
    STAMP(2, 2);
    RAW_BARRIER();
    STAMP(2, 3);
    if (w < 4) {
      f32x16 ar = {}, ai = {}, br = {}, bi = {};
#pragma unroll
      for (int t = 0; t < 8; ++t) {
        u32x4 dr = Dm[(t * 2) * 64 + lane], di = Dm[(t * 2 + 1) * 64 + lane];
        asm volatile("s_waitcnt vmcnt(%2)" : "+v"(ua[t]), "+v"(ub[t]) : "n"(14 - 2 * t) : "memory");
        ar = mfma(ua[t], dr, ar); ai = mfma(ua[t], di, ai); br = mfma(ub[t], dr, br); bi = mfma(ub[t], di, bi);
      }
      STAMP(2, 4);
      RAW_BARRIER();
      u32x4 yr[4] = {packs<0>(ar), packs<1>(ar), packs<0>(br), packs<1>(br)};
      u32x4 yi[4] = {packs<0>(ai), packs<1>(ai), packs<0>(bi), packs<1>(bi)};
      const unsigned k2 = r ? ~0u : 0u, k1 = (r || !h) ? ~0u : 0u;
      yr[2] &= k1; yr[3] &= k2; yi[2] &= k2; yi[3] &= k2;
      float* zp = zs + ((r ? 2 * r - 1 : 0) * 32 + l31) * ZS + 8 * h;
      float* zq = zp + (r ? 32 * ZS : 0);
      f32x16 zre[2] = {}, zim[2] = {};
      u32x4 C = F64[lane], S = F64[64 + lane];
#pragma unroll
      for (int n = 0; n < 12; ++n) {
        const int at = n >> 2, t = n & 3;
        if (n < 8) {
          const u32x4 Cn = F64[((n + 1) & 7) * 128 + lane], Sn = F64[((n + 1) & 7) * 128 + 64 + lane];
          SB();
          zim[at] = mfma(C, yi[t], zim[at]); zre[at] = mfma(C, yr[t], zre[at]);
          zim[at] = mfma(S, yr[t], zim[at]); zre[at] = mfma(S ^ 0x80008000u, yi[t], zre[at]);
          C = Cn; S = Sn;
          SB();
        }
        if (n >= 4) {
          const int off = (at - 1) * 32 + 16 * (t >> 1) + 4 * (t & 1);
          *(f32x4*)(zq + off) = f32x4{zim[at - 1][4 * t], zim[at - 1][4 * t + 1], zim[at - 1][4 * t + 2], zim[at - 1][4 * t + 3]};
          *(f32x4*)(zp + off) = f32x4{zre[at - 1][4 * t], zre[at - 1][4 * t + 1], zre[at - 1][4 * t + 2], zre[at - 1][4 * t + 3]};
        }
      }
      STAMP(2, 5);
    } else {
      VMCNT(0);
      RAW_BARRIER();
    }
    RAW_BARRIER();
    STAMP(2, 6);
    {
      const int o = tid & 15, row = tid >> 4, rot = ((o >> 1) + 4 * (o & 1)) % 7;
      float* zp = zs + row * ZS + 4 * o;
      f32x4 zl[7];
#pragma unroll
      for (int c = 0; c < 7; ++c) zl[c] = *(const f32x4*)(zp + c * 32 * ZS);
      float y[4][7];
#pragma unroll
      for (int jp = 0; jp < 2; ++jp) {
        f32x2 z[7];
#pragma unroll
        for (int c = 0; c < 7; ++c) z[c] = f32x2{zl[c][2 * jp], zl[c][2 * jp + 1]};
        f32x2 y0 = z[0] + z[1] + z[3] + z[5];
        f32x2 e1 = z[0] + z[1] * C71 + z[3] * C72 + z[5] * C73, o1 = z[2] * S71 + z[4] * S72 + z[6] * S73;
        f32x2 e2 = z[0] + z[1] * C72 + z[3] * C73 + z[5] * C71, o2 = z[2] * S72 - z[4] * S73 - z[6] * S71;
        f32x2 e3 = z[0] + z[1] * C73 + z[3] * C71 + z[5] * C72, o3 = z[2] * S73 - z[4] * S71 + z[6] * S72;
        f32x2 yy[7] = {y0, e1 - o1, e2 - o2, e3 - o3, e3 + o3, e2 + o2, e1 + o1};
#pragma unroll
        for (int b = 0; b < 7; ++b) { y[2 * jp][b] = yy[b][0]; y[2 * jp + 1][b] = yy[b][1]; }
      }
#pragma unroll
      for (int b = 0; b < 7; ++b) {
        int m = b - rot; m += m < 0 ? 7 : 0;
        *(u32x2*)(zp + m * 32 * ZS) = u32x2{pk2(y[0][b], y[1][(b + 1) % 7]), pk2(y[2][(b + 2) % 7], y[3][(b + 3) % 7])};
      }
      u16* dst = out + (s2 + 32 * (32 * sh + row)) * 896 + 448 + 4 * o;
#pragma unroll
      for (int m = 0; m < 7; ++m) *(u32x2*)(dst + 64 * m) = *(const u32x2*)(zp + m * 32 * ZS);
    }
  }
  STAMP(2, 12); STAMP(2, 15);
}

extern "C" void kernel_launch(void* const* d_in, const int* in_sizes, int n_in, void* d_out, int out_size, void* d_ws, size_t ws_size,
                              hipStream_t stream) {
  const u16* ax = (const u16*)d_in[0];
  const u16* wqkv = (const u16*)d_in[1];
  const u16* wo = (const u16*)d_in[2];
  const u16* bx = (const u16*)d_in[3];
  const u16* wr = (const u16*)d_in[4];
  const u16* wi = (const u16*)d_in[5];
  char* ws = (char*)d_ws;
  u32x4* Qf = (u32x4*)(ws);
  u32x4* KVf = (u32x4*)(ws + (2u << 20));
  u32x4* Of = (u32x4*)(ws + (6u << 20));
  unsigned* T = (unsigned*)(ws + (8u << 20));
  unsigned* U = (unsigned*)(ws + (10u << 20));
  (void)hipFuncSetAttribute((const void*)k1_kernel, hipFuncAttributeMaxDynamicSharedMemorySize, K1_LDS);
  (void)hipFuncSetAttribute((const void*)k2_kernel, hipFuncAttributeMaxDynamicSharedMemorySize, K2_LDS);
  (void)hipFuncSetAttribute((const void*)k3_kernel, hipFuncAttributeMaxDynamicSharedMemorySize, K3_LDS);
  k1_kernel<<<256, 512, K1_LDS, stream>>>(ax, wqkv, bx, Qf, KVf, (u32x4*)T);
  k2_kernel<<<256, 384, K2_LDS, stream>>>(Qf, KVf, Of, T, U, wr, wi, wo);
  k3_kernel<<<288, 512, K3_LDS, stream>>>(Of, wo, (const u32x4*)U, (u16*)d_out);
}
```

```cpp
#include <hip/hip_runtime.h>
#include <hip/hip_bf16.h>

typedef __attribute__((ext_vector_type(8))) __bf16 bf16x8;
typedef __attribute__((ext_vector_type(4))) __bf16 bf16x4;
typedef __attribute__((ext_vector_type(2))) __bf16 bf16x2;
typedef __attribute__((ext_vector_type(4))) short s16x4;
typedef __attribute__((ext_vector_type(16))) float f32x16;
typedef __attribute__((ext_vector_type(4))) float f32x4;
typedef __attribute__((ext_vector_type(2))) float f32x2;
typedef __attribute__((ext_vector_type(4))) unsigned u32x4;
typedef __attribute__((ext_vector_type(2))) unsigned u32x2;
typedef unsigned short u16;
#define LDSP __attribute__((address_space(3)))
#define DEV __device__ __forceinline__

DEV unsigned pk2(float lo, float hi) {
  f32x2 v = {lo, hi};
  return __builtin_bit_cast(unsigned, __builtin_convertvector(v, bf16x2));
}
DEV f32x16 mfma(u32x4 a, u32x4 b, f32x16 c) {
  return __builtin_amdgcn_mfma_f32_32x32x16_bf16(__builtin_bit_cast(bf16x8, a), __builtin_bit_cast(bf16x8, b), c, 0, 0, 0);
}
template <int S> DEV u32x4 packs(const f32x16& v) {
  u32x4 r = {pk2(v[8 * S + 0], v[8 * S + 1]), pk2(v[8 * S + 2], v[8 * S + 3]), pk2(v[8 * S + 4], v[8 * S + 5]), pk2(v[8 * S + 6], v[8 * S + 7])};
  return r;
}
template <int S> DEV u32x4 packsn(const f32x16& v) {
  u32x4 r = {pk2(-v[8 * S + 0], -v[8 * S + 1]), pk2(-v[8 * S + 2], -v[8 * S + 3]), pk2(-v[8 * S + 4], -v[8 * S + 5]), pk2(-v[8 * S + 6], -v[8 * S + 7])};
  return r;
}
DEV int sw23(int r) { return (r & ~12) | ((r & 4) << 1) | ((r & 8) >> 1); }
DEV float cosr(int n, float inv) { return __builtin_amdgcn_cosf((float)n * inv); }
DEV float sinr(int n, float inv) { return __builtin_amdgcn_sinf((float)n * inv); }
DEV void glds16(const void* g, char* l) {
  __builtin_amdgcn_global_load_lds((const unsigned*)g, (LDSP unsigned*)l, 16, 0, 0);
}
DEV void dma16(const void* base, unsigned voff, unsigned ldsaddr) {
  asm volatile("s_mov_b32 m0, %2\n\ts_nop 0\n\tglobal_load_lds_dwordx4 %0, %1" ::"v"(voff), "s"(base), "s"(ldsaddr) : "memory");
}
DEV unsigned ldsaddr_of(const char* p) { return (unsigned)(unsigned long long)(const LDSP char*)p; }
DEV u32x4 ald128(const void* base, unsigned off) {
  u32x4 r; asm volatile("global_load_dwordx4 %0, %1, %2" : "=v"(r) : "v"(off), "s"(base) : "memory"); return r;
}
DEV void st128(void* p, u32x4 v) { asm volatile("global_store_dwordx4 %0, %1, off sc1\n\ts_nop 1" ::"v"(p), "v"(v) : "memory"); }
#define SB() __builtin_amdgcn_sched_barrier(0)
DEV u32x2 ldtr(const char* l) {
  return __builtin_bit_cast(u32x2, __builtin_amdgcn_ds_read_tr16_b64_v4i16((LDSP s16x4*)l));
}
DEV float bflo(unsigned u) { return __builtin_bit_cast(float, u << 16); }
DEV float bfhi(unsigned u) { return __builtin_bit_cast(float, u & 0xffff0000u); }
#define RAW_BARRIER() do { asm volatile("s_waitcnt lgkmcnt(0)" ::: "memory"); __builtin_amdgcn_s_barrier(); asm volatile("" ::: "memory"); } while (0)
#define VMCNT(n) asm volatile("s_waitcnt vmcnt(" #n ")" ::: "memory")

#define STAMP_INIT
#define STAMP(k, i)
constexpr float C71 = 0.62348980185873353f, C72 = -0.22252093395631440f, C73 = -0.90096886790241913f;
constexpr float S71 = 0.78183148246802981f, S72 = 0.97492791218182361f, S73 = 0.43388373911755812f;

DEV void gen_cs(int i, int k0, int mul, int add, int mask, float inv, u32x4& C, u32x4& S) {
  float c[8], s[8];
#pragma unroll
  for (int j = 0; j < 8; ++j) {
    int n = (__mul24(i, mul * k0 + add) + __mul24(i, mul) * j) & mask;
    c[j] = cosr(n, inv); s[j] = sinr(n, inv);
  }
#pragma unroll
  for (int j = 0; j < 4; ++j) { C[j] = pk2(c[2 * j], c[2 * j + 1]); S[j] = pk2(s[2 * j], s[2 * j + 1]); }
}

constexpr double ccos64(int n) {
  n &= 63; if (n > 32) n = 64 - n;
  double sg = 1.0; if (n > 16) { n = 32 - n; sg = -1.0; }
  if (n == 16) return 0.0;
  double x = 6.283185307179586476925 * n / 64.0, x2 = x * x, term = 1.0, sum = 1.0;
  for (int i = 1; i < 14; ++i) { term *= -x2 / ((2 * i - 1) * (2 * i)); sum += term; }
  return sg * sum;
}
constexpr unsigned cbf(double v) { unsigned u = __builtin_bit_cast(unsigned, (float)v); return (u + 0x7fffu + ((u >> 16) & 1u)) >> 16; }
constexpr int csw(int r) { return (r & ~12) | ((r & 4) << 1) | ((r & 8) >> 1); }
struct alignas(16) Tabs { unsigned f64[16 * 256]; unsigned b2[14 * 256]; };
constexpr Tabs make_tabs() {
  Tabs t{};
  unsigned c[64] = {}, sn[64] = {};
  for (int n = 0; n < 64; ++n) { c[n] = cbf(ccos64(n)); sn[n] = cbf(ccos64(n - 16 + 64)); }
  for (int f = 0; f < 8; ++f)
    for (int l = 0; l < 64; ++l)
      for (int d = 0; d < 4; ++d) {
        int i = 32 * (f >> 2) + (l & 31), k = 16 * (f & 3) + 8 * (l >> 5) + 2 * d;
        t.f64[((f * 2) * 64 + l) * 4 + d] = c[(i * k) & 63] | (c[(i * (k + 1)) & 63] << 16);
        t.f64[((f * 2 + 1) * 64 + l) * 4 + d] = sn[(i * k) & 63] | (sn[(i * (k + 1)) & 63] << 16);
      }
  for (int l = 0; l < 64; ++l)
    for (int d = 0; d < 4; ++d) {
      for (int f = 0; f < 4; ++f) {
        int n = (2 * csw(l & 31) * (8 * f + 4 * (l >> 5) + d)) & 63;
        t.b2[(f * 64 + l) * 4 + d] = c[n] | (sn[n] << 16);
        t.b2[((4 + f) * 64 + l) * 4 + d] = (sn[n] ^ 0x8000u) | (c[n] << 16);
      }
      for (int f = 0; f < 2; ++f) {
        int k = 16 * f + 8 * (l >> 5) + 2 * d, n0 = (2 * (l & 31) * k) & 63, n1 = (2 * (l & 31) * (k + 1)) & 63;
        t.b2[((8 + f) * 64 + l) * 4 + d] = c[n0] | (c[n1] << 16);
        t.b2[((10 + f) * 64 + l) * 4 + d] = sn[n0] | (sn[n1] << 16);
        t.b2[((12 + f) * 64 + l) * 4 + d] = (sn[n0] | (sn[n1] << 16)) ^ 0x80008000u;
      }
    }
  return t;
}
__device__ const Tabs g_tabs = make_tabs();
DEV unsigned ald32(const void* base, unsigned off) {
  unsigned r; asm volatile("global_load_dword %0, %1, %2" : "=v"(r) : "v"(off), "s"(base) : "memory"); return r;
}
DEV unsigned ald16(const void* base, unsigned off) {
  unsigned r; asm volatile("global_load_ushort %0, %1, %2" : "=v"(r) : "v"(off), "s"(base) : "memory"); return r;
}
DEV void ast32(void* base, unsigned off, unsigned v) { asm volatile("global_store_dword %0, %1, %2 sc1" ::"v"(off), "v"(v), "s"(base) : "memory"); }
DEV int col_bin(int c) { int ct = c >> 5, k = ct == 7 ? 7 * (c - 192) : 7 * ((ct ? ((ct + 1) & 1) * 32 : 0) + (c & 31)) + 64 * ((ct + 1) >> 1); return k >= 448 ? k - 448 : k; }

constexpr int K1_SLOT = 32 * 1024;
constexpr int K1_LDS = 128 * 1024;
constexpr unsigned CODE_BYTES = 26000;
struct QF { u32x4 x0[4], x1[4], w[4]; };

template <bool ISV>
DEV void qkv_loop(const char* lds, unsigned ld, const u16* xb, const u16* wb, int kn, unsigned xo, unsigned wo0, unsigned wo1,
                  int xoff, int woff, f32x16& a0, f32x16& a1) {
  auto piece = [&](int slot, int i) {
    const unsigned d = ld + slot * K1_SLOT;
    if (i == 0) dma16(xb + kn * 64, xo, d);
    if (i == 1) dma16(wb + kn * (64 * 1344), wo0, d + 16384);
    if (i == 2) dma16(xb + kn * 64 + 64 * 448, xo, d + 8192);
    if (i == 3) { dma16(wb + kn * (64 * 1344), wo1, d + 24576); kn = kn == 6 ? 0 : kn + 1; }
  };
  auto rd = [&](QF& f, int slot, int ks) {
    const char* sl = lds + slot * K1_SLOT;
    f.x0[ks] = *(const u32x4*)(sl + (xoff ^ (ks * 32)));
    f.x1[ks] = *(const u32x4*)(sl + (xoff ^ (ks * 32)) + 4096);
    u32x2 wa = ldtr(sl + woff + ks * 16 * 224), wc = ldtr(sl + woff + ks * 16 * 224 + 224);
    f.w[ks] = u32x4{wa[0], wa[1], wc[0], wc[1]};
  };
#pragma unroll
  for (int i = 0; i < 8; ++i) piece(i >> 2, i & 3);
  QF f[2];
#pragma unroll
  for (int kc = 0; kc < 8; ++kc) {
    if (kc < 7) { if (kc < 6) VMCNT(4); else VMCNT(0); __builtin_amdgcn_s_barrier(); asm volatile("" ::: "memory"); }
#pragma unroll
    for (int ks = 0; ks < 4; ++ks) {
      if (kc < 7) rd(f[kc & 1], kc & 3, ks);
      if (kc > 0) {
        QF& g = f[(kc - 1) & 1];
        if (ISV) { a0 = mfma(g.x0[ks], g.w[ks], a0); a1 = mfma(g.x1[ks], g.w[ks], a1); }
        else     { a0 = mfma(g.w[ks], g.x0[ks], a0); a1 = mfma(g.w[ks], g.x1[ks], a1); }
      }
      if (kc < 5) piece((kc + 2) & 3, ks);
      SB();
    }
  }
}

__global__ __launch_bounds__(512, 2) void k1_kernel(const u16* __restrict__ ax, const u16* __restrict__ wqkv, const u16* __restrict__ bx,
                                                    u32x4* __restrict__ Qf, u32x4* __restrict__ KVf, u32x4* __restrict__ T,
                                                    const u16* Wr, const u16* Wi, const u16* wo, const u16* outp) {
  extern __shared__ __attribute__((aligned(16))) char lds[];
  const int tid = threadIdx.x, lane = tid & 63, w = __builtin_amdgcn_readfirstlane(tid >> 6);
  const int xcd = blockIdx.x & 7, idx = blockIdx.x >> 3;
  const int l31 = lane & 31, h = lane >> 5;
  const unsigned lb = ldsaddr_of(lds);
  unsigned pf = 0;
  if (idx == 0) {
    const char* pc = (const char*)(__builtin_amdgcn_s_getpc() & ~127ull);
    const unsigned off = (tid & 255) * 128, lim = w < 4 ? CODE_BYTES : 14336u;
    pf = ald32(w < 4 ? pc : (const char*)g_tabs.b2, off < lim ? off : 0u);
  }
  if (idx == 1) {
    const void* base = w < 3 ? (const void*)Qf : (w == 3 ? (const void*)outp : (w == 4 ? (const void*)Wr : (w == 5 ? (const void*)Wi : (const void*)wo)));
    const unsigned off = (w < 3 ? tid : lane) * 65536u, lim = w < 3 ? (12u << 20) : (w == 3 ? 3670016u : (w < 6 ? 921600u : 401408u));
    pf = ald32(base, off < lim ? off : 0u);
  }
  STAMP_INIT; STAMP(0, 0); STAMP(0, 1);
  if (idx >= 8) {
    const int u = idx - 8, which = u >> 3, tm = (xcd & 1) * 8 + (u & 7), g = xcd >> 1;
    const int row = 8 * w + (lane >> 3);
    const unsigned xo = (row * 448 + ((lane & 7) ^ ((row >> 1) & 7)) * 8) * 2;
    unsigned wo2[2];
#pragma unroll
    for (int i = 0; i < 2; ++i) {
      int q = 64 * (w + 8 * i) + lane; if (q > 895) q = 895;
      int rho = q / 14, cc = q - rho * 14;
      wo2[i] = (((rho & ~7) + ((rho & 7) >> 1) + 4 * (rho & 1)) * 1344 + cc * 8) * 2;
    }
    const int hh = w & 3, th = w >> 2, r0 = th * 64 + l31;
    const int xoff = r0 * 128 + ((h ^ ((r0 >> 1) & 7)) * 16);
    const int woff = 16384 + (8 * h + 2 * ((lane >> 2) & 3)) * 224 + (hh * 28 + ((lane >> 4) & 1) * 16 + 4 * (lane & 3)) * 2;
    const int head = g * 4 + hh, tile = tm * 4 + th * 2, kn = ((u & 7) + 2 * which) % 7;
    f32x16 a0 = {}, a1 = {};
    u32x4 o0, o1, o2, o3;
    if (which == 2) {
      qkv_loop<true>(lds, lb + w * 1024, ax + tm * 128 * 448, wqkv + 896 + g * 112, kn, xo, wo2[0], wo2[1], xoff, woff, a0, a1);
      o0 = packs<0>(a0); o1 = packs<1>(a0); o2 = packs<0>(a1); o3 = packs<1>(a1);
      if (l31 >= 28) { unsigned f = (l31 == 28) ? 0x3F803F80u : 0u; o0 = u32x4{f, f, f, f}; o1 = o0; o2 = o0; o3 = o0; }
    } else {
      qkv_loop<false>(lds, lb + w * 1024, ax + tm * 128 * 448, wqkv + which * 448 + g * 112, kn, xo, wo2[0], wo2[1], xoff, woff, a0, a1);
      const float sc = which ? 1.0f : 0.18898223650461362f * 1.4426950408889634f;
      a0 *= sc; a1 *= sc;
      o0 = packs<0>(a0); o1 = packs<1>(a0); o2 = packs<0>(a1); o3 = packs<1>(a1);
      if (h) { o1[2] = which ? 0x3F80u : 0u; o1[3] = 0; o3[2] = o1[2]; o3[3] = 0; }
    }
    STAMP(0, 7);
    u32x4* o = (which == 0 ? Qf + ((head * 64 + tile) * 2) * 64 : KVf + ((head * 64 + tile) * 4 + (which & 2)) * 64) + lane;
    const int st = which == 0 ? 128 : 256;
    st128(o, o0); st128(o + 64, o1); st128(o + st, o2); st128(o + st + 64, o3);
  } else {
    const int b1 = xcd * 8 + idx, s2 = b1 >> 1, hb = b1 & 1;
    u32x4* F64 = (u32x4*)(lds + 56 * 1024);
    u32x4* Ap = (u32x4*)(lds + 72 * 1024);
    u32x4* Xf = (u32x4*)(lds + 88 * 1024);
    {
      const int row = 8 * w + (lane >> 3);
      const unsigned xo = ((s2 + 32 * ((row & 32) + sw23(row & 31))) * 448 + ((lane & 7) ^ ((row >> 1) & 7)) * 8) * 2;
#pragma unroll
      for (int m = 0; m < 7; ++m) dma16(bx + 64 * m, xo, lb + m * 8192 + w * 1024);
      dma16(g_tabs.f64 + w * 512, lane * 16, lb + 56 * 1024 + w * 2048); dma16(g_tabs.f64 + w * 512 + 256, lane * 16, lb + 57 * 1024 + w * 2048);
    }
    SB();
    const int mt = w >> 2, o = 2 * (w & 3) + h;
    {
      u32x4 C, S;
      gen_cs(32 * mt + l31, 8 * o, 32, s2, 2047, 1.0f / 2048, C, S);
      Ap[(w * 2 + 0) * 64 + lane] = C; Ap[(w * 2 + 1) * 64 + lane] = S;
    }
    char* xp = lds + (32 * mt + l31) * 128 + ((o ^ ((l31 >> 1) & 7)) * 16);
    int xm[7];
#pragma unroll
    for (int b = 0; b < 7; ++b) { int m = b - o; m += m < 0 ? 7 : 0; m += m < 0 ? 7 : 0; xm[b] = m * 8192; asm volatile("" : "+v"(xm[b])); }
    STAMP(0, 2);
    asm volatile("s_waitcnt vmcnt(2)" : "+v"(pf) :: "memory");
    RAW_BARRIER();
    STAMP(0, 3);
    {
      u32x4 xr[7], z[4];
#pragma unroll
      for (int b = 0; b < 7; ++b) xr[b] = *(const u32x4*)(xp + xm[b]);
      const float ca = hb ? C72 : C71, cb = hb ? C73 : C72, cc = hb ? C71 : C73, sa = hb ? S72 : S71, sb = hb ? -S73 : S72, sc = hb ? -S71 : S73;
      const float da = hb ? C73 : 1.0f, db = hb ? C71 : 1.0f, dc = hb ? C72 : 1.0f, ta = hb ? S73 : 0.0f, tb = hb ? -S71 : 0.0f, tc = hb ? S72 : 0.0f;
#pragma unroll
      for (int jp = 0; jp < 4; ++jp) {
        f32x2 v[7];
#pragma unroll
        for (int b = 0; b < 7; ++b) {
          v[b][0] = bflo(xr[(b - 2 * jp + 14) % 7][jp]);
          v[b][1] = bfhi(xr[(b - 2 * jp - 1 + 14) % 7][jp]);
        }
        f32x2 p1 = v[1] + v[6], p2 = v[2] + v[5], p3 = v[3] + v[4];
        f32x2 m1 = v[6] - v[1], m2 = v[5] - v[2], m3 = v[4] - v[3];
        f32x2 ra = v[0] + p1 * ca + p2 * cb + p3 * cc, ia = m1 * sa + m2 * sb + m3 * sc;
        f32x2 rb = v[0] + p1 * da + p2 * db + p3 * dc, ib = m1 * ta + m2 * tb + m3 * tc;
        z[0][jp] = pk2(ra[0], ra[1]); z[1][jp] = pk2(ia[0], ia[1]); z[2][jp] = pk2(rb[0], rb[1]); z[3][jp] = pk2(ib[0], ib[1]);
      }
#pragma unroll
      for (int c = 0; c < 4; ++c) *(u32x4*)(xp + c * 8192) = z[c];
    }
    VMCNT(0);
    RAW_BARRIER();
    STAMP(0, 4);
    const int wc = w & 3, ct = hb ? 3 + wc : (wc == 2 ? 0 : (wc == 3 ? 7 : wc + 1)), kt = w & 1;
    {
      f32x16 xre = {}, xim = {};
      const char* zp = lds + (w & 2) * 8192 + mt * 4096 + l31 * 128;
      const int zsw = (h ^ ((l31 >> 1) & 7)) * 16;
#pragma unroll
      for (int tt = 0; tt < 4; ++tt) {
        u32x4 C = F64[((kt * 4 + tt) * 2) * 64 + lane], S = F64[((kt * 4 + tt) * 2 + 1) * 64 + lane];
        u32x4 nS = S ^ 0x80008000u;
        u32x4 zr = *(const u32x4*)(zp + (zsw ^ (tt * 32))), zi = *(const u32x4*)(zp + 8192 + (zsw ^ (tt * 32)));
        xre = mfma(zr, C, xre); xim = mfma(zi, C, xim);
        xre = mfma(zi, S, xre); xim = mfma(zr, nS, xim);
      }
      u32x4* xq = Xf + (wc * 4 + mt * 2) * 128 + lane;
      xq[0] = packs<0>(xre); xq[64] = packs<0>(xim); xq[128] = packs<1>(xre); xq[192] = packs<1>(xim);
    }
    RAW_BARRIER();
    STAMP(0, 5);
    {
      f32x16 tre = {}, tim = {};
#pragma unroll
      for (int tt = 0; tt < 4; ++tt) {
        u32x4 C = Ap[((mt * 4 + tt) * 2) * 64 + lane], S = Ap[((mt * 4 + tt) * 2 + 1) * 64 + lane];
        u32x4 bre = Xf[(wc * 4 + tt) * 128 + lane], bim = Xf[(wc * 4 + tt) * 128 + 64 + lane];
        tre = mfma(C, bre, tre); tim = mfma(C, bim, tim);
        tre = mfma(S, bim, tre); tim = mfma(S ^ 0x80008000u, bre, tim);
      }
      u32x4* tp = T + (s2 * 16 + mt * 8 + h) * 232 + ct * 32 + l31;
      if (ct < 7 || l31 < 8)
#pragma unroll
      for (int q = 0; q < 4; ++q)
        st128(tp + 2 * q * 232, u32x4{pk2(tre[4 * q], tim[4 * q]), pk2(tre[4 * q + 1], tim[4 * q + 1]), pk2(tre[4 * q + 2], tim[4 * q + 2]), pk2(tre[4 * q + 3], tim[4 * q + 3])});
    }
  }
  STAMP(0, 12); STAMP(0, 15);
}

constexpr int K2_LDS = 84 * 1024;
DEV float xhalf_max(float v) {
  float a = v, b = v;
  asm volatile("s_nop 1\n\tv_permlane32_swap_b32 %0, %1\n\ts_nop 1" : "+v"(a), "+v"(b));
  return fmaxf(a, b);
}
DEV float max16(const f32x16& s) {
  float m = fmaxf(fmaxf(s[0], s[1]), s[2]);
#pragma unroll
  for (int i = 3; i < 15; i += 2) m = fmaxf(fmaxf(m, s[i]), s[i + 1]);
  return fmaxf(m, s[15]);
}
#define PIN16(a) asm volatile("" : "+v"(a[0]), "+v"(a[1]), "+v"(a[2]), "+v"(a[3]), "+v"(a[4]), "+v"(a[5]), "+v"(a[6]), "+v"(a[7]), \
                                   "+v"(a[8]), "+v"(a[9]), "+v"(a[10]), "+v"(a[11]), "+v"(a[12]), "+v"(a[13]), "+v"(a[14]), "+v"(a[15]))
#define EX(s, i) s[i] = __builtin_amdgcn_exp2f(s[i])
#define EX4(s, i, P, j)                                                                                          \
  { const float t0_ = __builtin_amdgcn_exp2f(s[i]); SB(); P[j] = pk2(e0, e1); SB();                               \
    const float t1_ = __builtin_amdgcn_exp2f(s[i + 1]); SB(); P[j + 1] = pk2(e2, e3); SB();                       \
    e0 = t0_; e1 = t1_; e2 = __builtin_amdgcn_exp2f(s[i + 2]); SB(); e3 = __builtin_amdgcn_exp2f(s[i + 3]); SB(); }
#define TILE(sc, sn, K0, K1, Q0, Q1, oc, V0, op, V1, L0, L1, L2, L3)                 \
  sn = mfma(K0, Q0, f32x16{}); SB(); L0; EX4(sc, 0, p1, 2)                           \
  op = mfma(V1, p1, op); SB(); L1; EX4(sc, 4, p0, 0)                                 \
  sn = mfma(K1, Q1, sn); SB(); L2; EX4(sc, 8, p0, 2)                                 \
  oc = mfma(V0, p0, oc); SB(); L3; EX4(sc, 12, p1, 0)
#define KTILE(X, Y, P1, P2)                                                                                      \
  TILE(sa, sb, X##k0, X##k1, q0[1], q1[1], o[0], X##v0, o[3], Y##v1, , Y##v1 = (P1)[192], , )                     \
  TILE(sb, sa, X##k0, X##k1, q0[2], q1[2], o[1], X##v0, o[0], X##v1, , , , )                                      \
  TILE(sa, sb, X##k0, X##k1, q0[3], q1[3], o[2], X##v0, o[1], X##v1, X##k0 = (P2)[0], , X##k1 = (P2)[64], )       \
  TILE(sb, sa, Y##k0, Y##k1, q0[0], q1[0], o[3], X##v0, o[2], X##v1, , , , X##v0 = (P2)[128])

__global__ __launch_bounds__(384, 2) void k2_kernel(const u32x4* __restrict__ Qf, const u32x4* __restrict__ KVf, u32x4* __restrict__ Of,
                                                    const unsigned* __restrict__ T, unsigned* __restrict__ U,
                                                    const u16* __restrict__ Wr, const u16* __restrict__ Wi, const u16* __restrict__ wo) {
  extern __shared__ __attribute__((aligned(16))) char lds[];
  const int tid = threadIdx.x, lane = tid & 63, w = __builtin_amdgcn_readfirstlane(tid >> 6);
  const int xcd = blockIdx.x & 7, idx = blockIdx.x >> 3;
  const int l31 = lane & 31, h = lane >> 5;
  STAMP_INIT; STAMP(1, 0); STAMP(1, 1);
  if (w < 4) {
    __builtin_amdgcn_s_setprio(2);
    const int head = xcd * 2 + (idx >> 4), qt = (idx & 15) * 4;
    const u32x4* kp = KVf + (head * 64 + w * 16) * 256 + lane;
    u32x4 q0[4], q1[4];
    u32x4 ek0 = kp[0], ek1 = kp[64];
#pragma unroll
    for (int i = 0; i < 4; ++i) { const u32x4* qp = Qf + (head * 64 + qt + ((w + i) & 3)) * 128 + lane; q0[i] = qp[0]; q1[i] = qp[64]; }
    u32x4 ev0 = kp[128], ev1 = kp[192], dk0 = kp[256], dk1 = kp[320], dv0 = kp[384], dv1 = {}, p0, p1 = {};
    f32x16 o[4] = {}, sa, sb;
    float m[4], e0 = 0.f, e1 = 0.f, e2 = 0.f, e3 = 0.f;
    {
      f32x16 s4[4];
#pragma unroll
      for (int i = 0; i < 4; ++i) { s4[i] = mfma(ek0, q0[i], f32x16{}); s4[i] = mfma(ek1, q1[i], s4[i]); }
#pragma unroll
      for (int i = 0; i < 4; ++i) {
        const unsigned nm = pk2(-xhalf_max(max16(s4[i])), 0.0f);
        m[i] = -bflo(nm);
        if (h) q1[i][2] = nm;
      }
      sa = s4[0] - m[0];
    }
    STAMP(1, 3);
#pragma unroll 1
    for (int it = 0; it < 8; ++it) {
      const u32x4* kn = kp + ((2 * it + 2) & 15) * 256;
      KTILE(e, d, kp + (2 * it + 1) * 256, kn)
      KTILE(d, e, kn, kn + 256)
    }
    p1[2] = pk2(e0, e1); p1[3] = pk2(e2, e3);
    o[3] = mfma(dv1, p1, o[3]);
    STAMP(1, 11);
    if (__builtin_expect(__ballot(h && !(o[0][12] + o[1][12] + o[2][12] + o[3][12] < 1.0e12f)) != 0ull, 0)) {
#pragma unroll
      for (int u = 0; u < 4; ++u) {
        f32x16 oo = {}; float mm = -3.0e38f;
        if (h) q1[u][2] = 0;
#pragma unroll 1
        for (int t = 0; t < 16; ++t) {
          const u32x4* p = kp + t * 256;
          f32x16 s = mfma(p[0], q0[u], f32x16{}); s = mfma(p[64], q1[u], s);
          float mn = fmaxf(mm, xhalf_max(max16(s)));
          float al = __builtin_amdgcn_exp2f(mm - mn);
          mm = mn;
#pragma unroll
          for (int i = 0; i < 16; ++i) { oo[i] *= al; s[i] = __builtin_amdgcn_exp2f(s[i] - mn); }
          oo = mfma(p[128], packs<0>(s), oo); oo = mfma(p[192], packs<1>(s), oo);
        }
        o[u] = oo; m[u] = mm;
      }
    }
#pragma unroll
    for (int u = 1; u < 4; ++u) {
      if (h) o[u][13] = m[u];
      f32x4* ex = (f32x4*)(lds + (((w + u) & 3) * 3 + u - 1) * 4096) + lane;
#pragma unroll
      for (int i = 0; i < 4; ++i) ex[i * 64] = f32x4{o[u][4 * i], o[u][4 * i + 1], o[u][4 * i + 2], o[u][4 * i + 3]};
    }
    const float l0 = xhalf_max(h ? o[0][12] : -1.0f);
    RAW_BARRIER();
    STAMP(1, 12);
    const char* ey = lds + w * 12288;
    f32x2 st[3];
#pragma unroll
    for (int u = 0; u < 3; ++u) st[u] = *(const f32x2*)(ey + u * 4096 + 3584 + l31 * 16);
    f32x4 y[12];
#pragma unroll
    for (int i = 0; i < 12; ++i) y[i] = *(const f32x4*)(ey + i * 1024 + lane * 16);
    SB();
    const float mm = fmaxf(fmaxf(st[0][1], st[1][1]), fmaxf(st[2][1], m[0]));
    float f0 = __builtin_amdgcn_exp2f(m[0] - mm), f[3];
#pragma unroll
    for (int u = 0; u < 3; ++u) f[u] = __builtin_amdgcn_exp2f(st[u][1] - mm);
    const float inv = __builtin_amdgcn_rcpf(l0 * f0 + st[0][0] * f[0] + st[1][0] * f[1] + st[2][0] * f[2]);
    f0 *= inv;
    f32x16 r = o[0] * f0;
    asm volatile("" : "+v"(r) : "v"(f0));
#pragma unroll
    for (int u = 0; u < 3; ++u) {
      const float fu = f[u] * inv;
#pragma unroll
      for (int i = 0; i < 4; ++i) {
        const f32x4 v = y[u * 4 + i];
        r[4 * i] += v[0] * fu; r[4 * i + 1] += v[1] * fu; r[4 * i + 2] += v[2] * fu; r[4 * i + 3] += v[3] * fu;
      }
    }
    u32x4 r1 = packs<1>(r);
    if (h) { r1[2] = 0; r1[3] = 0; }
    u32x4* op = Of + (((qt + w) * 16 + head) * 2) * 64 + lane;
    st128(op, packs<0>(r)); st128(op + 64, r1);
  } else if (idx < 29) {
    const int jq = xcd * 2 + w - 4, col = idx * 8 + (l31 >> 2), kb = col_bin(col), jr = 4 * jq + (lane & 3) + 512 * h;
    const unsigned toff = (4 * h * 16 + jq) * 928 + idx * 32 + l31;
    const bool cj = kb > 224;
    const int of = jr * 450 + 2 * kb, om = (2048 - jr) * 450 + 2 * (448 - kb), om0 = jr ? om : om - 921600;
    const float sr = col > 224 ? 0.0f : ((col == 0 || col == 224) ? 0.5f : 1.0f), si = cj ? -sr : sr;
    u32x4 mt[14];
    unsigned ta[16], ur[16], ui[16]; float wr[16], wi[16];
#pragma unroll
    for (int i = 0; i < 16; ++i) ta[i] = ald32(T + (8 * (i >> 2) + (i & 3)) * 14848, toff * 4);
#pragma unroll
    for (int g = 0; g < 16; ++g) {
      const unsigned o_ = g ? (cj ? om : of) + ((g & 7) + 16 * (g >> 3)) * (cj ? -28800 : 28800) : (cj ? om0 : of);
      ur[g] = ald16(Wr, o_); ui[g] = ald16(Wi, o_);
    }
    const unsigned* tabp = g_tabs.b2;
    asm volatile("" : "+s"(tabp));
#pragma unroll
    for (int t = 0; t < 14; ++t) mt[t] = ald128(tabp + t * 256, lane * 16);
    asm volatile("s_waitcnt vmcnt(0)" : "+v"(mt[0]), "+v"(mt[1]), "+v"(mt[2]), "+v"(mt[3]), "+v"(mt[4]), "+v"(mt[5]), "+v"(mt[6]), "+v"(mt[7]), "+v"(mt[8]),
                 "+v"(mt[9]), "+v"(mt[10]), "+v"(mt[11]), "+v"(mt[12]), "+v"(mt[13]) :: "memory");
    PIN16(ta); PIN16(ur); PIN16(ui);
#pragma unroll
    for (int g = 0; g < 16; ++g) { wr[g] = bflo(ur[g]); wi[g] = bflo(ui[g]); }
    SB();
    f32x16 xr = {}, xi = {};
#pragma unroll
    for (int t = 0; t < 4; ++t) {
      u32x4 b = {ta[4 * t], ta[4 * t + 1], ta[4 * t + 2], ta[4 * t + 3]};
      xr = mfma(mt[t], b, xr); xi = mfma(mt[4 + t], b, xi);
    }
    f32x16 yr, yi;
#pragma unroll
    for (int g = 0; g < 16; g += 2) {
      f32x2 c = {wr[g], wr[g + 1]}, d = {wi[g], wi[g + 1]};
      asm volatile("" : "+v"(c), "+v"(d));
      c *= sr; d *= si;
      const f32x2 a = {xr[g], xr[g + 1]}, b = {xi[g], xi[g + 1]}, r = a * c - b * d, i = a * d + b * c;
      yr[g] = r[0]; yr[g + 1] = r[1]; yi[g] = i[0]; yi[g + 1] = i[1];
    }
    u32x4 r0 = packs<0>(yr), r1 = packs<1>(yr), i0 = packs<0>(yi), i1 = packs<1>(yi);
    f32x16 ure = {}, uim = {};
    ure = mfma(mt[8], r0, ure); uim = mfma(mt[8], i0, uim);
    ure = mfma(mt[12], i0, ure); uim = mfma(mt[10], r0, uim);
    ure = mfma(mt[9], r1, ure); uim = mfma(mt[9], i1, uim);
    ure = mfma(mt[13], i1, ure); uim = mfma(mt[11], r1, uim);
#pragma unroll
    for (int g = 0; g < 16; ++g) ast32(U + ((g & 3) + 8 * (g >> 2)) * 14848, toff * 4, pk2(ure[g], uim[g]));
    {
      const unsigned off = ((idx * 2 + w - 4) * 64 + lane) * 128;
      if (off < 401408u) { unsigned pf = *(const unsigned*)((const char*)wo + off); asm volatile("" ::"v"(pf)); }
    }
  }
  STAMP(1, 14); STAMP(1, 15);
}

constexpr int ZS = 68;
constexpr int K3_LDS = 77 * 1024;

__global__ __launch_bounds__(512, 4) void k3_kernel(const u32x4* __restrict__ Of, const u16* __restrict__ wo, const u32x4* __restrict__ U,
                                                    u16* __restrict__ out) {
  extern __shared__ __attribute__((aligned(16))) char lds[];
  const int tid = threadIdx.x, lane = tid & 63, w = __builtin_amdgcn_readfirstlane(tid >> 6);
  const int xcd = blockIdx.x & 7, idx = blockIdx.x >> 3;
  const int l31 = lane & 31, h = lane >> 5;
  STAMP_INIT; STAMP(2, 0); STAMP(2, 1);
  if (idx < 4 || idx >= 12) {
    if (w >= 4) return;
    const int u = idx < 4 ? idx : idx - 8, tq = xcd * 4 + u / 7, tn = u % 7;
    const int qi = w & 1, kh = w >> 1;
    u32x4 of[16];
    {
      const int r8 = lane >> 3;
      const unsigned so = (r8 * 448 + ((lane & 7) ^ ((r8 & 2) << 1)) * 8) * 2, l16 = lane * 16;
      const u16* src = wo + (kh * 224 + qi * 8) * 448 + tn * 64;
      const unsigned d = ldsaddr_of(lds) + (kh * 224 + qi * 8) * 128;
      const u32x4* op = Of + (((tq * 2 + qi) * 16 + kh * 8) * 2) * 64;
#pragma unroll
      for (int st = 0; st < 2; ++st) {
#pragma unroll
        for (int i = 0; i < 7; ++i) dma16(src + (st * 7 + i) * 16 * 448, so, d + (st * 7 + i) * 2048);
#pragma unroll
        for (int i = 0; i < 8; ++i) of[st * 8 + i] = ald128(op + (st * 8 + i) * 64, l16);
      }
    }
    const int q = (lane >> 2) & 3, p = lane & 3, pc = ((p & 1) << 1) | (p >> 1);
    const int colb = ((lane >> 4) & 1) * 32 + pc * 8;
    const int base = (kh * 224 + q) * 128 + (colb ^ (((q >> 1) ^ kh) << 6));
    f32x16 a0 = {}, a1 = {};
    STAMP(2, 2);
#pragma unroll
    for (int st = 0; st < 2; ++st) {
      if (st == 0) asm volatile("s_waitcnt vmcnt(15)" : "+v"(of[0]), "+v"(of[1]), "+v"(of[2]), "+v"(of[3]), "+v"(of[4]), "+v"(of[5]), "+v"(of[6]), "+v"(of[7]) :: "memory");
      else asm volatile("s_waitcnt vmcnt(0)" : "+v"(of[8]), "+v"(of[9]), "+v"(of[10]), "+v"(of[11]), "+v"(of[12]), "+v"(of[13]), "+v"(of[14]), "+v"(of[15]) :: "memory");
      RAW_BARRIER();
      if (st == 0) STAMP(2, 3); else STAMP(2, 4);
      u32x4 fx[3], fy[3];
#pragma unroll
      for (int n = 0; n < 10; ++n) {
        if (n < 8) {
          const int i = st * 8 + n, hd = i >> 1, s = i & 1;
          const char* pa = lds + (hd * 28 + 16 * s) * 128;
          const int ra = base + h * 512, rb = (s ? base : base + h * 512) + 1024;
          u32x2 x0 = ldtr(pa + ra), x1 = ldtr(pa + rb), y0 = ldtr(pa + (ra ^ 64)), y1 = ldtr(pa + (rb ^ 64));
          fx[n % 3] = u32x4{x0[0], x0[1], x1[0], x1[1]}; fy[n % 3] = u32x4{y0[0], y0[1], y1[0], y1[1]};
        }
        SB();
        if (n >= 2) { a0 = mfma(fx[(n - 2) % 3], of[st * 8 + n - 2], a0); a1 = mfma(fy[(n - 2) % 3], of[st * 8 + n - 2], a1); }
        SB();
      }
    }
    STAMP(2, 5);
    f32x4* ex = (f32x4*)(lds + 56 * 1024);
    f32x16 snd = a1, kp = a0;
#pragma unroll
    for (int i = 0; i < 4; ++i) ex[(w * 4 + i) * 64 + lane] = f32x4{snd[4 * i], snd[4 * i + 1], snd[4 * i + 2], snd[4 * i + 3]};
    RAW_BARRIER();
#pragma unroll
    for (int i = 0; i < 4; ++i) {
      f32x4 v = ex[((w ^ 2) * 4 + i) * 64 + lane];
      kp[4 * i] += v[0]; kp[4 * i + 1] += v[1]; kp[4 * i + 2] += v[2]; kp[4 * i + 3] += v[3];
    }
    u16* dst = out + ((tq * 2 + qi) * 32 + l31) * 896 + tn * 64 + kh * 32 + 8 * h;
    *(u32x4*)dst = packs<0>(kp); *(u32x4*)(dst + 16) = packs<1>(kp);
  } else {
    const int b3 = xcd * 8 + idx - 4, s2 = b3 >> 1, sh = b3 & 1, r = w;
    u32x4* Dm = (u32x4*)lds;
    u32x4* F64 = (u32x4*)(lds + 61 * 1024);
    float* zs = (float*)lds;
    u32x4 ua[8], ub[8];
    if (w < 4) {
      const u32x4* up = U + s2 * 16 * 232 + (r ? 2 * r - 1 : 0) * 32;
      const unsigned uo = (h * 232 + sw23(l31)) * 16, uo2 = r ? uo + 512 : (h * 232 + 224 + (sw23(l31) & 7)) * 16;
#pragma unroll
      for (int i = 0; i < 8; ++i) { ua[i] = ald128(up + i * 464, uo); ub[i] = ald128(up + i * 464, uo2); }
    } else {
      const unsigned pl = (h * 32 + sw23(l31)) * 16;
#pragma unroll
      for (int i = 0; i < 4; ++i) dma16(g_tabs.f64 + ((w & 3) * 4 + i) * 256, pl, ldsaddr_of(lds) + 61 * 1024 + ((w & 3) * 4 + i) * 1024);
    }
    SB();
    {
      const int s = s2 + 32 * (32 * sh + l31);
      const float NRM2 = 2.0f / (2048.0f * 448.0f);
      u32x4 dr, di;
#pragma unroll
      for (int i = 0; i < 4; ++i) {
        int n = __mul24(8 * w + 4 * h + i, s) & 2047;
        float c = cosr(n, 1.0f / 2048) * NRM2, sn = sinr(n, 1.0f / 2048) * NRM2;
        dr[i] = pk2(c, -sn); di[i] = pk2(sn, c);
      }
      Dm[(w * 2) * 64 + lane] = dr; Dm[(w * 2 + 1) * 64 + lane] = di;
    }
    STAMP(2, 2);
    RAW_BARRIER();
    STAMP(2, 3);
    if (w < 4) {
      f32x16 ar = {}, ai = {}, br = {}, bi = {};
#pragma unroll
      for (int t = 0; t < 8; ++t) {
        u32x4 dr = Dm[(t * 2) * 64 + lane], di = Dm[(t * 2 + 1) * 64 + lane];
        asm volatile("s_waitcnt vmcnt(%2)" : "+v"(ua[t]), "+v"(ub[t]) : "n"(14 - 2 * t) : "memory");
        ar = mfma(ua[t], dr, ar); ai = mfma(ua[t], di, ai); br = mfma(ub[t], dr, br); bi = mfma(ub[t], di, bi);
      }
      STAMP(2, 4);
      RAW_BARRIER();
      u32x4 yr[4] = {packs<0>(ar), packs<1>(ar), packs<0>(br), packs<1>(br)};
      u32x4 yi[4] = {packs<0>(ai), packs<1>(ai), packs<0>(bi), packs<1>(bi)};
      const unsigned k2 = r ? ~0u : 0u, k1 = (r || !h) ? ~0u : 0u;
      yr[2] &= k1; yr[3] &= k2; yi[2] &= k2; yi[3] &= k2;
      float* zp = zs + ((r ? 2 * r - 1 : 0) * 32 + l31) * ZS + 8 * h;
      float* zq = zp + (r ? 32 * ZS : 0);
      f32x16 zre[2] = {}, zim[2] = {};
      u32x4 C = F64[lane], S = F64[64 + lane];
#pragma unroll
      for (int n = 0; n < 12; ++n) {
        const int at = n >> 2, t = n & 3;
        if (n < 8) {
          const u32x4 Cn = F64[((n + 1) & 7) * 128 + lane], Sn = F64[((n + 1) & 7) * 128 + 64 + lane];
          SB();
          zim[at] = mfma(C, yi[t], zim[at]); zre[at] = mfma(C, yr[t], zre[at]);
          zim[at] = mfma(S, yr[t], zim[at]); zre[at] = mfma(S ^ 0x80008000u, yi[t], zre[at]);
          C = Cn; S = Sn;
          SB();
        }
        if (n >= 4) {
          const int off = (at - 1) * 32 + 16 * (t >> 1) + 4 * (t & 1);
          *(f32x4*)(zq + off) = f32x4{zim[at - 1][4 * t], zim[at - 1][4 * t + 1], zim[at - 1][4 * t + 2], zim[at - 1][4 * t + 3]};
          *(f32x4*)(zp + off) = f32x4{zre[at - 1][4 * t], zre[at - 1][4 * t + 1], zre[at - 1][4 * t + 2], zre[at - 1][4 * t + 3]};
        }
      }
      STAMP(2, 5);
    } else {
      VMCNT(0);
      RAW_BARRIER();
    }
    RAW_BARRIER();
    STAMP(2, 6);
    {
      const int o = tid & 15, row = tid >> 4, rot = ((o >> 1) + 4 * (o & 1)) % 7;
      float* zp = zs + row * ZS + 4 * o;
      f32x4 zl[7];
#pragma unroll
      for (int c = 0; c < 7; ++c) zl[c] = *(const f32x4*)(zp + c * 32 * ZS);
      float y[4][7];
#pragma unroll
      for (int jp = 0; jp < 2; ++jp) {
        f32x2 z[7];
#pragma unroll
        for (int c = 0; c < 7; ++c) z[c] = f32x2{zl[c][2 * jp], zl[c][2 * jp + 1]};
        f32x2 y0 = z[0] + z[1] + z[3] + z[5];
        f32x2 e1 = z[0] + z[1] * C71 + z[3] * C72 + z[5] * C73, o1 = z[2] * S71 + z[4] * S72 + z[6] * S73;
        f32x2 e2 = z[0] + z[1] * C72 + z[3] * C73 + z[5] * C71, o2 = z[2] * S72 - z[4] * S73 - z[6] * S71;
        f32x2 e3 = z[0] + z[1] * C73 + z[3] * C71 + z[5] * C72, o3 = z[2] * S73 - z[4] * S71 + z[6] * S72;
        f32x2 yy[7] = {y0, e1 - o1, e2 - o2, e3 - o3, e3 + o3, e2 + o2, e1 + o1};
#pragma unroll
        for (int b = 0; b < 7; ++b) { y[2 * jp][b] = yy[b][0]; y[2 * jp + 1][b] = yy[b][1]; }
      }
#pragma unroll
      for (int b = 0; b < 7; ++b) {
        int m = b - rot; m += m < 0 ? 7 : 0;
        *(u32x2*)(zp + m * 32 * ZS) = u32x2{pk2(y[0][b], y[1][(b + 1) % 7]), pk2(y[2][(b + 2) % 7], y[3][(b + 3) % 7])};
      }
      u16* dst = out + (s2 + 32 * (32 * sh + row)) * 896 + 448 + 4 * o;
#pragma unroll
      for (int m = 0; m < 7; ++m) *(u32x2*)(dst + 64 * m) = *(const u32x2*)(zp + m * 32 * ZS);
    }
  }
  STAMP(2, 12); STAMP(2, 15);
}

extern "C" void kernel_launch(void* const* d_in, const int* in_sizes, int n_in, void* d_out, int out_size, void* d_ws, size_t ws_size,
                              hipStream_t stream) {
  const u16* ax = (const u16*)d_in[0];
  const u16* wqkv = (const u16*)d_in[1];
  const u16* wo = (const u16*)d_in[2];
  const u16* bx = (const u16*)d_in[3];
  const u16* wr = (const u16*)d_in[4];
  const u16* wi = (const u16*)d_in[5];
  char* ws = (char*)d_ws;
  u32x4* Qf = (u32x4*)(ws);
  u32x4* KVf = (u32x4*)(ws + (2u << 20));
  u32x4* Of = (u32x4*)(ws + (6u << 20));
  unsigned* T = (unsigned*)(ws + (8u << 20));
  unsigned* U = (unsigned*)(ws + (10u << 20));
  (void)hipFuncSetAttribute((const void*)k1_kernel, hipFuncAttributeMaxDynamicSharedMemorySize, K1_LDS);
  (void)hipFuncSetAttribute((const void*)k2_kernel, hipFuncAttributeMaxDynamicSharedMemorySize, K2_LDS);
  (void)hipFuncSetAttribute((const void*)k3_kernel, hipFuncAttributeMaxDynamicSharedMemorySize, K3_LDS);
  k1_kernel<<<256, 512, K1_LDS, stream>>>(ax, wqkv, bx, Qf, KVf, (u32x4*)T, wr, wi, wo, (const u16*)d_out);
  k2_kernel<<<256, 384, K2_LDS, stream>>>(Qf, KVf, Of, T, U, wr, wi, wo);
  k3_kernel<<<288, 512, K3_LDS, stream>>>(Of, wo, (const u32x4*)U, (u16*)d_out);
}
```

```cpp
#include <hip/hip_runtime.h>
#include <hip/hip_bf16.h>

typedef __attribute__((ext_vector_type(8))) __bf16 bf16x8;
typedef __attribute__((ext_vector_type(4))) __bf16 bf16x4;
typedef __attribute__((ext_vector_type(2))) __bf16 bf16x2;
typedef __attribute__((ext_vector_type(4))) short s16x4;
typedef __attribute__((ext_vector_type(16))) float f32x16;
typedef __attribute__((ext_vector_type(4))) float f32x4;
typedef __attribute__((ext_vector_type(2))) float f32x2;
typedef __attribute__((ext_vector_type(4))) unsigned u32x4;
typedef __attribute__((ext_vector_type(2))) unsigned u32x2;
typedef unsigned short u16;
#define LDSP __attribute__((address_space(3)))
#define DEV __device__ __forceinline__

DEV unsigned pk2(float lo, float hi) {
  f32x2 v = {lo, hi};
  return __builtin_bit_cast(unsigned, __builtin_convertvector(v, bf16x2));
}
DEV f32x16 mfma(u32x4 a, u32x4 b, f32x16 c) {
  return __builtin_amdgcn_mfma_f32_32x32x16_bf16(__builtin_bit_cast(bf16x8, a), __builtin_bit_cast(bf16x8, b), c, 0, 0, 0);
}
template <int S> DEV u32x4 packs(const f32x16& v) {
  u32x4 r = {pk2(v[8 * S + 0], v[8 * S + 1]), pk2(v[8 * S + 2], v[8 * S + 3]), pk2(v[8 * S + 4], v[8 * S + 5]), pk2(v[8 * S + 6], v[8 * S + 7])};
  return r;
}
template <int S> DEV u32x4 packsn(const f32x16& v) {
  u32x4 r = {pk2(-v[8 * S + 0], -v[8 * S + 1]), pk2(-v[8 * S + 2], -v[8 * S + 3]), pk2(-v[8 * S + 4], -v[8 * S + 5]), pk2(-v[8 * S + 6], -v[8 * S + 7])};
  return r;
}
DEV int sw23(int r) { return (r & ~12) | ((r & 4) << 1) | ((r & 8) >> 1); }
DEV float cosr(int n, float inv) { return __builtin_amdgcn_cosf((float)n * inv); }
DEV float sinr(int n, float inv) { return __builtin_amdgcn_sinf((float)n * inv); }
DEV void glds16(const void* g, char* l) {
  __builtin_amdgcn_global_load_lds((const unsigned*)g, (LDSP unsigned*)l, 16, 0, 0);
}
DEV void dma16(const void* base, unsigned voff, unsigned ldsaddr) {
  asm volatile("s_mov_b32 m0, %2\n\ts_nop 0\n\tglobal_load_lds_dwordx4 %0, %1" ::"v"(voff), "s"(base), "s"(ldsaddr) : "memory");
}
DEV unsigned ldsaddr_of(const char* p) { return (unsigned)(unsigned long long)(const LDSP char*)p; }
DEV u32x4 ald128(const void* base, unsigned off) {
  u32x4 r; asm volatile("global_load_dwordx4 %0, %1, %2" : "=v"(r) : "v"(off), "s"(base) : "memory"); return r;
}
DEV void st128(void* p, u32x4 v) { asm volatile("global_store_dwordx4 %0, %1, off sc1\n\ts_nop 1" ::"v"(p), "v"(v) : "memory"); }
#define SB() __builtin_amdgcn_sched_barrier(0)
DEV u32x2 ldtr(const char* l) {
  return __builtin_bit_cast(u32x2, __builtin_amdgcn_ds_read_tr16_b64_v4i16((LDSP s16x4*)l));
}
DEV float bflo(unsigned u) { return __builtin_bit_cast(float, u << 16); }
DEV float bfhi(unsigned u) { return __builtin_bit_cast(float, u & 0xffff0000u); }
#define RAW_BARRIER() do { asm volatile("s_waitcnt lgkmcnt(0)" ::: "memory"); __builtin_amdgcn_s_barrier(); asm volatile("" ::: "memory"); } while (0)
#define VMCNT(n) asm volatile("s_waitcnt vmcnt(" #n ")" ::: "memory")

#define STAMP_INIT
#define STAMP(k, i)
constexpr float C71 = 0.62348980185873353f, C72 = -0.22252093395631440f, C73 = -0.90096886790241913f;
constexpr float S71 = 0.78183148246802981f, S72 = 0.97492791218182361f, S73 = 0.43388373911755812f;

DEV void gen_cs(int i, int k0, int mul, int add, int mask, float inv, u32x4& C, u32x4& S) {
  float c[8], s[8];
#pragma unroll
  for (int j = 0; j < 8; ++j) {
    int n = (__mul24(i, mul * k0 + add) + __mul24(i, mul) * j) & mask;
    c[j] = cosr(n, inv); s[j] = sinr(n, inv);
  }
#pragma unroll
  for (int j = 0; j < 4; ++j) { C[j] = pk2(c[2 * j], c[2 * j + 1]); S[j] = pk2(s[2 * j], s[2 * j + 1]); }
}

constexpr double ccos64(int n) {
  n &= 63; if (n > 32) n = 64 - n;
  double sg = 1.0; if (n > 16) { n = 32 - n; sg = -1.0; }
  if (n == 16) return 0.0;
  double x = 6.283185307179586476925 * n / 64.0, x2 = x * x, term = 1.0, sum = 1.0;
  for (int i = 1; i < 14; ++i) { term *= -x2 / ((2 * i - 1) * (2 * i)); sum += term; }
  return sg * sum;
}
constexpr unsigned cbf(double v) { unsigned u = __builtin_bit_cast(unsigned, (float)v); return (u + 0x7fffu + ((u >> 16) & 1u)) >> 16; }
constexpr int csw(int r) { return (r & ~12) | ((r & 4) << 1) | ((r & 8) >> 1); }
struct alignas(16) Tabs { unsigned f64[16 * 256]; unsigned b2[14 * 256]; };
constexpr Tabs make_tabs() {
  Tabs t{};
  unsigned c[64] = {}, sn[64] = {};
  for (int n = 0; n < 64; ++n) { c[n] = cbf(ccos64(n)); sn[n] = cbf(ccos64(n - 16 + 64)); }
  for (int f = 0; f < 8; ++f)
    for (int l = 0; l < 64; ++l)
      for (int d = 0; d < 4; ++d) {
        int i = 32 * (f >> 2) + (l & 31), k = 16 * (f & 3) + 8 * (l >> 5) + 2 * d;
        t.f64[((f * 2) * 64 + l) * 4 + d] = c[(i * k) & 63] | (c[(i * (k + 1)) & 63] << 16);
        t.f64[((f * 2 + 1) * 64 + l) * 4 + d] = sn[(i * k) & 63] | (sn[(i * (k + 1)) & 63] << 16);
      }
  for (int l = 0; l < 64; ++l)
    for (int d = 0; d < 4; ++d) {
      for (int f = 0; f < 4; ++f) {
        int n = (2 * csw(l & 31) * (8 * f + 4 * (l >> 5) + d)) & 63;
        t.b2[(f * 64 + l) * 4 + d] = c[n] | (sn[n] << 16);
        t.b2[((4 + f) * 64 + l) * 4 + d] = (sn[n] ^ 0x8000u) | (c[n] << 16);
      }
      for (int f = 0; f < 2; ++f) {
        int k = 16 * f + 8 * (l >> 5) + 2 * d, n0 = (2 * (l & 31) * k) & 63, n1 = (2 * (l & 31) * (k + 1)) & 63;
        t.b2[((8 + f) * 64 + l) * 4 + d] = c[n0] | (c[n1] << 16);
        t.b2[((10 + f) * 64 + l) * 4 + d] = sn[n0] | (sn[n1] << 16);
        t.b2[((12 + f) * 64 + l) * 4 + d] = (sn[n0] | (sn[n1] << 16)) ^ 0x80008000u;
      }
    }
  return t;
}
__device__ const Tabs g_tabs = make_tabs();
DEV unsigned ald32(const void* base, unsigned off) {
  unsigned r; asm volatile("global_load_dword %0, %1, %2" : "=v"(r) : "v"(off), "s"(base) : "memory"); return r;
}
DEV unsigned ald16(const void* base, unsigned off) {
  unsigned r; asm volatile("global_load_ushort %0, %1, %2" : "=v"(r) : "v"(off), "s"(base) : "memory"); return r;
}
DEV void ast32(void* base, unsigned off, unsigned v) { asm volatile("global_store_dword %0, %1, %2 sc1" ::"v"(off), "v"(v), "s"(base) : "memory"); }
DEV int col_bin(int c) { int ct = c >> 5, k = ct == 7 ? 7 * (c - 192) : 7 * ((ct ? ((ct + 1) & 1) * 32 : 0) + (c & 31)) + 64 * ((ct + 1) >> 1); return k >= 448 ? k - 448 : k; }

constexpr int K1_SLOT = 32 * 1024;
constexpr int K1_LDS = 128 * 1024;
constexpr unsigned CODE_BYTES = 26000;
struct QF { u32x4 x0[4], x1[4], w[4]; };

template <bool ISV>
DEV void qkv_loop(const char* lds, unsigned ld, const u16* xb, const u16* wb, int kn, unsigned xo, unsigned wo0, unsigned wo1,
                  int xoff, int woff, f32x16& a0, f32x16& a1) {
  auto piece = [&](int slot, int i) {
    const unsigned d = ld + slot * K1_SLOT;
    if (i == 0) dma16(xb + kn * 64, xo, d);
    if (i == 1) dma16(wb + kn * (64 * 1344), wo0, d + 16384);
    if (i == 2) dma16(xb + kn * 64 + 64 * 448, xo, d + 8192);
    if (i == 3) { dma16(wb + kn * (64 * 1344), wo1, d + 24576); kn = kn == 6 ? 0 : kn + 1; }
  };
  auto rd = [&](QF& f, int slot, int ks) {
    const char* sl = lds + slot * K1_SLOT;
    f.x0[ks] = *(const u32x4*)(sl + (xoff ^ (ks * 32)));
    f.x1[ks] = *(const u32x4*)(sl + (xoff ^ (ks * 32)) + 4096);
    u32x2 wa = ldtr(sl + woff + ks * 16 * 224), wc = ldtr(sl + woff + ks * 16 * 224 + 224);
    f.w[ks] = u32x4{wa[0], wa[1], wc[0], wc[1]};
  };
#pragma unroll
  for (int i = 0; i < 8; ++i) piece(i >> 2, i & 3);
  QF f[2];
#pragma unroll
  for (int kc = 0; kc < 8; ++kc) {
    if (kc < 7) { if (kc < 6) VMCNT(4); else VMCNT(0); __builtin_amdgcn_s_barrier(); asm volatile("" ::: "memory"); }
#pragma unroll
    for (int ks = 0; ks < 4; ++ks) {
      if (kc < 7) rd(f[kc & 1], kc & 3, ks);
      if (kc > 0) {
        QF& g = f[(kc - 1) & 1];
        if (ISV) { a0 = mfma(g.x0[ks], g.w[ks], a0); a1 = mfma(g.x1[ks], g.w[ks], a1); }
        else     { a0 = mfma(g.w[ks], g.x0[ks], a0); a1 = mfma(g.w[ks], g.x1[ks], a1); }
      }
      if (kc < 5) piece((kc + 2) & 3, ks);
      SB();
    }
  }
}

__global__ __launch_bounds__(512, 2) void k1_kernel(const u16* __restrict__ ax, const u16* __restrict__ wqkv, const u16* __restrict__ bx,
                                                    u32x4* __restrict__ Qf, u32x4* __restrict__ KVf, u32x4* __restrict__ T) {
  extern __shared__ __attribute__((aligned(16))) char lds[];
  const int tid = threadIdx.x, lane = tid & 63, w = __builtin_amdgcn_readfirstlane(tid >> 6);
  const int xcd = blockIdx.x & 7, idx = blockIdx.x >> 3;
  const int l31 = lane & 31, h = lane >> 5;
  const unsigned lb = ldsaddr_of(lds);
  unsigned long long pc0;
  asm volatile("s_getpc_b64 %0" : "=s"(pc0));
  const char* pc = (const char*)(pc0 & ~127ull);
  STAMP_INIT; STAMP(0, 0); STAMP(0, 1);
  if (idx >= 8) {
    const int u = idx - 8, which = u >> 3, tm = (xcd & 3) * 4 + (u & 3), g = (xcd >> 2) * 2 + ((u >> 2) & 1);
    const int row = 8 * w + (lane >> 3);
    const unsigned xo = (row * 448 + ((lane & 7) ^ ((row >> 1) & 7)) * 8) * 2;
    unsigned wo2[2];
#pragma unroll
    for (int i = 0; i < 2; ++i) {
      int q = 64 * (w + 8 * i) + lane; if (q > 895) q = 895;
      int rho = q / 14, cc = q - rho * 14;
      wo2[i] = (((rho & ~7) + ((rho & 7) >> 1) + 4 * (rho & 1)) * 1344 + cc * 8) * 2;
    }
    const int hh = w & 3, th = w >> 2, r0 = th * 64 + l31;
    const int xoff = r0 * 128 + ((h ^ ((r0 >> 1) & 7)) * 16);
    const int woff = 16384 + (8 * h + 2 * ((lane >> 2) & 3)) * 224 + (hh * 28 + ((lane >> 4) & 1) * 16 + 4 * (lane & 3)) * 2;
    const int head = g * 4 + hh, tile = tm * 4 + th * 2, kn = ((u & 3) * 2 + (u >> 2)) % 7;
    f32x16 a0 = {}, a1 = {};
    u32x4 o0, o1, o2, o3;
    if (which == 2) {
      qkv_loop<true>(lds, lb + w * 1024, ax + tm * 128 * 448, wqkv + 896 + g * 112, kn, xo, wo2[0], wo2[1], xoff, woff, a0, a1);
      o0 = packs<0>(a0); o1 = packs<1>(a0); o2 = packs<0>(a1); o3 = packs<1>(a1);
      if (l31 >= 28) { unsigned f = (l31 == 28) ? 0x3F803F80u : 0u; o0 = u32x4{f, f, f, f}; o1 = o0; o2 = o0; o3 = o0; }
    } else {
      qkv_loop<false>(lds, lb + w * 1024, ax + tm * 128 * 448, wqkv + which * 448 + g * 112, kn, xo, wo2[0], wo2[1], xoff, woff, a0, a1);
      const float sc = which ? 1.0f : 0.18898223650461362f * 1.4426950408889634f;
      a0 *= sc; a1 *= sc;
      o0 = packs<0>(a0); o1 = packs<1>(a0); o2 = packs<0>(a1); o3 = packs<1>(a1);
      if (h) { o1[2] = which ? 0x3F80u : 0u; o1[3] = 0; o3[2] = o1[2]; o3[3] = 0; }
    }
    STAMP(0, 7);
    u32x4* o = (which == 0 ? Qf + ((head * 64 + tile) * 2) * 64 : KVf + ((head * 64 + tile) * 4 + (which & 2)) * 64) + lane;
    const int st = which == 0 ? 128 : 256;
    st128(o, o0); st128(o + 64, o1); st128(o + st, o2); st128(o + st + 64, o3);
  } else {
    const int b1 = xcd * 8 + idx, s2 = b1 >> 1, hb = b1 & 1;
    u32x4* F64 = (u32x4*)(lds + 56 * 1024);
    u32x4* Ap = (u32x4*)(lds + 72 * 1024);
    u32x4* Xf = (u32x4*)(lds + 88 * 1024);
    {
      const int row = 8 * w + (lane >> 3);
      const unsigned xo = ((s2 + 32 * ((row & 32) + sw23(row & 31))) * 448 + ((lane & 7) ^ ((row >> 1) & 7)) * 8) * 2;
#pragma unroll
      for (int m = 0; m < 7; ++m) dma16(bx + 64 * m, xo, lb + m * 8192 + w * 1024);
      dma16(g_tabs.f64 + w * 512, lane * 16, lb + 56 * 1024 + w * 2048); dma16(g_tabs.f64 + w * 512 + 256, lane * 16, lb + 57 * 1024 + w * 2048);
    }
    SB();
    const int mt = w >> 2, o = 2 * (w & 3) + h;
    {
      u32x4 C, S;
      gen_cs(32 * mt + l31, 8 * o, 32, s2, 2047, 1.0f / 2048, C, S);
      Ap[(w * 2 + 0) * 64 + lane] = C; Ap[(w * 2 + 1) * 64 + lane] = S;
    }
    char* xp = lds + (32 * mt + l31) * 128 + ((o ^ ((l31 >> 1) & 7)) * 16);
    int xm[7];
#pragma unroll
    for (int b = 0; b < 7; ++b) { int m = b - o; m += m < 0 ? 7 : 0; m += m < 0 ? 7 : 0; xm[b] = m * 8192; asm volatile("" : "+v"(xm[b])); }
    STAMP(0, 2);
    VMCNT(2);
    RAW_BARRIER();
    STAMP(0, 3);
    {
      u32x4 xr[7], z[4];
#pragma unroll
      for (int b = 0; b < 7; ++b) xr[b] = *(const u32x4*)(xp + xm[b]);
      const float ca = hb ? C72 : C71, cb = hb ? C73 : C72, cc = hb ? C71 : C73, sa = hb ? S72 : S71, sb = hb ? -S73 : S72, sc = hb ? -S71 : S73;
      const float da = hb ? C73 : 1.0f, db = hb ? C71 : 1.0f, dc = hb ? C72 : 1.0f, ta = hb ? S73 : 0.0f, tb = hb ? -S71 : 0.0f, tc = hb ? S72 : 0.0f;
#pragma unroll
      for (int jp = 0; jp < 4; ++jp) {
        f32x2 v[7];
#pragma unroll
        for (int b = 0; b < 7; ++b) {
          v[b][0] = bflo(xr[(b - 2 * jp + 14) % 7][jp]);
          v[b][1] = bfhi(xr[(b - 2 * jp - 1 + 14) % 7][jp]);
        }
        f32x2 p1 = v[1] + v[6], p2 = v[2] + v[5], p3 = v[3] + v[4];
        f32x2 m1 = v[6] - v[1], m2 = v[5] - v[2], m3 = v[4] - v[3];
        f32x2 ra = v[0] + p1 * ca + p2 * cb + p3 * cc, ia = m1 * sa + m2 * sb + m3 * sc;
        f32x2 rb = v[0] + p1 * da + p2 * db + p3 * dc, ib = m1 * ta + m2 * tb + m3 * tc;
        z[0][jp] = pk2(ra[0], ra[1]); z[1][jp] = pk2(ia[0], ia[1]); z[2][jp] = pk2(rb[0], rb[1]); z[3][jp] = pk2(ib[0], ib[1]);
      }
#pragma unroll
      for (int c = 0; c < 4; ++c) *(u32x4*)(xp + c * 8192) = z[c];
    }
    VMCNT(0);
    RAW_BARRIER();
    STAMP(0, 4);
    const int wc = w & 3, ct = hb ? 3 + wc : (wc == 2 ? 0 : (wc == 3 ? 7 : wc + 1)), kt = w & 1;
    u32x4 ap[8];
    {
      f32x16 xre = {}, xim = {};
      const char* zp = lds + (w & 2) * 8192 + mt * 4096 + l31 * 128;
      const int zsw = (h ^ ((l31 >> 1) & 7)) * 16;
      u32x4 C[4], S[4], zr[4], zi[4];
#pragma unroll
      for (int tt = 0; tt < 4; ++tt) {
        zr[tt] = *(const u32x4*)(zp + (zsw ^ (tt * 32))); C[tt] = F64[((kt * 4 + tt) * 2) * 64 + lane];
        zi[tt] = *(const u32x4*)(zp + 8192 + (zsw ^ (tt * 32))); S[tt] = F64[((kt * 4 + tt) * 2 + 1) * 64 + lane];
      }
#pragma unroll
      for (int tt = 0; tt < 8; ++tt) ap[tt] = Ap[(mt * 8 + tt) * 64 + lane];
      SB();
#pragma unroll
      for (int tt = 0; tt < 4; ++tt) {
        xre = mfma(zr[tt], C[tt], xre); xim = mfma(zi[tt], C[tt], xim);
        xre = mfma(zi[tt], S[tt], xre); xim = mfma(zr[tt], S[tt] ^ 0x80008000u, xim);
      }
      u32x4* xq = Xf + (wc * 4 + mt * 2) * 128 + lane;
      xq[0] = packs<0>(xre); xq[64] = packs<0>(xim); xq[128] = packs<1>(xre); xq[192] = packs<1>(xim);
    }
    RAW_BARRIER();
    STAMP(0, 5);
    const unsigned po = (idx * 64 + lane) * 128;
    unsigned pf = ald32(idx < 4 ? pc : (const char*)g_tabs.b2 - 32768, (w == 0 && po < (idx < 4 ? CODE_BYTES : 47104u)) ? po : (idx < 4 ? 0u : 32768u));
    {
      f32x16 tre = {}, tim = {};
      u32x4 bf[8];
#pragma unroll
      for (int tt = 0; tt < 8; ++tt) bf[tt] = Xf[(wc * 8 + tt) * 64 + lane];
      SB();
#pragma unroll
      for (int tt = 0; tt < 4; ++tt) {
        tre = mfma(ap[2 * tt], bf[2 * tt], tre); tim = mfma(ap[2 * tt], bf[2 * tt + 1], tim);
        tre = mfma(ap[2 * tt + 1], bf[2 * tt + 1], tre); tim = mfma(ap[2 * tt + 1] ^ 0x80008000u, bf[2 * tt], tim);
      }
      u32x4* tp = T + (s2 * 16 + mt * 8 + h) * 232 + ct * 32 + l31;
      if (ct < 7 || l31 < 8)
#pragma unroll
      for (int q = 0; q < 4; ++q)
        st128(tp + 2 * q * 232, u32x4{pk2(tre[4 * q], tim[4 * q]), pk2(tre[4 * q + 1], tim[4 * q + 1]), pk2(tre[4 * q + 2], tim[4 * q + 2]), pk2(tre[4 * q + 3], tim[4 * q + 3])});
    }
    asm volatile("s_waitcnt vmcnt(0)" : "+v"(pf) :: "memory");
  }
  STAMP(0, 12); STAMP(0, 15);
}

constexpr int K2_LDS = 84 * 1024;
DEV float xhalf_max(float v) {
  float a = v, b = v;
  asm volatile("s_nop 1\n\tv_permlane32_swap_b32 %0, %1\n\ts_nop 1" : "+v"(a), "+v"(b));
  return fmaxf(a, b);
}
DEV float max16(const f32x16& s) {
  float m = fmaxf(fmaxf(s[0], s[1]), s[2]);
#pragma unroll
  for (int i = 3; i < 15; i += 2) m = fmaxf(fmaxf(m, s[i]), s[i + 1]);
  return fmaxf(m, s[15]);
}
#define PIN16(a) asm volatile("" : "+v"(a[0]), "+v"(a[1]), "+v"(a[2]), "+v"(a[3]), "+v"(a[4]), "+v"(a[5]), "+v"(a[6]), "+v"(a[7]), \
                                   "+v"(a[8]), "+v"(a[9]), "+v"(a[10]), "+v"(a[11]), "+v"(a[12]), "+v"(a[13]), "+v"(a[14]), "+v"(a[15]))
#define EX(s, i) s[i] = __builtin_amdgcn_exp2f(s[i])
#define EX4(s, i, P, j)                                                                                          \
  { const float t0_ = __builtin_amdgcn_exp2f(s[i]); SB(); P[j] = pk2(e0, e1); SB();                               \
    const float t1_ = __builtin_amdgcn_exp2f(s[i + 1]); SB(); P[j + 1] = pk2(e2, e3); SB();                       \
    e0 = t0_; e1 = t1_; e2 = __builtin_amdgcn_exp2f(s[i + 2]); SB(); e3 = __builtin_amdgcn_exp2f(s[i + 3]); SB(); }
#define TILE(sc, sn, K0, K1, Q0, Q1, oc, V0, op, V1, L0, L1, L2, L3)                 \
  sn = mfma(K0, Q0, f32x16{}); SB(); L0; EX4(sc, 0, p1, 2)                           \
  op = mfma(V1, p1, op); SB(); L1; EX4(sc, 4, p0, 0)                                 \
  sn = mfma(K1, Q1, sn); SB(); L2; EX4(sc, 8, p0, 2)                                 \
  oc = mfma(V0, p0, oc); SB(); L3; EX4(sc, 12, p1, 0)
#define KTILE(X, Y, P1, P2)                                                                                      \
  TILE(sa, sb, X##k0, X##k1, q0[1], q1[1], o[0], X##v0, o[3], Y##v1, , Y##v1 = (P1)[192], , )                     \
  TILE(sb, sa, X##k0, X##k1, q0[2], q1[2], o[1], X##v0, o[0], X##v1, , , , )                                      \
  TILE(sa, sb, X##k0, X##k1, q0[3], q1[3], o[2], X##v0, o[1], X##v1, X##k0 = (P2)[0], , X##k1 = (P2)[64], )       \
  TILE(sb, sa, Y##k0, Y##k1, q0[0], q1[0], o[3], X##v0, o[2], X##v1, , , , X##v0 = (P2)[128])

__global__ __launch_bounds__(384, 2) void k2_kernel(const u32x4* __restrict__ Qf, const u32x4* __restrict__ KVf, u32x4* __restrict__ Of,
                                                    const unsigned* __restrict__ T, unsigned* __restrict__ U,
                                                    const u16* __restrict__ Wr, const u16* __restrict__ Wi, const u16* __restrict__ wo) {
  extern __shared__ __attribute__((aligned(16))) char lds[];
  const int tid = threadIdx.x, lane = tid & 63, w = __builtin_amdgcn_readfirstlane(tid >> 6);
  const int xcd = blockIdx.x & 7, idx = blockIdx.x >> 3;
  const int l31 = lane & 31, h = lane >> 5;
  STAMP_INIT; STAMP(1, 0); STAMP(1, 1);
  if (w < 4) {
    __builtin_amdgcn_s_setprio(2);
    const int head = xcd * 2 + (idx >> 4), qt = (idx & 15) * 4;
    const u32x4* kp = KVf + (head * 64 + w * 16) * 256 + lane;
    u32x4 q0[4], q1[4];
    u32x4 ek0 = kp[0], ek1 = kp[64];
#pragma unroll
    for (int i = 0; i < 4; ++i) { const u32x4* qp = Qf + (head * 64 + qt + ((w + i) & 3)) * 128 + lane; q0[i] = qp[0]; q1[i] = qp[64]; }
    u32x4 ev0 = kp[128], ev1 = kp[192], dk0 = kp[256], dk1 = kp[320], dv0 = kp[384], dv1 = {}, p0, p1 = {};
    f32x16 o[4] = {}, sa, sb;
    float m[4], e0 = 0.f, e1 = 0.f, e2 = 0.f, e3 = 0.f;
    {
      f32x16 s4[4];
#pragma unroll
      for (int i = 0; i < 4; ++i) { s4[i] = mfma(ek0, q0[i], f32x16{}); s4[i] = mfma(ek1, q1[i], s4[i]); }
#pragma unroll
      for (int i = 0; i < 4; ++i) {
        const unsigned nm = pk2(-xhalf_max(max16(s4[i])), 0.0f);
        m[i] = -bflo(nm);
        if (h) q1[i][2] = nm;
      }
      sa = s4[0] - m[0];
    }
    STAMP(1, 3);
#pragma unroll 1
    for (int it = 0; it < 8; ++it) {
      const u32x4* kn = kp + ((2 * it + 2) & 15) * 256;
      KTILE(e, d, kp + (2 * it + 1) * 256, kn)
      KTILE(d, e, kn, kn + 256)
    }
    p1[2] = pk2(e0, e1); p1[3] = pk2(e2, e3);
    o[3] = mfma(dv1, p1, o[3]);
    STAMP(1, 11);
    if (__builtin_expect(__ballot(h && !(o[0][12] + o[1][12] + o[2][12] + o[3][12] < 1.0e12f)) != 0ull, 0)) {
#pragma unroll
      for (int u = 0; u < 4; ++u) {
        f32x16 oo = {}; float mm = -3.0e38f;
        if (h) q1[u][2] = 0;
#pragma unroll 1
        for (int t = 0; t < 16; ++t) {
          const u32x4* p = kp + t * 256;
          f32x16 s = mfma(p[0], q0[u], f32x16{}); s = mfma(p[64], q1[u], s);
          float mn = fmaxf(mm, xhalf_max(max16(s)));
          float al = __builtin_amdgcn_exp2f(mm - mn);
          mm = mn;
#pragma unroll
          for (int i = 0; i < 16; ++i) { oo[i] *= al; s[i] = __builtin_amdgcn_exp2f(s[i] - mn); }
          oo = mfma(p[128], packs<0>(s), oo); oo = mfma(p[192], packs<1>(s), oo);
        }
        o[u] = oo; m[u] = mm;
      }
    }
#pragma unroll
    for (int u = 1; u < 4; ++u) {
      if (h) o[u][13] = m[u];
      f32x4* ex = (f32x4*)(lds + (((w + u) & 3) * 3 + u - 1) * 4096) + lane;
#pragma unroll
      for (int i = 0; i < 4; ++i) ex[i * 64] = f32x4{o[u][4 * i], o[u][4 * i + 1], o[u][4 * i + 2], o[u][4 * i + 3]};
    }
    const float l0 = xhalf_max(h ? o[0][12] : -1.0f);
    RAW_BARRIER();
    STAMP(1, 12);
    const char* ey = lds + w * 12288;
    f32x2 st[3];
#pragma unroll
    for (int u = 0; u < 3; ++u) st[u] = *(const f32x2*)(ey + u * 4096 + 3584 + l31 * 16);
    f32x4 y[12];
#pragma unroll
    for (int i = 0; i < 12; ++i) y[i] = *(const f32x4*)(ey + i * 1024 + lane * 16);
    SB();
    const float mm = fmaxf(fmaxf(st[0][1], st[1][1]), fmaxf(st[2][1], m[0]));
    float f0 = __builtin_amdgcn_exp2f(m[0] - mm), f[3];
#pragma unroll
    for (int u = 0; u < 3; ++u) f[u] = __builtin_amdgcn_exp2f(st[u][1] - mm);
    const float inv = __builtin_amdgcn_rcpf(l0 * f0 + st[0][0] * f[0] + st[1][0] * f[1] + st[2][0] * f[2]);
    f0 *= inv;
    f32x16 r = o[0] * f0;
    asm volatile("" : "+v"(r) : "v"(f0));
#pragma unroll
    for (int u = 0; u < 3; ++u) {
      const float fu = f[u] * inv;
#pragma unroll
      for (int i = 0; i < 4; ++i) {
        const f32x4 v = y[u * 4 + i];
        r[4 * i] += v[0] * fu; r[4 * i + 1] += v[1] * fu; r[4 * i + 2] += v[2] * fu; r[4 * i + 3] += v[3] * fu;
      }
    }
    u32x4 r1 = packs<1>(r);
    if (h) { r1[2] = 0; r1[3] = 0; }
    u32x4* op = Of + (((qt + w) * 16 + head) * 2) * 64 + lane;
    st128(op, packs<0>(r)); st128(op + 64, r1);
  } else if (idx < 29) {
    const int jq = xcd * 2 + w - 4, col = idx * 8 + (l31 >> 2), kb = col_bin(col), jr = 4 * jq + (lane & 3) + 512 * h;
    const unsigned toff = (4 * h * 16 + jq) * 928 + idx * 32 + l31;
    const bool cj = kb > 224;
    const int of = jr * 450 + 2 * kb, om = (2048 - jr) * 450 + 2 * (448 - kb), om0 = jr ? om : om - 921600;
    const float sr = col > 224 ? 0.0f : ((col == 0 || col == 224) ? 0.5f : 1.0f), si = cj ? -sr : sr;
    u32x4 mt[14];
    unsigned ta[16], ur[16], ui[16]; float wr[16], wi[16];
#pragma unroll
    for (int i = 0; i < 16; ++i) ta[i] = ald32(T + (8 * (i >> 2) + (i & 3)) * 14848, toff * 4);
#pragma unroll
    for (int g = 0; g < 16; ++g) {
      const unsigned o_ = g ? (cj ? om : of) + ((g & 7) + 16 * (g >> 3)) * (cj ? -28800 : 28800) : (cj ? om0 : of);
      ur[g] = ald16(Wr, o_); ui[g] = ald16(Wi, o_);
    }
    const unsigned* tabp = g_tabs.b2;
    asm volatile("" : "+s"(tabp));
#pragma unroll
    for (int t = 0; t < 14; ++t) mt[t] = ald128(tabp + t * 256, lane * 16);
    asm volatile("s_waitcnt vmcnt(0)" : "+v"(mt[0]), "+v"(mt[1]), "+v"(mt[2]), "+v"(mt[3]), "+v"(mt[4]), "+v"(mt[5]), "+v"(mt[6]), "+v"(mt[7]), "+v"(mt[8]),
                 "+v"(mt[9]), "+v"(mt[10]), "+v"(mt[11]), "+v"(mt[12]), "+v"(mt[13]) :: "memory");
    PIN16(ta); PIN16(ur); PIN16(ui);
#pragma unroll
    for (int g = 0; g < 16; ++g) { wr[g] = bflo(ur[g]); wi[g] = bflo(ui[g]); }
    SB();
    f32x16 xr = {}, xi = {};
#pragma unroll
    for (int t = 0; t < 4; ++t) {
      u32x4 b = {ta[4 * t], ta[4 * t + 1], ta[4 * t + 2], ta[4 * t + 3]};
      xr = mfma(mt[t], b, xr); xi = mfma(mt[4 + t], b, xi);
    }
    f32x16 yr, yi;
#pragma unroll
    for (int g = 0; g < 16; g += 2) {
      f32x2 c = {wr[g], wr[g + 1]}, d = {wi[g], wi[g + 1]};
      asm volatile("" : "+v"(c), "+v"(d));
      c *= sr; d *= si;
      const f32x2 a = {xr[g], xr[g + 1]}, b = {xi[g], xi[g + 1]}, r = a * c - b * d, i = a * d + b * c;
      yr[g] = r[0]; yr[g + 1] = r[1]; yi[g] = i[0]; yi[g + 1] = i[1];
    }
    u32x4 r0 = packs<0>(yr), r1 = packs<1>(yr), i0 = packs<0>(yi), i1 = packs<1>(yi);
    f32x16 ure = {}, uim = {};
    ure = mfma(mt[8], r0, ure); uim = mfma(mt[8], i0, uim);
    ure = mfma(mt[12], i0, ure); uim = mfma(mt[10], r0, uim);
    ure = mfma(mt[9], r1, ure); uim = mfma(mt[9], i1, uim);
    ure = mfma(mt[13], i1, ure); uim = mfma(mt[11], r1, uim);
#pragma unroll
    for (int g = 0; g < 16; ++g) ast32(U + ((g & 3) + 8 * (g >> 2)) * 14848, toff * 4, pk2(ure[g], uim[g]));
    {
      const unsigned off = ((idx * 2 + w - 4) * 64 + lane) * 128;
      if (off < 401408u) { unsigned pf = *(const unsigned*)((const char*)wo + off); asm volatile("" ::"v"(pf)); }
    }
  }
  STAMP(1, 14); STAMP(1, 15);
}

constexpr int ZS = 68;
constexpr int K3_LDS = 77 * 1024;

__global__ __launch_bounds__(512, 4) void k3_kernel(const u32x4* __restrict__ Of, const u16* __restrict__ wo, const u32x4* __restrict__ U,
                                                    u16* __restrict__ out) {
  extern __shared__ __attribute__((aligned(16))) char lds[];
  const int tid = threadIdx.x, lane = tid & 63, w = __builtin_amdgcn_readfirstlane(tid >> 6);
  const int xcd = blockIdx.x & 7, idx = blockIdx.x >> 3;
  const int l31 = lane & 31, h = lane >> 5;
  STAMP_INIT; STAMP(2, 0); STAMP(2, 1);
  if (idx < 4 || idx >= 12) {
    if (w >= 4) return;
    const int u = idx < 4 ? idx : idx - 8, tq = xcd * 4 + u / 7, tn = u % 7;
    const int qi = w & 1, kh = w >> 1;
    u32x4 of[16];
    {
      const int r8 = lane >> 3;
      const unsigned so = (r8 * 448 + ((lane & 7) ^ ((r8 & 2) << 1)) * 8) * 2, l16 = lane * 16;
      const u16* src = wo + (kh * 224 + qi * 8) * 448 + tn * 64;
      const unsigned d = ldsaddr_of(lds) + (kh * 224 + qi * 8) * 128;
      const u32x4* op = Of + (((tq * 2 + qi) * 16 + kh * 8) * 2) * 64;
#pragma unroll
      for (int st = 0; st < 2; ++st) {
#pragma unroll
        for (int i = 0; i < 7; ++i) dma16(src + (st * 7 + i) * 16 * 448, so, d + (st * 7 + i) * 2048);
#pragma unroll
        for (int i = 0; i < 8; ++i) of[st * 8 + i] = ald128(op + (st * 8 + i) * 64, l16);
      }
    }
    const int q = (lane >> 2) & 3, p = lane & 3, pc = ((p & 1) << 1) | (p >> 1);
    const int colb = ((lane >> 4) & 1) * 32 + pc * 8;
    const int base = (kh * 224 + q) * 128 + (colb ^ (((q >> 1) ^ kh) << 6));
    f32x16 a0 = {}, a1 = {};
    STAMP(2, 2);
#pragma unroll
    for (int st = 0; st < 2; ++st) {
      if (st == 0) asm volatile("s_waitcnt vmcnt(15)" : "+v"(of[0]), "+v"(of[1]), "+v"(of[2]), "+v"(of[3]), "+v"(of[4]), "+v"(of[5]), "+v"(of[6]), "+v"(of[7]) :: "memory");
      else asm volatile("s_waitcnt vmcnt(0)" : "+v"(of[8]), "+v"(of[9]), "+v"(of[10]), "+v"(of[11]), "+v"(of[12]), "+v"(of[13]), "+v"(of[14]), "+v"(of[15]) :: "memory");
      RAW_BARRIER();
      if (st == 0) STAMP(2, 3); else STAMP(2, 4);
      u32x4 fx[3], fy[3];
#pragma unroll
      for (int n = 0; n < 10; ++n) {
        if (n < 8) {
          const int i = st * 8 + n, hd = i >> 1, s = i & 1;
          const char* pa = lds + (hd * 28 + 16 * s) * 128;
          const int ra = base + h * 512, rb = (s ? base : base + h * 512) + 1024;
          u32x2 x0 = ldtr(pa + ra), x1 = ldtr(pa + rb), y0 = ldtr(pa + (ra ^ 64)), y1 = ldtr(pa + (rb ^ 64));
          fx[n % 3] = u32x4{x0[0], x0[1], x1[0], x1[1]}; fy[n % 3] = u32x4{y0[0], y0[1], y1[0], y1[1]};
        }
        SB();
        if (n >= 2) { a0 = mfma(fx[(n - 2) % 3], of[st * 8 + n - 2], a0); a1 = mfma(fy[(n - 2) % 3], of[st * 8 + n - 2], a1); }
        SB();
      }
    }
    STAMP(2, 5);
    f32x4* ex = (f32x4*)(lds + 56 * 1024);
    f32x16 snd = a1, kp = a0;
#pragma unroll
    for (int i = 0; i < 4; ++i) ex[(w * 4 + i) * 64 + lane] = f32x4{snd[4 * i], snd[4 * i + 1], snd[4 * i + 2], snd[4 * i + 3]};
    RAW_BARRIER();
#pragma unroll
    for (int i = 0; i < 4; ++i) {
      f32x4 v = ex[((w ^ 2) * 4 + i) * 64 + lane];
      kp[4 * i] += v[0]; kp[4 * i + 1] += v[1]; kp[4 * i + 2] += v[2]; kp[4 * i + 3] += v[3];
    }
    u16* dst = out + ((tq * 2 + qi) * 32 + l31) * 896 + tn * 64 + kh * 32 + 8 * h;
    *(u32x4*)dst = packs<0>(kp); *(u32x4*)(dst + 16) = packs<1>(kp);
  } else {
    const int b3 = xcd * 8 + idx - 4, s2 = b3 >> 1, sh = b3 & 1, r = w;
    u32x4* Dm = (u32x4*)lds;
    u32x4* F64 = (u32x4*)(lds + 61 * 1024);
    float* zs = (float*)lds;
    u32x4 ua[8], ub[8];
    if (w < 4) {
      const u32x4* up = U + s2 * 16 * 232 + (r ? 2 * r - 1 : 0) * 32;
      const unsigned uo = (h * 232 + sw23(l31)) * 16, uo2 = r ? uo + 512 : (h * 232 + 224 + (sw23(l31) & 7)) * 16;
#pragma unroll
      for (int i = 0; i < 8; ++i) { ua[i] = ald128(up + i * 464, uo); ub[i] = ald128(up + i * 464, uo2); }
    } else {
      const unsigned pl = (h * 32 + sw23(l31)) * 16;
#pragma unroll
      for (int i = 0; i < 4; ++i) dma16(g_tabs.f64 + ((w & 3) * 4 + i) * 256, pl, ldsaddr_of(lds) + 61 * 1024 + ((w & 3) * 4 + i) * 1024);
    }
    SB();
    {
      const int s = s2 + 32 * (32 * sh + l31);
      const float NRM2 = 2.0f / (2048.0f * 448.0f);
      u32x4 dr, di;
#pragma unroll
      for (int i = 0; i < 4; ++i) {
        int n = __mul24(8 * w + 4 * h + i, s) & 2047;
        float c = cosr(n, 1.0f / 2048) * NRM2, sn = sinr(n, 1.0f / 2048) * NRM2;
        dr[i] = pk2(c, -sn); di[i] = pk2(sn, c);
      }
      Dm[(w * 2) * 64 + lane] = dr; Dm[(w * 2 + 1) * 64 + lane] = di;
    }
    STAMP(2, 2);
    RAW_BARRIER();
    STAMP(2, 3);
    if (w < 4) {
      f32x16 ar = {}, ai = {}, br = {}, bi = {};
#pragma unroll
      for (int t = 0; t < 8; ++t) {
        u32x4 dr = Dm[(t * 2) * 64 + lane], di = Dm[(t * 2 + 1) * 64 + lane];
        asm volatile("s_waitcnt vmcnt(%2)" : "+v"(ua[t]), "+v"(ub[t]) : "n"(14 - 2 * t) : "memory");
        ar = mfma(ua[t], dr, ar); ai = mfma(ua[t], di, ai); br = mfma(ub[t], dr, br); bi = mfma(ub[t], di, bi);
      }
      STAMP(2, 4);
      RAW_BARRIER();
      u32x4 yr[4] = {packs<0>(ar), packs<1>(ar), packs<0>(br), packs<1>(br)};
      u32x4 yi[4] = {packs<0>(ai), packs<1>(ai), packs<0>(bi), packs<1>(bi)};
      const unsigned k2 = r ? ~0u : 0u, k1 = (r || !h) ? ~0u : 0u;
      yr[2] &= k1; yr[3] &= k2; yi[2] &= k2; yi[3] &= k2;
      float* zp = zs + ((r ? 2 * r - 1 : 0) * 32 + l31) * ZS + 8 * h;
      float* zq = zp + (r ? 32 * ZS : 0);
      f32x16 zre[2] = {}, zim[2] = {};
      u32x4 C = F64[lane], S = F64[64 + lane];
#pragma unroll
      for (int n = 0; n < 12; ++n) {
        const int at = n >> 2, t = n & 3;
        if (n < 8) {
          const u32x4 Cn = F64[((n + 1) & 7) * 128 + lane], Sn = F64[((n + 1) & 7) * 128 + 64 + lane];
          SB();
          zim[at] = mfma(C, yi[t], zim[at]); zre[at] = mfma(C, yr[t], zre[at]);
          zim[at] = mfma(S, yr[t], zim[at]); zre[at] = mfma(S ^ 0x80008000u, yi[t], zre[at]);
          C = Cn; S = Sn;
          SB();
        }
        if (n >= 4) {
          const int off = (at - 1) * 32 + 16 * (t >> 1) + 4 * (t & 1);
          *(f32x4*)(zq + off) = f32x4{zim[at - 1][4 * t], zim[at - 1][4 * t + 1], zim[at - 1][4 * t + 2], zim[at - 1][4 * t + 3]};
          *(f32x4*)(zp + off) = f32x4{zre[at - 1][4 * t], zre[at - 1][4 * t + 1], zre[at - 1][4 * t + 2], zre[at - 1][4 * t + 3]};
        }
      }
      STAMP(2, 5);
    } else {
      VMCNT(0);
      RAW_BARRIER();
    }
    RAW_BARRIER();
    STAMP(2, 6);
    {
      const int o = tid & 15, row = tid >> 4, rot = ((o >> 1) + 4 * (o & 1)) % 7;
      float* zp = zs + row * ZS + 4 * o;
      f32x4 zl[7];
#pragma unroll
      for (int c = 0; c < 7; ++c) zl[c] = *(const f32x4*)(zp + c * 32 * ZS);
      float y[4][7];
#pragma unroll
      for (int jp = 0; jp < 2; ++jp) {
        f32x2 z[7];
#pragma unroll
        for (int c = 0; c < 7; ++c) z[c] = f32x2{zl[c][2 * jp], zl[c][2 * jp + 1]};
        f32x2 y0 = z[0] + z[1] + z[3] + z[5];
        f32x2 e1 = z[0] + z[1] * C71 + z[3] * C72 + z[5] * C73, o1 = z[2] * S71 + z[4] * S72 + z[6] * S73;
        f32x2 e2 = z[0] + z[1] * C72 + z[3] * C73 + z[5] * C71, o2 = z[2] * S72 - z[4] * S73 - z[6] * S71;
        f32x2 e3 = z[0] + z[1] * C73 + z[3] * C71 + z[5] * C72, o3 = z[2] * S73 - z[4] * S71 + z[6] * S72;
        f32x2 yy[7] = {y0, e1 - o1, e2 - o2, e3 - o3, e3 + o3, e2 + o2, e1 + o1};
#pragma unroll
        for (int b = 0; b < 7; ++b) { y[2 * jp][b] = yy[b][0]; y[2 * jp + 1][b] = yy[b][1]; }
      }
#pragma unroll
      for (int b = 0; b < 7; ++b) {
        int m = b - rot; m += m < 0 ? 7 : 0;
        *(u32x2*)(zp + m * 32 * ZS) = u32x2{pk2(y[0][b], y[1][(b + 1) % 7]), pk2(y[2][(b + 2) % 7], y[3][(b + 3) % 7])};
      }
      u16* dst = out + (s2 + 32 * (32 * sh + row)) * 896 + 448 + 4 * o;
#pragma unroll
      for (int m = 0; m < 7; ++m) *(u32x2*)(dst + 64 * m) = *(const u32x2*)(zp + m * 32 * ZS);
    }
  }
  STAMP(2, 12); STAMP(2, 15);
}

extern "C" void kernel_launch(void* const* d_in, const int* in_sizes, int n_in, void* d_out, int out_size, void* d_ws, size_t ws_size,
                              hipStream_t stream) {
  const u16* ax = (const u16*)d_in[0];
  const u16* wqkv = (const u16*)d_in[1];
  const u16* wo = (const u16*)d_in[2];
  const u16* bx = (const u16*)d_in[3];
  const u16* wr = (const u16*)d_in[4];
  const u16* wi = (const u16*)d_in[5];
  char* ws = (char*)d_ws;
  u32x4* Qf = (u32x4*)(ws);
  u32x4* KVf = (u32x4*)(ws + (2u << 20));
  u32x4* Of = (u32x4*)(ws + (6u << 20));
  unsigned* T = (unsigned*)(ws + (8u << 20));
  unsigned* U = (unsigned*)(ws + (10u << 20));
  (void)hipFuncSetAttribute((const void*)k1_kernel, hipFuncAttributeMaxDynamicSharedMemorySize, K1_LDS);
  (void)hipFuncSetAttribute((const void*)k2_kernel, hipFuncAttributeMaxDynamicSharedMemorySize, K2_LDS);
  (void)hipFuncSetAttribute((const void*)k3_kernel, hipFuncAttributeMaxDynamicSharedMemorySize, K3_LDS);
  k1_kernel<<<256, 512, K1_LDS, stream>>>(ax, wqkv, bx, Qf, KVf, (u32x4*)T);
  k2_kernel<<<256, 384, K2_LDS, stream>>>(Qf, KVf, Of, T, U, wr, wi, wo);
  k3_kernel<<<288, 512, K3_LDS, stream>>>(Of, wo, (const u32x4*)U, (u16*)d_out);
}
```

```cpp
#include <hip/hip_runtime.h>
#include <hip/hip_bf16.h>

typedef __attribute__((ext_vector_type(8))) __bf16 bf16x8;
typedef __attribute__((ext_vector_type(4))) __bf16 bf16x4;
typedef __attribute__((ext_vector_type(2))) __bf16 bf16x2;
typedef __attribute__((ext_vector_type(4))) short s16x4;
typedef __attribute__((ext_vector_type(16))) float f32x16;
typedef __attribute__((ext_vector_type(4))) float f32x4;
typedef __attribute__((ext_vector_type(2))) float f32x2;
typedef __attribute__((ext_vector_type(4))) unsigned u32x4;
typedef __attribute__((ext_vector_type(2))) unsigned u32x2;
typedef unsigned short u16;
#define LDSP __attribute__((address_space(3)))
#define DEV __device__ __forceinline__

DEV unsigned pk2(float lo, float hi) {
  f32x2 v = {lo, hi};
  return __builtin_bit_cast(unsigned, __builtin_convertvector(v, bf16x2));
}
DEV f32x16 mfma(u32x4 a, u32x4 b, f32x16 c) {
  return __builtin_amdgcn_mfma_f32_32x32x16_bf16(__builtin_bit_cast(bf16x8, a), __builtin_bit_cast(bf16x8, b), c, 0, 0, 0);
}
template <int S> DEV u32x4 packs(const f32x16& v) {
  u32x4 r = {pk2(v[8 * S + 0], v[8 * S + 1]), pk2(v[8 * S + 2], v[8 * S + 3]), pk2(v[8 * S + 4], v[8 * S + 5]), pk2(v[8 * S + 6], v[8 * S + 7])};
  return r;
}
template <int S> DEV u32x4 packsn(const f32x16& v) {
  u32x4 r = {pk2(-v[8 * S + 0], -v[8 * S + 1]), pk2(-v[8 * S + 2], -v[8 * S + 3]), pk2(-v[8 * S + 4], -v[8 * S + 5]), pk2(-v[8 * S + 6], -v[8 * S + 7])};
  return r;
}
DEV int sw23(int r) { return (r & ~12) | ((r & 4) << 1) | ((r & 8) >> 1); }
DEV float cosr(int n, float inv) { return __builtin_amdgcn_cosf((float)n * inv); }
DEV float sinr(int n, float inv) { return __builtin_amdgcn_sinf((float)n * inv); }
DEV void glds16(const void* g, char* l) {
  __builtin_amdgcn_global_load_lds((const unsigned*)g, (LDSP unsigned*)l, 16, 0, 0);
}
DEV void dma16(const void* base, unsigned voff, unsigned ldsaddr) {
  asm volatile("s_mov_b32 m0, %2\n\ts_nop 0\n\tglobal_load_lds_dwordx4 %0, %1" ::"v"(voff), "s"(base), "s"(ldsaddr) : "memory");
}
DEV unsigned ldsaddr_of(const char* p) { return (unsigned)(unsigned long long)(const LDSP char*)p; }
DEV u32x4 ald128(const void* base, unsigned off) {
  u32x4 r; asm volatile("global_load_dwordx4 %0, %1, %2" : "=v"(r) : "v"(off), "s"(base) : "memory"); return r;
}
DEV void st128(void* p, u32x4 v) { asm volatile("global_store_dwordx4 %0, %1, off sc1\n\ts_nop 1" ::"v"(p), "v"(v) : "memory"); }
#define SB() __builtin_amdgcn_sched_barrier(0)
DEV u32x2 ldtr(const char* l) {
  return __builtin_bit_cast(u32x2, __builtin_amdgcn_ds_read_tr16_b64_v4i16((LDSP s16x4*)l));
}
DEV float bflo(unsigned u) { return __builtin_bit_cast(float, u << 16); }
DEV float bfhi(unsigned u) { return __builtin_bit_cast(float, u & 0xffff0000u); }
#define RAW_BARRIER() do { asm volatile("s_waitcnt lgkmcnt(0)" ::: "memory"); __builtin_amdgcn_s_barrier(); asm volatile("" ::: "memory"); } while (0)
#define VMCNT(n) asm volatile("s_waitcnt vmcnt(" #n ")" ::: "memory")

#define STAMP_INIT
#define STAMP(k, i)
constexpr float C71 = 0.62348980185873353f, C72 = -0.22252093395631440f, C73 = -0.90096886790241913f;
constexpr float S71 = 0.78183148246802981f, S72 = 0.97492791218182361f, S73 = 0.43388373911755812f;

DEV void gen_cs(int i, int k0, int mul, int add, int mask, float inv, u32x4& C, u32x4& S) {
  float c[8], s[8];
#pragma unroll
  for (int j = 0; j < 8; ++j) {
    int n = (__mul24(i, mul * k0 + add) + __mul24(i, mul) * j) & mask;
    c[j] = cosr(n, inv); s[j] = sinr(n, inv);
  }
#pragma unroll
  for (int j = 0; j < 4; ++j) { C[j] = pk2(c[2 * j], c[2 * j + 1]); S[j] = pk2(s[2 * j], s[2 * j + 1]); }
}

constexpr double ccos64(int n) {
  n &= 63; if (n > 32) n = 64 - n;
  double sg = 1.0; if (n > 16) { n = 32 - n; sg = -1.0; }
  if (n == 16) return 0.0;
  double x = 6.283185307179586476925 * n / 64.0, x2 = x * x, term = 1.0, sum = 1.0;
  for (int i = 1; i < 14; ++i) { term *= -x2 / ((2 * i - 1) * (2 * i)); sum += term; }
  return sg * sum;
}
constexpr unsigned cbf(double v) { unsigned u = __builtin_bit_cast(unsigned, (float)v); return (u + 0x7fffu + ((u >> 16) & 1u)) >> 16; }
constexpr int csw(int r) { return (r & ~12) | ((r & 4) << 1) | ((r & 8) >> 1); }
struct alignas(16) Tabs { unsigned f64[16 * 256]; unsigned b2[14 * 256]; };
constexpr Tabs make_tabs() {
  Tabs t{};
  unsigned c[64] = {}, sn[64] = {};
  for (int n = 0; n < 64; ++n) { c[n] = cbf(ccos64(n)); sn[n] = cbf(ccos64(n - 16 + 64)); }
  for (int f = 0; f < 8; ++f)
    for (int l = 0; l < 64; ++l)
      for (int d = 0; d < 4; ++d) {
        int i = 32 * (f >> 2) + (l & 31), k = 16 * (f & 3) + 8 * (l >> 5) + 2 * d;
        t.f64[((f * 2) * 64 + l) * 4 + d] = c[(i * k) & 63] | (c[(i * (k + 1)) & 63] << 16);
        t.f64[((f * 2 + 1) * 64 + l) * 4 + d] = sn[(i * k) & 63] | (sn[(i * (k + 1)) & 63] << 16);
      }
  for (int l = 0; l < 64; ++l)
    for (int d = 0; d < 4; ++d) {
      for (int f = 0; f < 4; ++f) {
        int n = (2 * csw(l & 31) * (8 * f + 4 * (l >> 5) + d)) & 63;
        t.b2[(f * 64 + l) * 4 + d] = c[n] | (sn[n] << 16);
        t.b2[((4 + f) * 64 + l) * 4 + d] = (sn[n] ^ 0x8000u) | (c[n] << 16);
      }
      for (int f = 0; f < 2; ++f) {
        int k = 16 * f + 8 * (l >> 5) + 2 * d, n0 = (2 * (l & 31) * k) & 63, n1 = (2 * (l & 31) * (k + 1)) & 63;
        t.b2[((8 + f) * 64 + l) * 4 + d] = c[n0] | (c[n1] << 16);
        t.b2[((10 + f) * 64 + l) * 4 + d] = sn[n0] | (sn[n1] << 16);
        t.b2[((12 + f) * 64 + l) * 4 + d] = (sn[n0] | (sn[n1] << 16)) ^ 0x80008000u;
      }
    }
  return t;
}
__device__ const Tabs g_tabs = make_tabs();
DEV unsigned ald32(const void* base, unsigned off) {
  unsigned r; asm volatile("global_load_dword %0, %1, %2" : "=v"(r) : "v"(off), "s"(base) : "memory"); return r;
}
DEV unsigned ald16(const void* base, unsigned off) {
  unsigned r; asm volatile("global_load_ushort %0, %1, %2" : "=v"(r) : "v"(off), "s"(base) : "memory"); return r;
}
DEV void ast32(void* base, unsigned off, unsigned v) { asm volatile("global_store_dword %0, %1, %2 sc1" ::"v"(off), "v"(v), "s"(base) : "memory"); }
DEV int col_bin(int c) { int ct = c >> 5, k = ct == 7 ? 7 * (c - 192) : 7 * ((ct ? ((ct + 1) & 1) * 32 : 0) + (c & 31)) + 64 * ((ct + 1) >> 1); return k >= 448 ? k - 448 : k; }

constexpr int K1_SLOT = 32 * 1024;
constexpr int K1_LDS = 128 * 1024;
constexpr unsigned CODE_BYTES = 26000;
struct QF { u32x4 x0[4], x1[4], w[4]; };

template <bool ISV>
DEV void qkv_loop(const char* lds, unsigned ld, const u16* xb, const u16* wb, int kn, unsigned xo, unsigned wo0, unsigned wo1,
                  int xoff, int woff, f32x16& a0, f32x16& a1) {
  auto piece = [&](int slot, int i) {
    const unsigned d = ld + slot * K1_SLOT;
    if (i == 0) dma16(xb + kn * 64, xo, d);
    if (i == 1) dma16(wb + kn * (64 * 1344), wo0, d + 16384);
    if (i == 2) dma16(xb + kn * 64 + 64 * 448, xo, d + 8192);
    if (i == 3) { dma16(wb + kn * (64 * 1344), wo1, d + 24576); kn = kn == 6 ? 0 : kn + 1; }
  };
  auto rd = [&](QF& f, int slot, int ks) {
    const char* sl = lds + slot * K1_SLOT;
    f.x0[ks] = *(const u32x4*)(sl + (xoff ^ (ks * 32)));
    f.x1[ks] = *(const u32x4*)(sl + (xoff ^ (ks * 32)) + 4096);
    u32x2 wa = ldtr(sl + woff + ks * 16 * 224), wc = ldtr(sl + woff + ks * 16 * 224 + 224);
    f.w[ks] = u32x4{wa[0], wa[1], wc[0], wc[1]};
  };
  QF f[2];
#pragma unroll
  for (int kc = 0; kc < 8; ++kc) {
    if (kc < 7) { if (kc < 6) VMCNT(4); else VMCNT(0); __builtin_amdgcn_s_barrier(); asm volatile("" ::: "memory"); }
#pragma unroll
    for (int ks = 0; ks < 4; ++ks) {
      if (kc < 7) rd(f[kc & 1], kc & 3, ks);
      if (kc > 0) {
        QF& g = f[(kc - 1) & 1];
        if (ISV) { a0 = mfma(g.x0[ks], g.w[ks], a0); a1 = mfma(g.x1[ks], g.w[ks], a1); }
        else     { a0 = mfma(g.w[ks], g.x0[ks], a0); a1 = mfma(g.w[ks], g.x1[ks], a1); }
      }
      if (kc < 5) piece((kc + 2) & 3, ks);
      SB();
    }
  }
}

__global__ __launch_bounds__(512, 2) void k1_kernel(const u16* __restrict__ ax, const u16* __restrict__ wqkv, const u16* __restrict__ bx,
                                                    u32x4* __restrict__ Qf, u32x4* __restrict__ KVf, u32x4* __restrict__ T) {
  extern __shared__ __attribute__((aligned(16))) char lds[];
  const int tid = threadIdx.x, lane = tid & 63, w = __builtin_amdgcn_readfirstlane(tid >> 6);
  const int xcd = blockIdx.x & 7, idx = blockIdx.x >> 3;
  const int l31 = lane & 31, h = lane >> 5;
  const unsigned lb = ldsaddr_of(lds);
  unsigned long long pc0;
  asm volatile("s_getpc_b64 %0" : "=s"(pc0));
  const unsigned po = (tid & 255) * 128;
  unsigned pf = ald32(w < 4 ? (const char*)(pc0 & ~127ull) : (const char*)&g_tabs, (idx == 0 && po < (w < 4 ? CODE_BYTES : 30720u)) ? po : 0u);
  STAMP_INIT; STAMP(0, 0); STAMP(0, 1);
  if (__builtin_expect(idx >= 8, 1)) {
    const int u = idx - 8, which = u >> 3, tm = (xcd & 3) * 4 + (u & 3), g = (xcd >> 2) * 2 + ((u >> 2) & 1);
    const int row = 8 * w + (lane >> 3);
    const unsigned xo = (row * 448 + ((lane & 7) ^ ((row >> 1) & 7)) * 8) * 2;
    unsigned wo2[2];
#pragma unroll
    for (int i = 0; i < 2; ++i) {
      int q = 64 * (w + 8 * i) + lane; if (q > 895) q = 895;
      int rho = q / 14, cc = q - rho * 14;
      wo2[i] = (((rho & ~7) + ((rho & 7) >> 1) + 4 * (rho & 1)) * 1344 + cc * 8) * 2;
    }
    const u16* xb = ax + tm * 128 * 448;
    const u16* wb = wqkv + which * 448 + g * 112;
    int kn = ((u & 3) * 2 + (u >> 2)) % 7;
#pragma unroll
    for (int i = 0; i < 2; ++i) {
      const unsigned d = lb + w * 1024 + i * K1_SLOT;
      dma16(xb + kn * 64, xo, d); dma16(wb + kn * (64 * 1344), wo2[0], d + 16384);
      dma16(xb + kn * 64 + 64 * 448, xo, d + 8192); dma16(wb + kn * (64 * 1344), wo2[1], d + 24576);
      kn = kn == 6 ? 0 : kn + 1;
    }
    SB();
    const int hh = w & 3, th = w >> 2, r0 = th * 64 + l31;
    const int xoff = r0 * 128 + ((h ^ ((r0 >> 1) & 7)) * 16);
    const int woff = 16384 + (8 * h + 2 * ((lane >> 2) & 3)) * 224 + (hh * 28 + ((lane >> 4) & 1) * 16 + 4 * (lane & 3)) * 2;
    const int head = g * 4 + hh, tile = tm * 4 + th * 2;
    f32x16 a0 = {}, a1 = {};
    u32x4 o0, o1, o2, o3;
    if (which == 2) {
      qkv_loop<true>(lds, lb + w * 1024, xb, wb, kn, xo, wo2[0], wo2[1], xoff, woff, a0, a1);
      o0 = packs<0>(a0); o1 = packs<1>(a0); o2 = packs<0>(a1); o3 = packs<1>(a1);
      if (l31 >= 28) { unsigned f = (l31 == 28) ? 0x3F803F80u : 0u; o0 = u32x4{f, f, f, f}; o1 = o0; o2 = o0; o3 = o0; }
    } else {
      qkv_loop<false>(lds, lb + w * 1024, xb, wb, kn, xo, wo2[0], wo2[1], xoff, woff, a0, a1);
      const float sc = which ? 1.0f : 0.18898223650461362f * 1.4426950408889634f;
      a0 *= sc; a1 *= sc;
      o0 = packs<0>(a0); o1 = packs<1>(a0); o2 = packs<0>(a1); o3 = packs<1>(a1);
      if (h) { o1[2] = which ? 0x3F80u : 0u; o1[3] = 0; o3[2] = o1[2]; o3[3] = 0; }
    }
    STAMP(0, 7);
    asm volatile("" ::"v"(pf));
    u32x4* o = (which == 0 ? Qf + ((head * 64 + tile) * 2) * 64 : KVf + ((head * 64 + tile) * 4 + (which & 2)) * 64) + lane;
    const int st = which == 0 ? 128 : 256;
    st128(o, o0); st128(o + 64, o1); st128(o + st, o2); st128(o + st + 64, o3);
  } else {
    const int b1 = xcd * 8 + idx, s2 = b1 >> 1, hb = b1 & 1;
    u32x4* F64 = (u32x4*)(lds + 56 * 1024);
    u32x4* Ap = (u32x4*)(lds + 72 * 1024);
    u32x4* Xf = (u32x4*)(lds + 88 * 1024);
    {
      const int row = 8 * w + (lane >> 3);
      const unsigned xo = ((s2 + 32 * ((row & 32) + sw23(row & 31))) * 448 + ((lane & 7) ^ ((row >> 1) & 7)) * 8) * 2;
#pragma unroll
      for (int m = 0; m < 7; ++m) dma16(bx + 64 * m, xo, lb + m * 8192 + w * 1024);
      dma16(g_tabs.f64 + w * 512, lane * 16, lb + 56 * 1024 + w * 2048); dma16(g_tabs.f64 + w * 512 + 256, lane * 16, lb + 57 * 1024 + w * 2048);
    }
    SB();
    const int mt = w >> 2, o = 2 * (w & 3) + h;
    {
      u32x4 C, S;
      gen_cs(32 * mt + l31, 8 * o, 32, s2, 2047, 1.0f / 2048, C, S);
      Ap[(w * 2 + 0) * 64 + lane] = C; Ap[(w * 2 + 1) * 64 + lane] = S;
    }
    char* xp = lds + (32 * mt + l31) * 128 + ((o ^ ((l31 >> 1) & 7)) * 16);
    int xm[7];
#pragma unroll
    for (int b = 0; b < 7; ++b) { int m = b - o; m += m < 0 ? 7 : 0; m += m < 0 ? 7 : 0; xm[b] = m * 8192; asm volatile("" : "+v"(xm[b])); }
    STAMP(0, 2);
    asm volatile("s_waitcnt vmcnt(2)" : "+v"(pf) :: "memory");
    RAW_BARRIER();
    STAMP(0, 3);
    {
      u32x4 xr[7], z[4];
#pragma unroll
      for (int b = 0; b < 7; ++b) xr[b] = *(const u32x4*)(xp + xm[b]);
      const float ca = hb ? C72 : C71, cb = hb ? C73 : C72, cc = hb ? C71 : C73, sa = hb ? S72 : S71, sb = hb ? -S73 : S72, sc = hb ? -S71 : S73;
      const float da = hb ? C73 : 1.0f, db = hb ? C71 : 1.0f, dc = hb ? C72 : 1.0f, ta = hb ? S73 : 0.0f, tb = hb ? -S71 : 0.0f, tc = hb ? S72 : 0.0f;
#pragma unroll
      for (int jp = 0; jp < 4; ++jp) {
        f32x2 v[7];
#pragma unroll
        for (int b = 0; b < 7; ++b) {
          v[b][0] = bflo(xr[(b - 2 * jp + 14) % 7][jp]);
          v[b][1] = bfhi(xr[(b - 2 * jp - 1 + 14) % 7][jp]);
        }
        f32x2 p1 = v[1] + v[6], p2 = v[2] + v[5], p3 = v[3] + v[4];
        f32x2 m1 = v[6] - v[1], m2 = v[5] - v[2], m3 = v[4] - v[3];
        f32x2 ra = v[0] + p1 * ca + p2 * cb + p3 * cc, ia = m1 * sa + m2 * sb + m3 * sc;
        f32x2 rb = v[0] + p1 * da + p2 * db + p3 * dc, ib = m1 * ta + m2 * tb + m3 * tc;
        z[0][jp] = pk2(ra[0], ra[1]); z[1][jp] = pk2(ia[0], ia[1]); z[2][jp] = pk2(rb[0], rb[1]); z[3][jp] = pk2(ib[0], ib[1]);
      }
#pragma unroll
      for (int c = 0; c < 4; ++c) *(u32x4*)(xp + c * 8192) = z[c];
    }
    VMCNT(0);
    RAW_BARRIER();
    STAMP(0, 4);
    const int wc = w & 3, ct = hb ? 3 + wc : (wc == 2 ? 0 : (wc == 3 ? 7 : wc + 1)), kt = w & 1;
    u32x4 ap[8];
    {
      f32x16 xre = {}, xim = {};
      const char* zp = lds + (w & 2) * 8192 + mt * 4096 + l31 * 128;
      const int zsw = (h ^ ((l31 >> 1) & 7)) * 16;
      u32x4 C[4], S[4], zr[4], zi[4];
#pragma unroll
      for (int tt = 0; tt < 4; ++tt) {
        zr[tt] = *(const u32x4*)(zp + (zsw ^ (tt * 32))); C[tt] = F64[((kt * 4 + tt) * 2) * 64 + lane];
        zi[tt] = *(const u32x4*)(zp + 8192 + (zsw ^ (tt * 32))); S[tt] = F64[((kt * 4 + tt) * 2 + 1) * 64 + lane];
      }
#pragma unroll
      for (int tt = 0; tt < 8; ++tt) ap[tt] = Ap[(mt * 8 + tt) * 64 + lane];
      SB();
#pragma unroll
      for (int tt = 0; tt < 4; ++tt) {
        xre = mfma(zr[tt], C[tt], xre); xim = mfma(zi[tt], C[tt], xim);
        xre = mfma(zi[tt], S[tt], xre); xim = mfma(zr[tt], S[tt] ^ 0x80008000u, xim);
      }
      u32x4* xq = Xf + (wc * 4 + mt * 2) * 128 + lane;
      xq[0] = packs<0>(xre); xq[64] = packs<0>(xim); xq[128] = packs<1>(xre); xq[192] = packs<1>(xim);
    }
    RAW_BARRIER();
    STAMP(0, 5);
    {
      f32x16 tre = {}, tim = {};
      u32x4 bf[8];
#pragma unroll
      for (int tt = 0; tt < 8; ++tt) bf[tt] = Xf[(wc * 8 + tt) * 64 + lane];
      SB();
#pragma unroll
      for (int tt = 0; tt < 4; ++tt) {
        tre = mfma(ap[2 * tt], bf[2 * tt], tre); tim = mfma(ap[2 * tt], bf[2 * tt + 1], tim);
        tre = mfma(ap[2 * tt + 1], bf[2 * tt + 1], tre); tim = mfma(ap[2 * tt + 1] ^ 0x80008000u, bf[2 * tt], tim);
      }
      u32x4* tp = T + (s2 * 16 + mt * 8 + h) * 232 + ct * 32 + l31;
      if (ct < 7 || l31 < 8)
#pragma unroll
      for (int q = 0; q < 4; ++q)
        st128(tp + 2 * q * 232, u32x4{pk2(tre[4 * q], tim[4 * q]), pk2(tre[4 * q + 1], tim[4 * q + 1]), pk2(tre[4 * q + 2], tim[4 * q + 2]), pk2(tre[4 * q + 3], tim[4 * q + 3])});
    }
  }
  STAMP(0, 12); STAMP(0, 15);
}

constexpr int K2_LDS = 84 * 1024;
DEV float xhalf_max(float v) {
  float a = v, b = v;
  asm volatile("s_nop 1\n\tv_permlane32_swap_b32 %0, %1\n\ts_nop 1" : "+v"(a), "+v"(b));
  return fmaxf(a, b);
}
DEV float max16(const f32x16& s) {
  float m = fmaxf(fmaxf(s[0], s[1]), s[2]);
#pragma unroll
  for (int i = 3; i < 15; i += 2) m = fmaxf(fmaxf(m, s[i]), s[i + 1]);
  return fmaxf(m, s[15]);
}
#define PIN16(a) asm volatile("" : "+v"(a[0]), "+v"(a[1]), "+v"(a[2]), "+v"(a[3]), "+v"(a[4]), "+v"(a[5]), "+v"(a[6]), "+v"(a[7]), \
                                   "+v"(a[8]), "+v"(a[9]), "+v"(a[10]), "+v"(a[11]), "+v"(a[12]), "+v"(a[13]), "+v"(a[14]), "+v"(a[15]))
#define EX(s, i) s[i] = __builtin_amdgcn_exp2f(s[i])
#define EX4(s, i, P, j)                                                                                          \
  { const float t0_ = __builtin_amdgcn_exp2f(s[i]); SB(); P[j] = pk2(e0, e1); SB();                               \
    const float t1_ = __builtin_amdgcn_exp2f(s[i + 1]); SB(); P[j + 1] = pk2(e2, e3); SB();                       \
    e0 = t0_; e1 = t1_; e2 = __builtin_amdgcn_exp2f(s[i + 2]); SB(); e3 = __builtin_amdgcn_exp2f(s[i + 3]); SB(); }
#define TILE(sc, sn, K0, K1, Q0, Q1, oc, V0, op, V1, L0, L1, L2, L3)                 \
  sn = mfma(K0, Q0, f32x16{}); SB(); L0; EX4(sc, 0, p1, 2)                           \
  op = mfma(V1, p1, op); SB(); L1; EX4(sc, 4, p0, 0)                                 \
  sn = mfma(K1, Q1, sn); SB(); L2; EX4(sc, 8, p0, 2)                                 \
  oc = mfma(V0, p0, oc); SB(); L3; EX4(sc, 12, p1, 0)
#define KTILE(X, Y, P1, P2)                                                                                      \
  TILE(sa, sb, X##k0, X##k1, q0[1], q1[1], o[0], X##v0, o[3], Y##v1, , Y##v1 = (P1)[192], , )                     \
  TILE(sb, sa, X##k0, X##k1, q0[2], q1[2], o[1], X##v0, o[0], X##v1, , , , )                                      \
  TILE(sa, sb, X##k0, X##k1, q0[3], q1[3], o[2], X##v0, o[1], X##v1, X##k0 = (P2)[0], , X##k1 = (P2)[64], )       \
  TILE(sb, sa, Y##k0, Y##k1, q0[0], q1[0], o[3], X##v0, o[2], X##v1, , , , X##v0 = (P2)[128])

__global__ __launch_bounds__(384, 2) void k2_kernel(const u32x4* __restrict__ Qf, const u32x4* __restrict__ KVf, u32x4* __restrict__ Of,
                                                    const unsigned* __restrict__ T, unsigned* __restrict__ U,
                                                    const u16* __restrict__ Wr, const u16* __restrict__ Wi, const u16* __restrict__ wo) {
  extern __shared__ __attribute__((aligned(16))) char lds[];
  const int tid = threadIdx.x, lane = tid & 63, w = __builtin_amdgcn_readfirstlane(tid >> 6);
  const int xcd = blockIdx.x & 7, idx = blockIdx.x >> 3;
  const int l31 = lane & 31, h = lane >> 5;
  STAMP_INIT; STAMP(1, 0); STAMP(1, 1);
  if (__builtin_expect(w < 4, 1)) {
    __builtin_amdgcn_s_setprio(2);
    const int head = xcd * 2 + (idx >> 4), qt = (idx & 15) * 4;
    const u32x4* kp = KVf + (head * 64 + w * 16) * 256 + lane;
    u32x4 q0[4], q1[4];
    u32x4 ek0 = kp[0], ek1 = kp[64];
#pragma unroll
    for (int i = 0; i < 4; ++i) { const u32x4* qp = Qf + (head * 64 + qt + ((w + i) & 3)) * 128 + lane; q0[i] = qp[0]; q1[i] = qp[64]; }
    u32x4 ev0 = kp[128], ev1 = kp[192], dk0 = kp[256], dk1 = kp[320], dv0 = kp[384], dv1 = {}, p0, p1 = {};
    f32x16 o[4] = {}, sa, sb;
    float m[4], e0 = 0.f, e1 = 0.f, e2 = 0.f, e3 = 0.f;
    {
      f32x16 s4[4];
#pragma unroll
      for (int i = 0; i < 4; ++i) { s4[i] = mfma(ek0, q0[i], f32x16{}); s4[i] = mfma(ek1, q1[i], s4[i]); }
#pragma unroll
      for (int i = 0; i < 4; ++i) {
        const unsigned nm = pk2(-xhalf_max(max16(s4[i])), 0.0f);
        m[i] = -bflo(nm);
        if (h) q1[i][2] = nm;
      }
      sa = s4[0] - m[0];
    }
    STAMP(1, 3);
#pragma unroll 1
    for (int it = 0; it < 8; ++it) {
      const u32x4* kn = kp + ((2 * it + 2) & 15) * 256;
      KTILE(e, d, kp + (2 * it + 1) * 256, kn)
      KTILE(d, e, kn, kn + 256)
    }
    p1[2] = pk2(e0, e1); p1[3] = pk2(e2, e3);
    o[3] = mfma(dv1, p1, o[3]);
    STAMP(1, 11);
    if (__builtin_expect(__ballot(h && !(o[0][12] + o[1][12] + o[2][12] + o[3][12] < 1.0e12f)) != 0ull, 0)) {
#pragma unroll
      for (int u = 0; u < 4; ++u) {
        f32x16 oo = {}; float mm = -3.0e38f;
        if (h) q1[u][2] = 0;
#pragma unroll 1
        for (int t = 0; t < 16; ++t) {
          const u32x4* p = kp + t * 256;
          f32x16 s = mfma(p[0], q0[u], f32x16{}); s = mfma(p[64], q1[u], s);
          float mn = fmaxf(mm, xhalf_max(max16(s)));
          float al = __builtin_amdgcn_exp2f(mm - mn);
          mm = mn;
#pragma unroll
          for (int i = 0; i < 16; ++i) { oo[i] *= al; s[i] = __builtin_amdgcn_exp2f(s[i] - mn); }
          oo = mfma(p[128], packs<0>(s), oo); oo = mfma(p[192], packs<1>(s), oo);
        }
        o[u] = oo; m[u] = mm;
      }
    }
#pragma unroll
    for (int u = 1; u < 4; ++u) {
      if (h) o[u][13] = m[u];
      f32x4* ex = (f32x4*)(lds + (((w + u) & 3) * 3 + u - 1) * 4096) + lane;
#pragma unroll
      for (int i = 0; i < 4; ++i) ex[i * 64] = f32x4{o[u][4 * i], o[u][4 * i + 1], o[u][4 * i + 2], o[u][4 * i + 3]};
    }
    const float l0 = xhalf_max(h ? o[0][12] : -1.0f);
    RAW_BARRIER();
    STAMP(1, 12);
    const char* ey = lds + w * 12288;
    f32x2 st[3];
#pragma unroll
    for (int u = 0; u < 3; ++u) st[u] = *(const f32x2*)(ey + u * 4096 + 3584 + l31 * 16);
    f32x4 y[12];
#pragma unroll
    for (int i = 0; i < 12; ++i) y[i] = *(const f32x4*)(ey + i * 1024 + lane * 16);
    SB();
    const float mm = fmaxf(fmaxf(st[0][1], st[1][1]), fmaxf(st[2][1], m[0]));
    float f0 = __builtin_amdgcn_exp2f(m[0] - mm), f[3];
#pragma unroll
    for (int u = 0; u < 3; ++u) f[u] = __builtin_amdgcn_exp2f(st[u][1] - mm);
    const float inv = __builtin_amdgcn_rcpf(l0 * f0 + st[0][0] * f[0] + st[1][0] * f[1] + st[2][0] * f[2]);
    f0 *= inv;
    f32x16 r = o[0] * f0;
    asm volatile("" : "+v"(r) : "v"(f0));
#pragma unroll
    for (int u = 0; u < 3; ++u) {
      const float fu = f[u] * inv;
#pragma unroll
      for (int i = 0; i < 4; ++i) {
        const f32x4 v = y[u * 4 + i];
        r[4 * i] += v[0] * fu; r[4 * i + 1] += v[1] * fu; r[4 * i + 2] += v[2] * fu; r[4 * i + 3] += v[3] * fu;
      }
    }
    u32x4 r1 = packs<1>(r);
    if (h) { r1[2] = 0; r1[3] = 0; }
    u32x4* op = Of + (((qt + w) * 16 + head) * 2) * 64 + lane;
    st128(op, packs<0>(r)); st128(op + 64, r1);
  } else if (idx < 29) {
    const int jq = xcd * 2 + w - 4, col = idx * 8 + (l31 >> 2), kb = col_bin(col), jr = 4 * jq + (lane & 3) + 512 * h;
    const unsigned toff = (4 * h * 16 + jq) * 928 + idx * 32 + l31;
    const bool cj = kb > 224;
    const int of = jr * 450 + 2 * kb, om = (2048 - jr) * 450 + 2 * (448 - kb), om0 = jr ? om : om - 921600;
    const float sr = col > 224 ? 0.0f : ((col == 0 || col == 224) ? 0.5f : 1.0f), si = cj ? -sr : sr;
    u32x4 mt[14];
    unsigned ta[16], ur[16], ui[16]; float wr[16], wi[16];
#pragma unroll
    for (int i = 0; i < 16; ++i) ta[i] = ald32(T + (8 * (i >> 2) + (i & 3)) * 14848, toff * 4);
#pragma unroll
    for (int g = 0; g < 16; ++g) {
      const unsigned o_ = g ? (cj ? om : of) + ((g & 7) + 16 * (g >> 3)) * (cj ? -28800 : 28800) : (cj ? om0 : of);
      ur[g] = ald16(Wr, o_); ui[g] = ald16(Wi, o_);
    }
    const unsigned* tabp = g_tabs.b2;
    asm volatile("" : "+s"(tabp));
#pragma unroll
    for (int t = 0; t < 14; ++t) mt[t] = ald128(tabp + t * 256, lane * 16);
    asm volatile("s_waitcnt vmcnt(0)" : "+v"(mt[0]), "+v"(mt[1]), "+v"(mt[2]), "+v"(mt[3]), "+v"(mt[4]), "+v"(mt[5]), "+v"(mt[6]), "+v"(mt[7]), "+v"(mt[8]),
                 "+v"(mt[9]), "+v"(mt[10]), "+v"(mt[11]), "+v"(mt[12]), "+v"(mt[13]) :: "memory");
    PIN16(ta); PIN16(ur); PIN16(ui);
#pragma unroll
    for (int g = 0; g < 16; ++g) { wr[g] = bflo(ur[g]); wi[g] = bflo(ui[g]); }
    SB();
    f32x16 xr = {}, xi = {};
#pragma unroll
    for (int t = 0; t < 4; ++t) {
      u32x4 b = {ta[4 * t], ta[4 * t + 1], ta[4 * t + 2], ta[4 * t + 3]};
      xr = mfma(mt[t], b, xr); xi = mfma(mt[4 + t], b, xi);
    }
    f32x16 yr, yi;
#pragma unroll
    for (int g = 0; g < 16; g += 2) {
      f32x2 c = {wr[g], wr[g + 1]}, d = {wi[g], wi[g + 1]};
      asm volatile("" : "+v"(c), "+v"(d));
      c *= sr; d *= si;
      const f32x2 a = {xr[g], xr[g + 1]}, b = {xi[g], xi[g + 1]}, r = a * c - b * d, i = a * d + b * c;
      yr[g] = r[0]; yr[g + 1] = r[1]; yi[g] = i[0]; yi[g + 1] = i[1];
    }
    u32x4 r0 = packs<0>(yr), r1 = packs<1>(yr), i0 = packs<0>(yi), i1 = packs<1>(yi);
    f32x16 ure = {}, uim = {};
    ure = mfma(mt[8], r0, ure); uim = mfma(mt[8], i0, uim);
    ure = mfma(mt[12], i0, ure); uim = mfma(mt[10], r0, uim);
    ure = mfma(mt[9], r1, ure); uim = mfma(mt[9], i1, uim);
    ure = mfma(mt[13], i1, ure); uim = mfma(mt[11], r1, uim);
#pragma unroll
    for (int g = 0; g < 16; ++g) ast32(U + ((g & 3) + 8 * (g >> 2)) * 14848, toff * 4, pk2(ure[g], uim[g]));
    {
      const unsigned off = ((idx * 2 + w - 4) * 64 + lane) * 128;
      if (off < 401408u) { unsigned pf = *(const unsigned*)((const char*)wo + off); asm volatile("" ::"v"(pf)); }
    }
  }
  STAMP(1, 14); STAMP(1, 15);
}

constexpr int ZS = 68;
constexpr int K3_LDS = 77 * 1024;

__global__ __launch_bounds__(512, 4) void k3_kernel(const u32x4* __restrict__ Of, const u16* __restrict__ wo, const u32x4* __restrict__ U,
                                                    u16* __restrict__ out) {
  extern __shared__ __attribute__((aligned(16))) char lds[];
  const int tid = threadIdx.x, lane = tid & 63, w = __builtin_amdgcn_readfirstlane(tid >> 6);
  const int xcd = blockIdx.x & 7, idx = blockIdx.x >> 3;
  const int l31 = lane & 31, h = lane >> 5;
  STAMP_INIT; STAMP(2, 0); STAMP(2, 1);
  if (idx < 4 || idx >= 12) {
    if (w >= 4) return;
    const int u = idx < 4 ? idx : idx - 8, tq = xcd * 4 + u / 7, tn = u % 7;
    const int qi = w & 1, kh = w >> 1;
    u32x4 of[16];
    {
      const int r8 = lane >> 3;
      const unsigned so = (r8 * 448 + ((lane & 7) ^ ((r8 & 2) << 1)) * 8) * 2, l16 = lane * 16;
      const u16* src = wo + (kh * 224 + qi * 8) * 448 + tn * 64;
      const unsigned d = ldsaddr_of(lds) + (kh * 224 + qi * 8) * 128;
      const u32x4* op = Of + (((tq * 2 + qi) * 16 + kh * 8) * 2) * 64;
#pragma unroll
      for (int st = 0; st < 2; ++st) {
#pragma unroll
        for (int i = 0; i < 7; ++i) dma16(src + (st * 7 + i) * 16 * 448, so, d + (st * 7 + i) * 2048);
#pragma unroll
        for (int i = 0; i < 8; ++i) of[st * 8 + i] = ald128(op + (st * 8 + i) * 64, l16);
      }
    }
    const int q = (lane >> 2) & 3, p = lane & 3, pc = ((p & 1) << 1) | (p >> 1);
    const int colb = ((lane >> 4) & 1) * 32 + pc * 8;
    const int base = (kh * 224 + q) * 128 + (colb ^ (((q >> 1) ^ kh) << 6));
    f32x16 a0 = {}, a1 = {};
    STAMP(2, 2);
#pragma unroll
    for (int st = 0; st < 2; ++st) {
      if (st == 0) asm volatile("s_waitcnt vmcnt(15)" : "+v"(of[0]), "+v"(of[1]), "+v"(of[2]), "+v"(of[3]), "+v"(of[4]), "+v"(of[5]), "+v"(of[6]), "+v"(of[7]) :: "memory");
      else asm volatile("s_waitcnt vmcnt(0)" : "+v"(of[8]), "+v"(of[9]), "+v"(of[10]), "+v"(of[11]), "+v"(of[12]), "+v"(of[13]), "+v"(of[14]), "+v"(of[15]) :: "memory");
      RAW_BARRIER();
      if (st == 0) STAMP(2, 3); else STAMP(2, 4);
      u32x4 fx[3], fy[3];
#pragma unroll
      for (int n = 0; n < 10; ++n) {
        if (n < 8) {
          const int i = st * 8 + n, hd = i >> 1, s = i & 1;
          const char* pa = lds + (hd * 28 + 16 * s) * 128;
          const int ra = base + h * 512, rb = (s ? base : base + h * 512) + 1024;
          u32x2 x0 = ldtr(pa + ra), x1 = ldtr(pa + rb), y0 = ldtr(pa + (ra ^ 64)), y1 = ldtr(pa + (rb ^ 64));
          fx[n % 3] = u32x4{x0[0], x0[1], x1[0], x1[1]}; fy[n % 3] = u32x4{y0[0], y0[1], y1[0], y1[1]};
        }
        SB();
        if (n >= 2) { a0 = mfma(fx[(n - 2) % 3], of[st * 8 + n - 2], a0); a1 = mfma(fy[(n - 2) % 3], of[st * 8 + n - 2], a1); }
        SB();
      }
    }
    STAMP(2, 5);
    f32x4* ex = (f32x4*)(lds + 56 * 1024);
    f32x16 snd = a1, kp = a0;
#pragma unroll
    for (int i = 0; i < 4; ++i) ex[(w * 4 + i) * 64 + lane] = f32x4{snd[4 * i], snd[4 * i + 1], snd[4 * i + 2], snd[4 * i + 3]};
    RAW_BARRIER();
#pragma unroll
    for (int i = 0; i < 4; ++i) {
      f32x4 v = ex[((w ^ 2) * 4 + i) * 64 + lane];
      kp[4 * i] += v[0]; kp[4 * i + 1] += v[1]; kp[4 * i + 2] += v[2]; kp[4 * i + 3] += v[3];
    }
    u16* dst = out + ((tq * 2 + qi) * 32 + l31) * 896 + tn * 64 + kh * 32 + 8 * h;
    *(u32x4*)dst = packs<0>(kp); *(u32x4*)(dst + 16) = packs<1>(kp);
  } else {
    const int b3 = xcd * 8 + idx - 4, s2 = b3 >> 1, sh = b3 & 1, r = w;
    u32x4* Dm = (u32x4*)lds;
    u32x4* F64 = (u32x4*)(lds + 61 * 1024);
    float* zs = (float*)lds;
    u32x4 ua[8], ub[8];
    if (__builtin_expect(w < 4, 1)) {
      const u32x4* up = U + s2 * 16 * 232 + (r ? 2 * r - 1 : 0) * 32;
      const unsigned uo = (h * 232 + sw23(l31)) * 16, uo2 = (h * 232 + (sw23(l31) & (r ? 31 : 7)) + (r ? 32 : 224)) * 16;
#pragma unroll
      for (int i = 0; i < 8; ++i) { ua[i] = ald128(up + i * 464, uo); ub[i] = ald128(up + i * 464, uo2); }
    } else {
      const unsigned pl = (h * 32 + sw23(l31)) * 16;
#pragma unroll
      for (int i = 0; i < 4; ++i) dma16(g_tabs.f64 + ((w & 3) * 4 + i) * 256, pl, ldsaddr_of(lds) + 61 * 1024 + ((w & 3) * 4 + i) * 1024);
    }
    SB();
    {
      const int s = s2 + 32 * (32 * sh + l31);
      const float NRM2 = 2.0f / (2048.0f * 448.0f);
      u32x4 dr, di;
#pragma unroll
      for (int i = 0; i < 4; ++i) {
        int n = __mul24(8 * w + 4 * h + i, s) & 2047;
        float c = cosr(n, 1.0f / 2048) * NRM2, sn = sinr(n, 1.0f / 2048) * NRM2;
        dr[i] = pk2(c, -sn); di[i] = pk2(sn, c);
      }
      Dm[(w * 2) * 64 + lane] = dr; Dm[(w * 2 + 1) * 64 + lane] = di;
    }
    STAMP(2, 2);
    RAW_BARRIER();
    STAMP(2, 3);
    if (__builtin_expect(w < 4, 1)) {
      f32x16 ar = {}, ai = {}, br = {}, bi = {};
#pragma unroll
      for (int t = 0; t < 8; ++t) {
        u32x4 dr = Dm[(t * 2) * 64 + lane], di = Dm[(t * 2 + 1) * 64 + lane];
        asm volatile("s_waitcnt vmcnt(%2)" : "+v"(ua[t]), "+v"(ub[t]) : "n"(14 - 2 * t) : "memory");
        ar = mfma(ua[t], dr, ar); ai = mfma(ua[t], di, ai); br = mfma(ub[t], dr, br); bi = mfma(ub[t], di, bi);
      }
      STAMP(2, 4);
      RAW_BARRIER();
      u32x4 yr[4] = {packs<0>(ar), packs<1>(ar), packs<0>(br), packs<1>(br)};
      u32x4 yi[4] = {packs<0>(ai), packs<1>(ai), packs<0>(bi), packs<1>(bi)};
      const unsigned k2 = r ? ~0u : 0u, k1 = (r || !h) ? ~0u : 0u;
      yr[2] &= k1; yr[3] &= k2; yi[2] &= k2; yi[3] &= k2;
      float* zp = zs + ((r ? 2 * r - 1 : 0) * 32 + l31) * ZS + 8 * h;
      float* zq = zp + (r ? 32 * ZS : 0);
      f32x16 zre[2] = {}, zim[2] = {};
      u32x4 C = F64[lane], S = F64[64 + lane];
#pragma unroll
      for (int n = 0; n < 12; ++n) {
        const int at = n >> 2, t = n & 3;
        if (n < 8) {
          const u32x4 Cn = F64[((n + 1) & 7) * 128 + lane], Sn = F64[((n + 1) & 7) * 128 + 64 + lane];
          SB();
          zim[at] = mfma(C, yi[t], zim[at]); zre[at] = mfma(C, yr[t], zre[at]);
          zim[at] = mfma(S, yr[t], zim[at]); zre[at] = mfma(S ^ 0x80008000u, yi[t], zre[at]);
          C = Cn; S = Sn;
          SB();
        }
        if (n >= 4) {
          const int off = (at - 1) * 32 + 16 * (t >> 1) + 4 * (t & 1);
          *(f32x4*)(zq + off) = f32x4{zim[at - 1][4 * t], zim[at - 1][4 * t + 1], zim[at - 1][4 * t + 2], zim[at - 1][4 * t + 3]};
          *(f32x4*)(zp + off) = f32x4{zre[at - 1][4 * t], zre[at - 1][4 * t + 1], zre[at - 1][4 * t + 2], zre[at - 1][4 * t + 3]};
        }
      }
      STAMP(2, 5);
    } else {
      VMCNT(0);
      RAW_BARRIER();
    }
    RAW_BARRIER();
    STAMP(2, 6);
    {
      const int o = tid & 15, row = tid >> 4, rot = ((o >> 1) + 4 * (o & 1)) % 7;
      float* zp = zs + row * ZS + 4 * o;
      f32x4 zl[7];
#pragma unroll
      for (int c = 0; c < 7; ++c) zl[c] = *(const f32x4*)(zp + c * 32 * ZS);
      float y[4][7];
#pragma unroll
      for (int jp = 0; jp < 2; ++jp) {
        f32x2 z[7];
#pragma unroll
        for (int c = 0; c < 7; ++c) z[c] = f32x2{zl[c][2 * jp], zl[c][2 * jp + 1]};
        f32x2 y0 = z[0] + z[1] + z[3] + z[5];
        f32x2 e1 = z[0] + z[1] * C71 + z[3] * C72 + z[5] * C73, o1 = z[2] * S71 + z[4] * S72 + z[6] * S73;
        f32x2 e2 = z[0] + z[1] * C72 + z[3] * C73 + z[5] * C71, o2 = z[2] * S72 - z[4] * S73 - z[6] * S71;
        f32x2 e3 = z[0] + z[1] * C73 + z[3] * C71 + z[5] * C72, o3 = z[2] * S73 - z[4] * S71 + z[6] * S72;
        f32x2 yy[7] = {y0, e1 - o1, e2 - o2, e3 - o3, e3 + o3, e2 + o2, e1 + o1};
#pragma unroll
        for (int b = 0; b < 7; ++b) { y[2 * jp][b] = yy[b][0]; y[2 * jp + 1][b] = yy[b][1]; }
      }
#pragma unroll
      for (int b = 0; b < 7; ++b) {
        int m = b - rot; m += m < 0 ? 7 : 0;
        *(u32x2*)(zp + m * 32 * ZS) = u32x2{pk2(y[0][b], y[1][(b + 1) % 7]), pk2(y[2][(b + 2) % 7], y[3][(b + 3) % 7])};
      }
      u16* dst = out + (s2 + 32 * (32 * sh + row)) * 896 + 448 + 4 * o;
#pragma unroll
      for (int m = 0; m < 7; ++m) *(u32x2*)(dst + 64 * m) = *(const u32x2*)(zp + m * 32 * ZS);
    }
  }
  STAMP(2, 12); STAMP(2, 15);
}

extern "C" void kernel_launch(void* const* d_in, const int* in_sizes, int n_in, void* d_out, int out_size, void* d_ws, size_t ws_size,
                              hipStream_t stream) {
  const u16* ax = (const u16*)d_in[0];
  const u16* wqkv = (const u16*)d_in[1];
  const u16* wo = (const u16*)d_in[2];
  const u16* bx = (const u16*)d_in[3];
  const u16* wr = (const u16*)d_in[4];
  const u16* wi = (const u16*)d_in[5];
  char* ws = (char*)d_ws;
  u32x4* Qf = (u32x4*)(ws);
  u32x4* KVf = (u32x4*)(ws + (2u << 20));
  u32x4* Of = (u32x4*)(ws + (6u << 20));
  unsigned* T = (unsigned*)(ws + (8u << 20));
  unsigned* U = (unsigned*)(ws + (10u << 20));
  (void)hipFuncSetAttribute((const void*)k1_kernel, hipFuncAttributeMaxDynamicSharedMemorySize, K1_LDS);
  (void)hipFuncSetAttribute((const void*)k2_kernel, hipFuncAttributeMaxDynamicSharedMemorySize, K2_LDS);
  (void)hipFuncSetAttribute((const void*)k3_kernel, hipFuncAttributeMaxDynamicSharedMemorySize, K3_LDS);
  k1_kernel<<<256, 512, K1_LDS, stream>>>(ax, wqkv, bx, Qf, KVf, (u32x4*)T);
  k2_kernel<<<256, 384, K2_LDS, stream>>>(Qf, KVf, Of, T, U, wr, wi, wo);
  k3_kernel<<<288, 512, K3_LDS, stream>>>(Of, wo, (const u32x4*)U, (u16*)d_out);
}
```

```cpp
#include <hip/hip_runtime.h>
#include <hip/hip_bf16.h>

typedef __attribute__((ext_vector_type(8))) __bf16 bf16x8;
typedef __attribute__((ext_vector_type(4))) __bf16 bf16x4;
typedef __attribute__((ext_vector_type(2))) __bf16 bf16x2;
typedef __attribute__((ext_vector_type(4))) short s16x4;
typedef __attribute__((ext_vector_type(16))) float f32x16;
typedef __attribute__((ext_vector_type(4))) float f32x4;
typedef __attribute__((ext_vector_type(2))) float f32x2;
typedef __attribute__((ext_vector_type(4))) unsigned u32x4;
typedef __attribute__((ext_vector_type(2))) unsigned u32x2;
typedef unsigned short u16;
#define LDSP __attribute__((address_space(3)))
#define DEV __device__ __forceinline__

DEV unsigned pk2(float lo, float hi) {
  f32x2 v = {lo, hi};
  return __builtin_bit_cast(unsigned, __builtin_convertvector(v, bf16x2));
}
DEV f32x16 mfma(u32x4 a, u32x4 b, f32x16 c) {
  return __builtin_amdgcn_mfma_f32_32x32x16_bf16(__builtin_bit_cast(bf16x8, a), __builtin_bit_cast(bf16x8, b), c, 0, 0, 0);
}
template <int S> DEV u32x4 packs(const f32x16& v) {
  u32x4 r = {pk2(v[8 * S + 0], v[8 * S + 1]), pk2(v[8 * S + 2], v[8 * S + 3]), pk2(v[8 * S + 4], v[8 * S + 5]), pk2(v[8 * S + 6], v[8 * S + 7])};
  return r;
}
DEV int sw23(int r) { return (r & ~12) | ((r & 4) << 1) | ((r & 8) >> 1); }
DEV float cosr(int n, float inv) { return __builtin_amdgcn_cosf((float)n * inv); }
DEV float sinr(int n, float inv) { return __builtin_amdgcn_sinf((float)n * inv); }
DEV void dma16(const void* base, unsigned voff, unsigned ldsaddr) {
  asm volatile("s_mov_b32 m0, %2\n\ts_nop 0\n\tglobal_load_lds_dwordx4 %0, %1" ::"v"(voff), "s"(base), "s"(ldsaddr) : "memory");
}
DEV unsigned ldsaddr_of(const char* p) { return (unsigned)(unsigned long long)(const LDSP char*)p; }
DEV u32x4 ald128(const void* base, unsigned off) {
  u32x4 r; asm volatile("global_load_dwordx4 %0, %1, %2" : "=v"(r) : "v"(off), "s"(base) : "memory"); return r;
}
DEV void st128(void* p, u32x4 v) { asm volatile("global_store_dwordx4 %0, %1, off sc1\n\ts_nop 1" ::"v"(p), "v"(v) : "memory"); }
#define SB() __builtin_amdgcn_sched_barrier(0)
DEV u32x2 ldtr(const char* l) {
  return __builtin_bit_cast(u32x2, __builtin_amdgcn_ds_read_tr16_b64_v4i16((LDSP s16x4*)l));
}
DEV float bflo(unsigned u) { return __builtin_bit_cast(float, u << 16); }
DEV float bfhi(unsigned u) { return __builtin_bit_cast(float, u & 0xffff0000u); }
#define RAW_BARRIER() do { asm volatile("s_waitcnt lgkmcnt(0)" ::: "memory"); __builtin_amdgcn_s_barrier(); asm volatile("" ::: "memory"); } while (0)
#define VMCNT(n) asm volatile("s_waitcnt vmcnt(" #n ")" ::: "memory")

constexpr float C71 = 0.62348980185873353f, C72 = -0.22252093395631440f, C73 = -0.90096886790241913f;
constexpr float S71 = 0.78183148246802981f, S72 = 0.97492791218182361f, S73 = 0.43388373911755812f;

DEV void gen_cs(int i, int k0, int mul, int add, int mask, float inv, u32x4& C, u32x4& S) {
  float c[8], s[8];
#pragma unroll
  for (int j = 0; j < 8; ++j) {
    int n = (__mul24(i, mul * k0 + add) + __mul24(i, mul) * j) & mask;
    c[j] = cosr(n, inv); s[j] = sinr(n, inv);
  }
#pragma unroll
  for (int j = 0; j < 4; ++j) { C[j] = pk2(c[2 * j], c[2 * j + 1]); S[j] = pk2(s[2 * j], s[2 * j + 1]); }
}

constexpr double ccos64(int n) {
  n &= 63; if (n > 32) n = 64 - n;
  double sg = 1.0; if (n > 16) { n = 32 - n; sg = -1.0; }
  if (n == 16) return 0.0;
  double x = 6.283185307179586476925 * n / 64.0, x2 = x * x, term = 1.0, sum = 1.0;
  for (int i = 1; i < 14; ++i) { term *= -x2 / ((2 * i - 1) * (2 * i)); sum += term; }
  return sg * sum;
}
constexpr unsigned cbf(double v) { unsigned u = __builtin_bit_cast(unsigned, (float)v); return (u + 0x7fffu + ((u >> 16) & 1u)) >> 16; }
constexpr int csw(int r) { return (r & ~12) | ((r & 4) << 1) | ((r & 8) >> 1); }
struct alignas(16) Tabs { unsigned f64[16 * 256]; unsigned b2[14 * 256]; };
constexpr Tabs make_tabs() {
  Tabs t{};
  unsigned c[64] = {}, sn[64] = {};
  for (int n = 0; n < 64; ++n) { c[n] = cbf(ccos64(n)); sn[n] = cbf(ccos64(n - 16 + 64)); }
  for (int f = 0; f < 8; ++f)
    for (int l = 0; l < 64; ++l)
      for (int d = 0; d < 4; ++d) {
        int i = 32 * (f >> 2) + (l & 31), k = 16 * (f & 3) + 8 * (l >> 5) + 2 * d;
        t.f64[((f * 2) * 64 + l) * 4 + d] = c[(i * k) & 63] | (c[(i * (k + 1)) & 63] << 16);
        t.f64[((f * 2 + 1) * 64 + l) * 4 + d] = sn[(i * k) & 63] | (sn[(i * (k + 1)) & 63] << 16);
      }
  for (int l = 0; l < 64; ++l)
    for (int d = 0; d < 4; ++d) {
      for (int f = 0; f < 4; ++f) {
        int n = (2 * csw(l & 31) * (8 * f + 4 * (l >> 5) + d)) & 63;
        t.b2[(f * 64 + l) * 4 + d] = c[n] | (sn[n] << 16);
        t.b2[((4 + f) * 64 + l) * 4 + d] = (sn[n] ^ 0x8000u) | (c[n] << 16);
      }
      for (int f = 0; f < 2; ++f) {
        int k = 16 * f + 8 * (l >> 5) + 2 * d, n0 = (2 * (l & 31) * k) & 63, n1 = (2 * (l & 31) * (k + 1)) & 63;
        t.b2[((8 + f) * 64 + l) * 4 + d] = c[n0] | (c[n1] << 16);
        t.b2[((10 + f) * 64 + l) * 4 + d] = sn[n0] | (sn[n1] << 16);
        t.b2[((12 + f) * 64 + l) * 4 + d] = (sn[n0] | (sn[n1] << 16)) ^ 0x80008000u;
      }
    }
  return t;
}
__device__ const Tabs g_tabs = make_tabs();
DEV unsigned ald32(const void* base, unsigned off) {
  unsigned r; asm volatile("global_load_dword %0, %1, %2" : "=v"(r) : "v"(off), "s"(base) : "memory"); return r;
}
DEV unsigned ald16(const void* base, unsigned off) {
  unsigned r; asm volatile("global_load_ushort %0, %1, %2" : "=v"(r) : "v"(off), "s"(base) : "memory"); return r;
}
DEV void ast32(void* base, unsigned off, unsigned v) { asm volatile("global_store_dword %0, %1, %2 sc1" ::"v"(off), "v"(v), "s"(base) : "memory"); }
DEV int col_bin(int c) { int ct = c >> 5, k = ct == 7 ? 7 * (c - 192) : 7 * ((ct ? ((ct + 1) & 1) * 32 : 0) + (c & 31)) + 64 * ((ct + 1) >> 1); return k >= 448 ? k - 448 : k; }

constexpr int K1_SLOT = 32 * 1024;
constexpr int K1_LDS = 128 * 1024;
constexpr unsigned CODE_BYTES = 26000;
struct QF { u32x4 x0[4], x1[4], w[4]; };

template <bool ISV>
DEV void qkv_loop(const char* lds, unsigned ld, const u16* xb, const u16* wb, int kn, unsigned xo, unsigned wo0, unsigned wo1,
                  int xoff, int woff, f32x16& a0, f32x16& a1) {
  auto piece = [&](int slot, int i) {
    const unsigned d = ld + slot * K1_SLOT;
    if (i == 0) dma16(xb + kn * 64, xo, d);
    if (i == 1) dma16(wb + kn * (64 * 1344), wo0, d + 16384);
    if (i == 2) dma16(xb + kn * 64 + 64 * 448, xo, d + 8192);
    if (i == 3) { dma16(wb + kn * (64 * 1344), wo1, d + 24576); kn = kn == 6 ? 0 : kn + 1; }
  };
  auto rd = [&](QF& f, int slot, int ks) {
    const char* sl = lds + slot * K1_SLOT;
    f.x0[ks] = *(const u32x4*)(sl + (xoff ^ (ks * 32)));
    f.x1[ks] = *(const u32x4*)(sl + (xoff ^ (ks * 32)) + 4096);
    u32x2 wa = ldtr(sl + woff + ks * 16 * 224), wc = ldtr(sl + woff + ks * 16 * 224 + 224);
    f.w[ks] = u32x4{wa[0], wa[1], wc[0], wc[1]};
  };
  QF f[2];
#pragma unroll
  for (int kc = 0; kc < 8; ++kc) {
    if (kc < 7) { if (kc < 6) VMCNT(4); else VMCNT(0); __builtin_amdgcn_s_barrier(); asm volatile("" ::: "memory"); }
#pragma unroll
    for (int ks = 0; ks < 4; ++ks) {
      if (kc < 7) rd(f[kc & 1], kc & 3, ks);
      if (kc > 0) {
        QF& g = f[(kc - 1) & 1];
        if (ISV) { a0 = mfma(g.x0[ks], g.w[ks], a0); a1 = mfma(g.x1[ks], g.w[ks], a1); }
        else     { a0 = mfma(g.w[ks], g.x0[ks], a0); a1 = mfma(g.w[ks], g.x1[ks], a1); }
      }
      if (kc < 5) piece((kc + 2) & 3, ks);
      SB();
    }
  }
}

__global__ __launch_bounds__(512, 2) void k1_kernel(const u16* __restrict__ ax, const u16* __restrict__ wqkv, const u16* __restrict__ bx,
                                                    u32x4* __restrict__ Qf, u32x4* __restrict__ KVf, u32x4* __restrict__ T) {
  extern __shared__ __attribute__((aligned(16))) char lds[];
  const int tid = threadIdx.x, lane = tid & 63, w = __builtin_amdgcn_readfirstlane(tid >> 6);
  const int xcd = blockIdx.x & 7, idx = blockIdx.x >> 3;
  const int l31 = lane & 31, h = lane >> 5;
  const unsigned lb = ldsaddr_of(lds);
  unsigned long long pc0;
  asm volatile("s_getpc_b64 %0" : "=s"(pc0));
  const unsigned po = (tid & 255) * 128;
  unsigned pf = ald32(w < 4 ? (const char*)(pc0 & ~127ull) : (const char*)&g_tabs, (idx == 0 && po < (w < 4 ? CODE_BYTES : 30720u)) ? po : 0u);
  if (__builtin_expect(idx >= 8, 1)) {
    const int u = idx - 8, which = u >> 3, tm = (xcd & 3) * 4 + (u & 3), g = (xcd >> 2) * 2 + ((u >> 2) & 1);
    const int row = 8 * w + (lane >> 3);
    const unsigned xo = (row * 448 + ((lane & 7) ^ ((row >> 1) & 7)) * 8) * 2;
    unsigned wo2[2];
#pragma unroll
    for (int i = 0; i < 2; ++i) {
      int q = 64 * (w + 8 * i) + lane; if (q > 895) q = 895;
      int rho = q / 14, cc = q - rho * 14;
      wo2[i] = (((rho & ~7) + ((rho & 7) >> 1) + 4 * (rho & 1)) * 1344 + cc * 8) * 2;
    }
    const u16* xb = ax + tm * 128 * 448;
    const u16* wb = wqkv + which * 448 + g * 112;
    int kn = ((u & 3) * 2 + (u >> 2)) % 7;
#pragma unroll
    for (int i = 0; i < 2; ++i) {
      const unsigned d = lb + w * 1024 + i * K1_SLOT;
      dma16(xb + kn * 64, xo, d); dma16(wb + kn * (64 * 1344), wo2[0], d + 16384);
      dma16(xb + kn * 64 + 64 * 448, xo, d + 8192); dma16(wb + kn * (64 * 1344), wo2[1], d + 24576);
      kn = kn == 6 ? 0 : kn + 1;
    }
    SB();
    const int hh = w & 3, th = w >> 2, r0 = th * 64 + l31;
    const int xoff = r0 * 128 + ((h ^ ((r0 >> 1) & 7)) * 16);
    const int woff = 16384 + (8 * h + 2 * ((lane >> 2) & 3)) * 224 + (hh * 28 + ((lane >> 4) & 1) * 16 + 4 * (lane & 3)) * 2;
    const int head = g * 4 + hh, tile = tm * 4 + th * 2;
    f32x16 a0 = {}, a1 = {};
    u32x4 o0, o1, o2, o3;
    if (which == 2) {
      qkv_loop<true>(lds, lb + w * 1024, xb, wb, kn, xo, wo2[0], wo2[1], xoff, woff, a0, a1);
      o0 = packs<0>(a0); o1 = packs<1>(a0); o2 = packs<0>(a1); o3 = packs<1>(a1);
      if (l31 >= 28) { unsigned f = (l31 == 28) ? 0x3F803F80u : 0u; o0 = u32x4{f, f, f, f}; o1 = o0; o2 = o0; o3 = o0; }
    } else {
      qkv_loop<false>(lds, lb + w * 1024, xb, wb, kn, xo, wo2[0], wo2[1], xoff, woff, a0, a1);
      const float sc = which ? 1.0f : 0.18898223650461362f * 1.4426950408889634f;
      a0 *= sc; a1 *= sc;
      o0 = packs<0>(a0); o1 = packs<1>(a0); o2 = packs<0>(a1); o3 = packs<1>(a1);
      if (h) { o1[2] = which ? 0x3F80u : 0u; o1[3] = 0; o3[2] = o1[2]; o3[3] = 0; }
    }
    asm volatile("" ::"v"(pf));
    u32x4* o = (which == 0 ? Qf + ((head * 64 + tile) * 2) * 64 : KVf + ((head * 64 + tile) * 4 + (which & 2)) * 64) + lane;
    const int st = which == 0 ? 128 : 256;
    st128(o, o0); st128(o + 64, o1); st128(o + st, o2); st128(o + st + 64, o3);
  } else {
    const int b1 = xcd * 8 + idx, s2 = b1 >> 1, hb = b1 & 1;
    u32x4* F64 = (u32x4*)(lds + 56 * 1024);
    u32x4* Ap = (u32x4*)(lds + 72 * 1024);
    u32x4* Xf = (u32x4*)(lds + 88 * 1024);
    {
      const int row = 8 * w + (lane >> 3);
      const unsigned xo = ((s2 + 32 * ((row & 32) + sw23(row & 31))) * 448 + ((lane & 7) ^ ((row >> 1) & 7)) * 8) * 2;
#pragma unroll
      for (int m = 0; m < 7; ++m) dma16(bx + 64 * m, xo, lb + m * 8192 + w * 1024);
      dma16(g_tabs.f64 + w * 512, lane * 16, lb + 56 * 1024 + w * 2048); dma16(g_tabs.f64 + w * 512 + 256, lane * 16, lb + 57 * 1024 + w * 2048);
    }
    SB();
    const int mt = w >> 2, o = 2 * (w & 3) + h;
    {
      u32x4 C, S;
      gen_cs(32 * mt + l31, 8 * o, 32, s2, 2047, 1.0f / 2048, C, S);
      Ap[(w * 2 + 0) * 64 + lane] = C; Ap[(w * 2 + 1) * 64 + lane] = S;
    }
    char* xp = lds + (32 * mt + l31) * 128 + ((o ^ ((l31 >> 1) & 7)) * 16);
    int xm[7];
#pragma unroll
    for (int b = 0; b < 7; ++b) { int m = b - o; m += m < 0 ? 7 : 0; m += m < 0 ? 7 : 0; xm[b] = m * 8192; asm volatile("" : "+v"(xm[b])); }
    asm volatile("s_waitcnt vmcnt(2)" : "+v"(pf) :: "memory");
    RAW_BARRIER();
    {
      u32x4 xr[7], z[4];
#pragma unroll
      for (int b = 0; b < 7; ++b) xr[b] = *(const u32x4*)(xp + xm[b]);
      const float ca = hb ? C72 : C71, cb = hb ? C73 : C72, cc = hb ? C71 : C73, sa = hb ? S72 : S71, sb = hb ? -S73 : S72, sc = hb ? -S71 : S73;
      const float da = hb ? C73 : 1.0f, db = hb ? C71 : 1.0f, dc = hb ? C72 : 1.0f, ta = hb ? S73 : 0.0f, tb = hb ? -S71 : 0.0f, tc = hb ? S72 : 0.0f;
#pragma unroll
      for (int jp = 0; jp < 4; ++jp) {
        f32x2 v[7];
#pragma unroll
        for (int b = 0; b < 7; ++b) {
          v[b][0] = bflo(xr[(b - 2 * jp + 14) % 7][jp]);
          v[b][1] = bfhi(xr[(b - 2 * jp - 1 + 14) % 7][jp]);
        }
        f32x2 p1 = v[1] + v[6], p2 = v[2] + v[5], p3 = v[3] + v[4];
        f32x2 m1 = v[6] - v[1], m2 = v[5] - v[2], m3 = v[4] - v[3];
        f32x2 ra = v[0] + p1 * ca + p2 * cb + p3 * cc, ia = m1 * sa + m2 * sb + m3 * sc;
        f32x2 rb = v[0] + p1 * da + p2 * db + p3 * dc, ib = m1 * ta + m2 * tb + m3 * tc;
        z[0][jp] = pk2(ra[0], ra[1]); z[1][jp] = pk2(ia[0], ia[1]); z[2][jp] = pk2(rb[0], rb[1]); z[3][jp] = pk2(ib[0], ib[1]);
      }
#pragma unroll
      for (int c = 0; c < 4; ++c) *(u32x4*)(xp + c * 8192) = z[c];
    }
    VMCNT(0);
    RAW_BARRIER();
    const int wc = w & 3, ct = hb ? 3 + wc : (wc == 2 ? 0 : (wc == 3 ? 7 : wc + 1)), kt = w & 1;
    u32x4 ap[8];
    {
      f32x16 xre = {}, xim = {};
      const char* zp = lds + (w & 2) * 8192 + mt * 4096 + l31 * 128;
      const int zsw = (h ^ ((l31 >> 1) & 7)) * 16;
      u32x4 C[4], S[4], zr[4], zi[4];
#pragma unroll
      for (int tt = 0; tt < 4; ++tt) {
        zr[tt] = *(const u32x4*)(zp + (zsw ^ (tt * 32))); C[tt] = F64[((kt * 4 + tt) * 2) * 64 + lane];
        zi[tt] = *(const u32x4*)(zp + 8192 + (zsw ^ (tt * 32))); S[tt] = F64[((kt * 4 + tt) * 2 + 1) * 64 + lane];
      }
#pragma unroll
      for (int tt = 0; tt < 8; ++tt) ap[tt] = Ap[(mt * 8 + tt) * 64 + lane];
      SB();
#pragma unroll
      for (int tt = 0; tt < 4; ++tt) {
        xre = mfma(zr[tt], C[tt], xre); xim = mfma(zi[tt], C[tt], xim);
        xre = mfma(zi[tt], S[tt], xre); xim = mfma(zr[tt], S[tt] ^ 0x80008000u, xim);
      }
      u32x4* xq = Xf + (wc * 4 + mt * 2) * 128 + lane;
      xq[0] = packs<0>(xre); xq[64] = packs<0>(xim); xq[128] = packs<1>(xre); xq[192] = packs<1>(xim);
    }
    RAW_BARRIER();
    {
      f32x16 tre = {}, tim = {};
      u32x4 bf[8];
#pragma unroll
      for (int tt = 0; tt < 8; ++tt) bf[tt] = Xf[(wc * 8 + tt) * 64 + lane];
      SB();
#pragma unroll
      for (int tt = 0; tt < 4; ++tt) {
        tre = mfma(ap[2 * tt], bf[2 * tt], tre); tim = mfma(ap[2 * tt], bf[2 * tt + 1], tim);
        tre = mfma(ap[2 * tt + 1], bf[2 * tt + 1], tre); tim = mfma(ap[2 * tt + 1] ^ 0x80008000u, bf[2 * tt], tim);
      }
      u32x4* tp = T + (s2 * 16 + mt * 8 + h) * 232 + ct * 32 + l31;
      if (ct < 7 || l31 < 8)
#pragma unroll
      for (int q = 0; q < 4; ++q)
        st128(tp + 2 * q * 232, u32x4{pk2(tre[4 * q], tim[4 * q]), pk2(tre[4 * q + 1], tim[4 * q + 1]), pk2(tre[4 * q + 2], tim[4 * q + 2]), pk2(tre[4 * q + 3], tim[4 * q + 3])});
    }
  }
}

constexpr int K2_LDS = 84 * 1024;
DEV float xhalf_max(float v) {
  float a = v, b = v;
  asm volatile("s_nop 1\n\tv_permlane32_swap_b32 %0, %1\n\ts_nop 1" : "+v"(a), "+v"(b));
  return fmaxf(a, b);
}
DEV float max16(const f32x16& s) {
  float m = fmaxf(fmaxf(s[0], s[1]), s[2]);
#pragma unroll
  for (int i = 3; i < 15; i += 2) m = fmaxf(fmaxf(m, s[i]), s[i + 1]);
  return fmaxf(m, s[15]);
}
#define PIN16(a) asm volatile("" : "+v"(a[0]), "+v"(a[1]), "+v"(a[2]), "+v"(a[3]), "+v"(a[4]), "+v"(a[5]), "+v"(a[6]), "+v"(a[7]), \
                                   "+v"(a[8]), "+v"(a[9]), "+v"(a[10]), "+v"(a[11]), "+v"(a[12]), "+v"(a[13]), "+v"(a[14]), "+v"(a[15]))
#define EX4(s, i, P, j)                                                                                          \
  { const float t0_ = __builtin_amdgcn_exp2f(s[i]); SB(); P[j] = pk2(e0, e1); SB();                               \
    const float t1_ = __builtin_amdgcn_exp2f(s[i + 1]); SB(); P[j + 1] = pk2(e2, e3); SB();                       \
    e0 = t0_; e1 = t1_; e2 = __builtin_amdgcn_exp2f(s[i + 2]); SB(); e3 = __builtin_amdgcn_exp2f(s[i + 3]); SB(); }
#define TILE(sc, sn, K0, K1, Q0, Q1, oc, V0, op, V1, L0, L1, L2, L3)                 \
  sn = mfma(K0, Q0, f32x16{}); SB(); L0; EX4(sc, 0, p1, 2)                           \
  op = mfma(V1, p1, op); SB(); L1; EX4(sc, 4, p0, 0)                                 \
  sn = mfma(K1, Q1, sn); SB(); L2; EX4(sc, 8, p0, 2)                                 \
  oc = mfma(V0, p0, oc); SB(); L3; EX4(sc, 12, p1, 0)
#define KTILE(X, Y, P1, P2)                                                                                      \
  TILE(sa, sb, X##k0, X##k1, q0[1], q1[1], o[0], X##v0, o[3], Y##v1, , Y##v1 = (P1)[192], , )                     \
  TILE(sb, sa, X##k0, X##k1, q0[2], q1[2], o[1], X##v0, o[0], X##v1, , , , )                                      \
  TILE(sa, sb, X##k0, X##k1, q0[3], q1[3], o[2], X##v0, o[1], X##v1, X##k0 = (P2)[0], , X##k1 = (P2)[64], )       \
  TILE(sb, sa, Y##k0, Y##k1, q0[0], q1[0], o[3], X##v0, o[2], X##v1, , , , X##v0 = (P2)[128])

__global__ __launch_bounds__(384, 2) void k2_kernel(const u32x4* __restrict__ Qf, const u32x4* __restrict__ KVf, u32x4* __restrict__ Of,
                                                    const unsigned* __restrict__ T, unsigned* __restrict__ U,
                                                    const u16* __restrict__ Wr, const u16* __restrict__ Wi, const u16* __restrict__ wo) {
  extern __shared__ __attribute__((aligned(16))) char lds[];
  const int tid = threadIdx.x, lane = tid & 63, w = __builtin_amdgcn_readfirstlane(tid >> 6);
  const int xcd = blockIdx.x & 7, idx = blockIdx.x >> 3;
  const int l31 = lane & 31, h = lane >> 5;
  if (__builtin_expect(w < 4, 1)) {
    __builtin_amdgcn_s_setprio(2);
    const int head = xcd * 2 + (idx >> 4), qt = (idx & 15) * 4;
    const u32x4* kp = KVf + (head * 64 + w * 16) * 256 + lane;
    u32x4 q0[4], q1[4];
    u32x4 ek0 = kp[0], ek1 = kp[64];
#pragma unroll
    for (int i = 0; i < 4; ++i) { const u32x4* qp = Qf + (head * 64 + qt + ((w + i) & 3)) * 128 + lane; q0[i] = qp[0]; q1[i] = qp[64]; }
    u32x4 ev0 = kp[128], ev1 = kp[192], dk0 = kp[256], dk1 = kp[320], dv0 = kp[384], dv1 = {}, p0, p1 = {};
    f32x16 o[4] = {}, sa, sb;
    float m[4], e0 = 0.f, e1 = 0.f, e2 = 0.f, e3 = 0.f;
    {
      f32x16 s4[4];
#pragma unroll
      for (int i = 0; i < 4; ++i) { s4[i] = mfma(ek0, q0[i], f32x16{}); s4[i] = mfma(ek1, q1[i], s4[i]); }
#pragma unroll
      for (int i = 0; i < 4; ++i) {
        const unsigned nm = pk2(-xhalf_max(max16(s4[i])), 0.0f);
        m[i] = -bflo(nm);
        if (h) q1[i][2] = nm;
      }
      sa = s4[0] - m[0];
    }
#pragma unroll 1
    for (int it = 0; it < 8; ++it) {
      const u32x4* kn = kp + ((2 * it + 2) & 15) * 256;
      KTILE(e, d, kp + (2 * it + 1) * 256, kn)
      KTILE(d, e, kn, kn + 256)
    }
    p1[2] = pk2(e0, e1); p1[3] = pk2(e2, e3);
    o[3] = mfma(dv1, p1, o[3]);
    if (__builtin_expect(__ballot(h && !(o[0][12] + o[1][12] + o[2][12] + o[3][12] < 1.0e30f)) != 0ull, 0)) {
#pragma unroll
      for (int u = 0; u < 4; ++u) {
        f32x16 oo = {}; float mm = -3.0e38f;
        if (h) q1[u][2] = 0;
#pragma unroll 1
        for (int t = 0; t < 16; ++t) {
          const u32x4* p = kp + t * 256;
          f32x16 s = mfma(p[0], q0[u], f32x16{}); s = mfma(p[64], q1[u], s);
          float mn = fmaxf(mm, xhalf_max(max16(s)));
          float al = __builtin_amdgcn_exp2f(mm - mn);
          mm = mn;
#pragma unroll
          for (int i = 0; i < 16; ++i) { oo[i] *= al; s[i] = __builtin_amdgcn_exp2f(s[i] - mn); }
          oo = mfma(p[128], packs<0>(s), oo); oo = mfma(p[192], packs<1>(s), oo);
        }
        o[u] = oo; m[u] = mm;
      }
    }
#pragma unroll
    for (int u = 1; u < 4; ++u) {
      if (h) o[u][13] = m[u];
      f32x4* ex = (f32x4*)(lds + (((w + u) & 3) * 3 + u - 1) * 4096) + lane;
#pragma unroll
      for (int i = 0; i < 4; ++i) ex[i * 64] = f32x4{o[u][4 * i], o[u][4 * i + 1], o[u][4 * i + 2], o[u][4 * i + 3]};
    }
    const float l0 = xhalf_max(h ? o[0][12] : -1.0f);
    RAW_BARRIER();
    const char* ey = lds + w * 12288;
    f32x2 st[3];
#pragma unroll
    for (int u = 0; u < 3; ++u) st[u] = *(const f32x2*)(ey + u * 4096 + 3584 + l31 * 16);
    f32x4 y[12];
#pragma unroll
    for (int i = 0; i < 12; ++i) y[i] = *(const f32x4*)(ey + i * 1024 + lane * 16);
    SB();
    const float mm = fmaxf(fmaxf(st[0][1], st[1][1]), fmaxf(st[2][1], m[0]));
    float f0 = __builtin_amdgcn_exp2f(m[0] - mm), f[3];
#pragma unroll
    for (int u = 0; u < 3; ++u) f[u] = __builtin_amdgcn_exp2f(st[u][1] - mm);
    const float inv = __builtin_amdgcn_rcpf(l0 * f0 + st[0][0] * f[0] + st[1][0] * f[1] + st[2][0] * f[2]);
    f0 *= inv;
    f32x16 r = o[0] * f0;
    asm volatile("" : "+v"(r) : "v"(f0));
#pragma unroll
    for (int u = 0; u < 3; ++u) {
      const float fu = f[u] * inv;
#pragma unroll
      for (int i = 0; i < 4; ++i) {
        const f32x4 v = y[u * 4 + i];
        r[4 * i] += v[0] * fu; r[4 * i + 1] += v[1] * fu; r[4 * i + 2] += v[2] * fu; r[4 * i + 3] += v[3] * fu;
      }
    }
    u32x4 r1 = packs<1>(r);
    if (h) { r1[2] = 0; r1[3] = 0; }
    u32x4* op = Of + (((qt + w) * 16 + head) * 2) * 64 + lane;
    st128(op, packs<0>(r)); st128(op + 64, r1);
  } else if (idx < 29) {
    const int jq = xcd * 2 + w - 4, col = idx * 8 + (l31 >> 2), kb = col_bin(col), jr = 4 * jq + (lane & 3) + 512 * h;
    const unsigned toff = (4 * h * 16 + jq) * 928 + idx * 32 + l31;
    const bool cj = kb > 224;
    const int of = jr * 450 + 2 * kb, om = (2048 - jr) * 450 + 2 * (448 - kb), om0 = jr ? om : om - 921600;
    const float sr = col > 224 ? 0.0f : ((col == 0 || col == 224) ? 0.5f : 1.0f), si = cj ? -sr : sr;
    u32x4 mt[14];
    unsigned ta[16], ur[16], ui[16]; float wr[16], wi[16];
#pragma unroll
    for (int i = 0; i < 16; ++i) ta[i] = ald32(T + (8 * (i >> 2) + (i & 3)) * 14848, toff * 4);
#pragma unroll
    for (int g = 0; g < 16; ++g) {
      const unsigned o_ = g ? (cj ? om : of) + ((g & 7) + 16 * (g >> 3)) * (cj ? -28800 : 28800) : (cj ? om0 : of);
      ur[g] = ald16(Wr, o_); ui[g] = ald16(Wi, o_);
    }
    const unsigned* tabp = g_tabs.b2;
    asm volatile("" : "+s"(tabp));
#pragma unroll
    for (int t = 0; t < 14; ++t) mt[t] = ald128(tabp + t * 256, lane * 16);
    asm volatile("s_waitcnt vmcnt(0)" : "+v"(mt[0]), "+v"(mt[1]), "+v"(mt[2]), "+v"(mt[3]), "+v"(mt[4]), "+v"(mt[5]), "+v"(mt[6]), "+v"(mt[7]), "+v"(mt[8]),
                 "+v"(mt[9]), "+v"(mt[10]), "+v"(mt[11]), "+v"(mt[12]), "+v"(mt[13]) :: "memory");
    PIN16(ta); PIN16(ur); PIN16(ui);
#pragma unroll
    for (int g = 0; g < 16; ++g) { wr[g] = bflo(ur[g]); wi[g] = bflo(ui[g]); }
    SB();
    f32x16 xr = {}, xi = {};
#pragma unroll
    for (int t = 0; t < 4; ++t) {
      u32x4 b = {ta[4 * t], ta[4 * t + 1], ta[4 * t + 2], ta[4 * t + 3]};
      xr = mfma(mt[t], b, xr); xi = mfma(mt[4 + t], b, xi);
    }
    f32x16 yr, yi;
#pragma unroll
    for (int g = 0; g < 16; g += 2) {
      f32x2 c = {wr[g], wr[g + 1]}, d = {wi[g], wi[g + 1]};
      asm volatile("" : "+v"(c), "+v"(d));
      c *= sr; d *= si;
      const f32x2 a = {xr[g], xr[g + 1]}, b = {xi[g], xi[g + 1]}, r = a * c - b * d, i = a * d + b * c;
      yr[g] = r[0]; yr[g + 1] = r[1]; yi[g] = i[0]; yi[g + 1] = i[1];
    }
    u32x4 r0 = packs<0>(yr), r1 = packs<1>(yr), i0 = packs<0>(yi), i1 = packs<1>(yi);
    f32x16 ure = {}, uim = {};
    ure = mfma(mt[8], r0, ure); uim = mfma(mt[8], i0, uim);
    ure = mfma(mt[12], i0, ure); uim = mfma(mt[10], r0, uim);
    ure = mfma(mt[9], r1, ure); uim = mfma(mt[9], i1, uim);
    ure = mfma(mt[13], i1, ure); uim = mfma(mt[11], r1, uim);
#pragma unroll
    for (int g = 0; g < 16; ++g) ast32(U + ((g & 3) + 8 * (g >> 2)) * 14848, toff * 4, pk2(ure[g], uim[g]));
    {
      const unsigned off = ((idx * 2 + w - 4) * 64 + lane) * 128;
      if (off < 401408u) { unsigned pf = *(const unsigned*)((const char*)wo + off); asm volatile("" ::"v"(pf)); }
    }
  }
}

constexpr int ZS = 68;
constexpr int K3_LDS = 77 * 1024;

__global__ __launch_bounds__(512, 4) void k3_kernel(const u32x4* __restrict__ Of, const u16* __restrict__ wo, const u32x4* __restrict__ U,
                                                    u16* __restrict__ out) {
  extern __shared__ __attribute__((aligned(16))) char lds[];
  const int tid = threadIdx.x, lane = tid & 63, w = __builtin_amdgcn_readfirstlane(tid >> 6);
  const int xcd = blockIdx.x & 7, idx = blockIdx.x >> 3;
  const int l31 = lane & 31, h = lane >> 5;
  if (idx < 4 || idx >= 12) {
    if (w >= 4) return;
    const int u = idx < 4 ? idx : idx - 8, tq = xcd * 4 + u / 7, tn = u % 7;
    const int qi = w & 1, kh = w >> 1;
    u32x4 of[16];
    {
      const int r8 = lane >> 3;
      const unsigned so = (r8 * 448 + ((lane & 7) ^ ((r8 & 2) << 1)) * 8) * 2, l16 = lane * 16;
      const u16* src = wo + (kh * 224 + qi * 8) * 448 + tn * 64;
      const unsigned d = ldsaddr_of(lds) + (kh * 224 + qi * 8) * 128;
      const u32x4* op = Of + (((tq * 2 + qi) * 16 + kh * 8) * 2) * 64;
#pragma unroll
      for (int st = 0; st < 2; ++st) {
#pragma unroll
        for (int i = 0; i < 7; ++i) dma16(src + (st * 7 + i) * 16 * 448, so, d + (st * 7 + i) * 2048);
#pragma unroll
        for (int i = 0; i < 8; ++i) of[st * 8 + i] = ald128(op + (st * 8 + i) * 64, l16);
      }
    }
    const int q = (lane >> 2) & 3, p = lane & 3, pc = ((p & 1) << 1) | (p >> 1);
    const int colb = ((lane >> 4) & 1) * 32 + pc * 8;
    const int base = (kh * 224 + q) * 128 + (colb ^ (((q >> 1) ^ kh) << 6));
    f32x16 a0 = {}, a1 = {};
#pragma unroll
    for (int st = 0; st < 2; ++st) {
      if (st == 0) asm volatile("s_waitcnt vmcnt(15)" : "+v"(of[0]), "+v"(of[1]), "+v"(of[2]), "+v"(of[3]), "+v"(of[4]), "+v"(of[5]), "+v"(of[6]), "+v"(of[7]) :: "memory");
      else asm volatile("s_waitcnt vmcnt(0)" : "+v"(of[8]), "+v"(of[9]), "+v"(of[10]), "+v"(of[11]), "+v"(of[12]), "+v"(of[13]), "+v"(of[14]), "+v"(of[15]) :: "memory");
      RAW_BARRIER();
      u32x4 fx[3], fy[3];
#pragma unroll
      for (int n = 0; n < 10; ++n) {
        if (n < 8) {
          const int i = st * 8 + n, hd = i >> 1, s = i & 1;
          const char* pa = lds + (hd * 28 + 16 * s) * 128;
          const int ra = base + h * 512, rb = (s ? base : base + h * 512) + 1024;
          u32x2 x0 = ldtr(pa + ra), x1 = ldtr(pa + rb), y0 = ldtr(pa + (ra ^ 64)), y1 = ldtr(pa + (rb ^ 64));
          fx[n % 3] = u32x4{x0[0], x0[1], x1[0], x1[1]}; fy[n % 3] = u32x4{y0[0], y0[1], y1[0], y1[1]};
        }
        SB();
        if (n >= 2) { a0 = mfma(fx[(n - 2) % 3], of[st * 8 + n - 2], a0); a1 = mfma(fy[(n - 2) % 3], of[st * 8 + n - 2], a1); }
        SB();
      }
    }
    f32x4* ex = (f32x4*)(lds + 56 * 1024);
    f32x16 snd = a1, kp = a0;
#pragma unroll
    for (int i = 0; i < 4; ++i) ex[(w * 4 + i) * 64 + lane] = f32x4{snd[4 * i], snd[4 * i + 1], snd[4 * i + 2], snd[4 * i + 3]};
    RAW_BARRIER();
#pragma unroll
    for (int i = 0; i < 4; ++i) {
      f32x4 v = ex[((w ^ 2) * 4 + i) * 64 + lane];
      kp[4 * i] += v[0]; kp[4 * i + 1] += v[1]; kp[4 * i + 2] += v[2]; kp[4 * i + 3] += v[3];
    }
    u16* dst = out + ((tq * 2 + qi) * 32 + l31) * 896 + tn * 64 + kh * 32 + 8 * h;
    st128(dst, packs<0>(kp)); st128(dst + 16, packs<1>(kp));
  } else {
    const int b3 = xcd * 8 + idx - 4, s2 = b3 >> 1, sh = b3 & 1, r = w;
    u32x4* Dm = (u32x4*)lds;
    u32x4* F64 = (u32x4*)(lds + 61 * 1024);
    float* zs = (float*)lds;
    u32x4 ua[8], ub[8];
    if (__builtin_expect(w < 4, 1)) {
      const u32x4* up = U + s2 * 16 * 232 + (r ? 2 * r - 1 : 0) * 32;
      const unsigned uo = (h * 232 + sw23(l31)) * 16, uo2 = (h * 232 + (sw23(l31) & (r ? 31 : 7)) + (r ? 32 : 224)) * 16;
#pragma unroll
      for (int i = 0; i < 8; ++i) { ua[i] = ald128(up + i * 464, uo); ub[i] = ald128(up + i * 464, uo2); }
    } else {
      const unsigned pl = (h * 32 + sw23(l31)) * 16;
#pragma unroll
      for (int i = 0; i < 4; ++i) dma16(g_tabs.f64 + ((w & 3) * 4 + i) * 256, pl, ldsaddr_of(lds) + 61 * 1024 + ((w & 3) * 4 + i) * 1024);
    }
    SB();
    {
      const int s = s2 + 32 * (32 * sh + l31);
      const float NRM2 = 2.0f / (2048.0f * 448.0f);
      u32x4 dr, di;
#pragma unroll
      for (int i = 0; i < 4; ++i) {
        int n = __mul24(8 * w + 4 * h + i, s) & 2047;
        float c = cosr(n, 1.0f / 2048) * NRM2, sn = sinr(n, 1.0f / 2048) * NRM2;
        dr[i] = pk2(c, -sn); di[i] = pk2(sn, c);
      }
      Dm[(w * 2) * 64 + lane] = dr; Dm[(w * 2 + 1) * 64 + lane] = di;
    }
    RAW_BARRIER();
    if (__builtin_expect(w < 4, 1)) {
      f32x16 ar = {}, ai = {}, br = {}, bi = {};
#pragma unroll
      for (int t = 0; t < 8; ++t) {
        u32x4 dr = Dm[(t * 2) * 64 + lane], di = Dm[(t * 2 + 1) * 64 + lane];
        asm volatile("s_waitcnt vmcnt(%2)" : "+v"(ua[t]), "+v"(ub[t]) : "n"(14 - 2 * t) : "memory");
        ar = mfma(ua[t], dr, ar); ai = mfma(ua[t], di, ai); br = mfma(ub[t], dr, br); bi = mfma(ub[t], di, bi);
      }
      RAW_BARRIER();
      u32x4 yr[4] = {packs<0>(ar), packs<1>(ar), packs<0>(br), packs<1>(br)};
      u32x4 yi[4] = {packs<0>(ai), packs<1>(ai), packs<0>(bi), packs<1>(bi)};
      const unsigned k2 = r ? ~0u : 0u, k1 = (r || !h) ? ~0u : 0u;
      yr[2] &= k1; yr[3] &= k2; yi[2] &= k2; yi[3] &= k2;
      float* zp = zs + ((r ? 2 * r - 1 : 0) * 32 + l31) * ZS + 8 * h;
      float* zq = zp + (r ? 32 * ZS : 0);
      f32x16 zre[2] = {}, zim[2] = {};
      u32x4 C = F64[lane], S = F64[64 + lane];
#pragma unroll
      for (int n = 0; n < 12; ++n) {
        const int at = n >> 2, t = n & 3;
        if (n < 8) {
          const u32x4 Cn = F64[((n + 1) & 7) * 128 + lane], Sn = F64[((n + 1) & 7) * 128 + 64 + lane];
          SB();
          zim[at] = mfma(C, yi[t], zim[at]); zre[at] = mfma(C, yr[t], zre[at]);
          zim[at] = mfma(S, yr[t], zim[at]); zre[at] = mfma(S ^ 0x80008000u, yi[t], zre[at]);
          C = Cn; S = Sn;
          SB();
        }
        if (n >= 4) {
          const int off = (at - 1) * 32 + 16 * (t >> 1) + 4 * (t & 1);
          *(f32x4*)(zq + off) = f32x4{zim[at - 1][4 * t], zim[at - 1][4 * t + 1], zim[at - 1][4 * t + 2], zim[at - 1][4 * t + 3]};
          *(f32x4*)(zp + off) = f32x4{zre[at - 1][4 * t], zre[at - 1][4 * t + 1], zre[at - 1][4 * t + 2], zre[at - 1][4 * t + 3]};
        }
      }
    } else {
      VMCNT(0);
      RAW_BARRIER();
    }
    RAW_BARRIER();
    {
      const int o = tid & 15, row = tid >> 4, rot = ((o >> 1) + 4 * (o & 1)) % 7;
      float* zp = zs + row * ZS + 4 * o;
      f32x4 zl[7];
#pragma unroll
      for (int c = 0; c < 7; ++c) zl[c] = *(const f32x4*)(zp + c * 32 * ZS);
      float y[4][7];
#pragma unroll
      for (int jp = 0; jp < 2; ++jp) {
        f32x2 z[7];
#pragma unroll
        for (int c = 0; c < 7; ++c) z[c] = f32x2{zl[c][2 * jp], zl[c][2 * jp + 1]};
        f32x2 y0 = z[0] + z[1] + z[3] + z[5];
        f32x2 e1 = z[0] + z[1] * C71 + z[3] * C72 + z[5] * C73, o1 = z[2] * S71 + z[4] * S72 + z[6] * S73;
        f32x2 e2 = z[0] + z[1] * C72 + z[3] * C73 + z[5] * C71, o2 = z[2] * S72 - z[4] * S73 - z[6] * S71;
        f32x2 e3 = z[0] + z[1] * C73 + z[3] * C71 + z[5] * C72, o3 = z[2] * S73 - z[4] * S71 + z[6] * S72;
        f32x2 yy[7] = {y0, e1 - o1, e2 - o2, e3 - o3, e3 + o3, e2 + o2, e1 + o1};
#pragma unroll
        for (int b = 0; b < 7; ++b) { y[2 * jp][b] = yy[b][0]; y[2 * jp + 1][b] = yy[b][1]; }
      }
#pragma unroll
      for (int b = 0; b < 7; ++b) {
        int m = b - rot; m += m < 0 ? 7 : 0;
        *(u32x2*)(zp + m * 32 * ZS) = u32x2{pk2(y[0][b], y[1][(b + 1) % 7]), pk2(y[2][(b + 2) % 7], y[3][(b + 3) % 7])};
      }
      u16* dst = out + (s2 + 32 * (32 * sh + row)) * 896 + 448 + 4 * o;
#pragma unroll
      for (int m = 0; m < 7; ++m) { const u32x2 v_ = *(const u32x2*)(zp + m * 32 * ZS); asm volatile("global_store_dwordx2 %0, %1, off sc1" ::"v"(dst + 64 * m), "v"(v_) : "memory"); }
    }
  }
}

extern "C" void kernel_launch(void* const* d_in, const int* in_sizes, int n_in, void* d_out, int out_size, void* d_ws, size_t ws_size,
                              hipStream_t stream) {
  const u16* ax = (const u16*)d_in[0];
  const u16* wqkv = (const u16*)d_in[1];
  const u16* wo = (const u16*)d_in[2];
  const u16* bx = (const u16*)d_in[3];
  const u16* wr = (const u16*)d_in[4];
  const u16* wi = (const u16*)d_in[5];
  char* ws = (char*)d_ws;
  u32x4* Qf = (u32x4*)(ws);
  u32x4* KVf = (u32x4*)(ws + (2u << 20));
  u32x4* Of = (u32x4*)(ws + (6u << 20));
  unsigned* T = (unsigned*)(ws + (8u << 20));
  unsigned* U = (unsigned*)(ws + (10u << 20));
  (void)hipFuncSetAttribute((const void*)k1_kernel, hipFuncAttributeMaxDynamicSharedMemorySize, K1_LDS);
  (void)hipFuncSetAttribute((const void*)k2_kernel, hipFuncAttributeMaxDynamicSharedMemorySize, K2_LDS);
  (void)hipFuncSetAttribute((const void*)k3_kernel, hipFuncAttributeMaxDynamicSharedMemorySize, K3_LDS);
  k1_kernel<<<256, 512, K1_LDS, stream>>>(ax, wqkv, bx, Qf, KVf, (u32x4*)T);
  k2_kernel<<<256, 384, K2_LDS, stream>>>(Qf, KVf, Of, T, U, wr, wi, wo);
  k3_kernel<<<288, 512, K3_LDS, stream>>>(Of, wo, (const u32x4*)U, (u16*)d_out);
}
```

```cpp
#include <hip/hip_runtime.h>
#include <hip/hip_bf16.h>

typedef __attribute__((ext_vector_type(8))) __bf16 bf16x8;
typedef __attribute__((ext_vector_type(4))) __bf16 bf16x4;
typedef __attribute__((ext_vector_type(2))) __bf16 bf16x2;
typedef __attribute__((ext_vector_type(4))) short s16x4;
typedef __attribute__((ext_vector_type(16))) float f32x16;
typedef __attribute__((ext_vector_type(4))) float f32x4;
typedef __attribute__((ext_vector_type(2))) float f32x2;
typedef __attribute__((ext_vector_type(4))) unsigned u32x4;
typedef __attribute__((ext_vector_type(2))) unsigned u32x2;
typedef unsigned short u16;
#define LDSP __attribute__((address_space(3)))
#define DEV __device__ __forceinline__

DEV unsigned pk2(float lo, float hi) {
  f32x2 v = {lo, hi};
  return __builtin_bit_cast(unsigned, __builtin_convertvector(v, bf16x2));
}
DEV f32x16 mfma(u32x4 a, u32x4 b, f32x16 c) {
  return __builtin_amdgcn_mfma_f32_32x32x16_bf16(__builtin_bit_cast(bf16x8, a), __builtin_bit_cast(bf16x8, b), c, 0, 0, 0);
}
template <int S> DEV u32x4 packs(const f32x16& v) {
  u32x4 r = {pk2(v[8 * S + 0], v[8 * S + 1]), pk2(v[8 * S + 2], v[8 * S + 3]), pk2(v[8 * S + 4], v[8 * S + 5]), pk2(v[8 * S + 6], v[8 * S + 7])};
  return r;
}
DEV int sw23(int r) { return (r & ~12) | ((r & 4) << 1) | ((r & 8) >> 1); }
DEV float cosr(int n, float inv) { return __builtin_amdgcn_cosf((float)n * inv); }
DEV float sinr(int n, float inv) { return __builtin_amdgcn_sinf((float)n * inv); }
DEV void dma16(const void* base, unsigned voff, unsigned ldsaddr) {
  asm volatile("s_mov_b32 m0, %2\n\ts_nop 0\n\tglobal_load_lds_dwordx4 %0, %1" ::"v"(voff), "s"(base), "s"(ldsaddr) : "memory");
}
DEV unsigned ldsaddr_of(const char* p) { return (unsigned)(unsigned long long)(const LDSP char*)p; }
DEV u32x4 ald128(const void* base, unsigned off) {
  u32x4 r; asm volatile("global_load_dwordx4 %0, %1, %2" : "=v"(r) : "v"(off), "s"(base) : "memory"); return r;
}
DEV void st128(void* p, u32x4 v) { asm volatile("global_store_dwordx4 %0, %1, off sc1\n\ts_nop 1" ::"v"(p), "v"(v) : "memory"); }
#define SB() __builtin_amdgcn_sched_barrier(0)
DEV u32x2 ldtr(const char* l) {
  return __builtin_bit_cast(u32x2, __builtin_amdgcn_ds_read_tr16_b64_v4i16((LDSP s16x4*)l));
}
DEV float bflo(unsigned u) { return __builtin_bit_cast(float, u << 16); }
DEV float bfhi(unsigned u) { return __builtin_bit_cast(float, u & 0xffff0000u); }
#define RAW_BARRIER() do { asm volatile("s_waitcnt lgkmcnt(0)" ::: "memory"); __builtin_amdgcn_s_barrier(); asm volatile("" ::: "memory"); } while (0)
#define VMCNT(n) asm volatile("s_waitcnt vmcnt(" #n ")" ::: "memory")

constexpr float C71 = 0.62348980185873353f, C72 = -0.22252093395631440f, C73 = -0.90096886790241913f;
constexpr float S71 = 0.78183148246802981f, S72 = 0.97492791218182361f, S73 = 0.43388373911755812f;

DEV void gen_cs(int i, int k0, int mul, int add, int mask, float inv, u32x4& C, u32x4& S) {
  float c[8], s[8];
#pragma unroll
  for (int j = 0; j < 8; ++j) {
    int n = (__mul24(i, mul * k0 + add) + __mul24(i, mul) * j) & mask;
    c[j] = cosr(n, inv); s[j] = sinr(n, inv);
  }
#pragma unroll
  for (int j = 0; j < 4; ++j) { C[j] = pk2(c[2 * j], c[2 * j + 1]); S[j] = pk2(s[2 * j], s[2 * j + 1]); }
}

constexpr double ccos64(int n) {
  n &= 63; if (n > 32) n = 64 - n;
  double sg = 1.0; if (n > 16) { n = 32 - n; sg = -1.0; }
  if (n == 16) return 0.0;
  double x = 6.283185307179586476925 * n / 64.0, x2 = x * x, term = 1.0, sum = 1.0;
  for (int i = 1; i < 14; ++i) { term *= -x2 / ((2 * i - 1) * (2 * i)); sum += term; }
  return sg * sum;
}
constexpr unsigned cbf(double v) { unsigned u = __builtin_bit_cast(unsigned, (float)v); return (u + 0x7fffu + ((u >> 16) & 1u)) >> 16; }
constexpr int csw(int r) { return (r & ~12) | ((r & 4) << 1) | ((r & 8) >> 1); }
struct alignas(16) Tabs { unsigned f64[16 * 256]; unsigned b2[14 * 256]; };
constexpr Tabs make_tabs() {
  Tabs t{};
  unsigned c[64] = {}, sn[64] = {};
  for (int n = 0; n < 64; ++n) { c[n] = cbf(ccos64(n)); sn[n] = cbf(ccos64(n - 16 + 64)); }
  for (int f = 0; f < 8; ++f)
    for (int l = 0; l < 64; ++l)
      for (int d = 0; d < 4; ++d) {
        int i = 32 * (f >> 2) + (l & 31), k = 16 * (f & 3) + 8 * (l >> 5) + 2 * d;
        t.f64[((f * 2) * 64 + l) * 4 + d] = c[(i * k) & 63] | (c[(i * (k + 1)) & 63] << 16);
        t.f64[((f * 2 + 1) * 64 + l) * 4 + d] = sn[(i * k) & 63] | (sn[(i * (k + 1)) & 63] << 16);
      }
  for (int l = 0; l < 64; ++l)
    for (int d = 0; d < 4; ++d) {
      for (int f = 0; f < 4; ++f) {
        int n = (2 * csw(l & 31) * (8 * f + 4 * (l >> 5) + d)) & 63;
        t.b2[(f * 64 + l) * 4 + d] = c[n] | (sn[n] << 16);
        t.b2[((4 + f) * 64 + l) * 4 + d] = (sn[n] ^ 0x8000u) | (c[n] << 16);
      }
      for (int f = 0; f < 2; ++f) {
        int k = 16 * f + 8 * (l >> 5) + 2 * d, n0 = (2 * (l & 31) * k) & 63, n1 = (2 * (l & 31) * (k + 1)) & 63;
        t.b2[((8 + f) * 64 + l) * 4 + d] = c[n0] | (c[n1] << 16);
        t.b2[((10 + f) * 64 + l) * 4 + d] = sn[n0] | (sn[n1] << 16);
        t.b2[((12 + f) * 64 + l) * 4 + d] = (sn[n0] | (sn[n1] << 16)) ^ 0x80008000u;
      }
    }
  return t;
}
__device__ const Tabs g_tabs = make_tabs();
DEV unsigned ald32(const void* base, unsigned off) {
  unsigned r; asm volatile("global_load_dword %0, %1, %2" : "=v"(r) : "v"(off), "s"(base) : "memory"); return r;
}
DEV unsigned ald16(const void* base, unsigned off) {
  unsigned r; asm volatile("global_load_ushort %0, %1, %2" : "=v"(r) : "v"(off), "s"(base) : "memory"); return r;
}
DEV void ast32(void* base, unsigned off, unsigned v) { asm volatile("global_store_dword %0, %1, %2 sc1" ::"v"(off), "v"(v), "s"(base) : "memory"); }
DEV int col_bin(int c) { int ct = c >> 5, k = ct == 7 ? 7 * (c - 192) : 7 * ((ct ? ((ct + 1) & 1) * 32 : 0) + (c & 31)) + 64 * ((ct + 1) >> 1); return k >= 448 ? k - 448 : k; }

constexpr int K1_SLOT = 32 * 1024;
constexpr int K1_LDS = 128 * 1024;
constexpr unsigned CODE_BYTES = 26000;
struct QF { u32x4 x0[4], x1[4], w[4]; };

template <bool ISV>
DEV void qkv_loop(const char* lds, unsigned ld, const u16* xb, const u16* wb, int kn, unsigned xo, unsigned wo0, unsigned wo1,
                  int xoff, int woff, f32x16& a0, f32x16& a1) {
  auto piece = [&](int slot, int i) {
    const unsigned d = ld + slot * K1_SLOT;
    if (i == 0) dma16(xb + kn * 64, xo, d);
    if (i == 1) dma16(wb + kn * (64 * 1344), wo0, d + 16384);
    if (i == 2) dma16(xb + kn * 64 + 64 * 448, xo, d + 8192);
    if (i == 3) { dma16(wb + kn * (64 * 1344), wo1, d + 24576); kn = kn == 6 ? 0 : kn + 1; }
  };
  auto rd = [&](QF& f, int slot, int ks) {
    const char* sl = lds + slot * K1_SLOT;
    f.x0[ks] = *(const u32x4*)(sl + (xoff ^ (ks * 32)));
    f.x1[ks] = *(const u32x4*)(sl + (xoff ^ (ks * 32)) + 4096);
    u32x2 wa = ldtr(sl + woff + ks * 16 * 224), wc = ldtr(sl + woff + ks * 16 * 224 + 224);
    f.w[ks] = u32x4{wa[0], wa[1], wc[0], wc[1]};
  };
  QF f[2];
#pragma unroll
  for (int kc = 0; kc < 8; ++kc) {
    if (kc < 7) { if (kc < 6) VMCNT(4); else VMCNT(0); __builtin_amdgcn_s_barrier(); asm volatile("" ::: "memory"); }
#pragma unroll
    for (int ks = 0; ks < 4; ++ks) {
      if (kc < 7) rd(f[kc & 1], kc & 3, ks);
      if (kc > 0) {
        QF& g = f[(kc - 1) & 1];
        if (ISV) { a0 = mfma(g.x0[ks], g.w[ks], a0); a1 = mfma(g.x1[ks], g.w[ks], a1); }
        else     { a0 = mfma(g.w[ks], g.x0[ks], a0); a1 = mfma(g.w[ks], g.x1[ks], a1); }
      }
      if (kc < 5) piece((kc + 2) & 3, ks);
      SB();
    }
  }
}

__global__ __launch_bounds__(512, 2) void k1_kernel(const u16* __restrict__ ax, const u16* __restrict__ wqkv, const u16* __restrict__ bx,
                                                    u32x4* __restrict__ Qf, u32x4* __restrict__ KVf, u32x4* __restrict__ T) {
  extern __shared__ __attribute__((aligned(16))) char lds[];
  const int tid = threadIdx.x, lane = tid & 63, w = __builtin_amdgcn_readfirstlane(tid >> 6);
  const int xcd = blockIdx.x & 7, idx = blockIdx.x >> 3;
  const int l31 = lane & 31, h = lane >> 5;
  const unsigned lb = ldsaddr_of(lds);
  unsigned long long pc0;
  asm volatile("s_getpc_b64 %0" : "=s"(pc0));
  const unsigned po = ((idx & 7) * 8 + w) * 512;
  const unsigned long long pcw = (pc0 & ~127ull) + (po < CODE_BYTES - 512 ? po : 0u);
#define CODE_PF() asm volatile("s_load_dword s96, %0, 0\n\ts_load_dword s96, %0, 0x80\n\ts_load_dword s96, %0, 0x100\n\ts_load_dword s96, %0, 0x180" ::"s"(pcw) : "s96", "memory")
  if (__builtin_expect(idx >= 8, 1)) {
    const int u = idx - 8, which = u >> 3, tm = (xcd & 3) * 4 + (u & 3), g = (xcd >> 2) * 2 + ((u >> 2) & 1);
    const int row = 8 * w + (lane >> 3);
    const unsigned xo = (row * 448 + ((lane & 7) ^ ((row >> 1) & 7)) * 8) * 2;
    unsigned wo2[2];
#pragma unroll
    for (int i = 0; i < 2; ++i) {
      int q = 64 * (w + 8 * i) + lane; if (q > 895) q = 895;
      int rho = q / 14, cc = q - rho * 14;
      wo2[i] = (((rho & ~7) + ((rho & 7) >> 1) + 4 * (rho & 1)) * 1344 + cc * 8) * 2;
    }
    const u16* xb = ax + tm * 128 * 448;
    asm volatile("" ::"s"(xb), "s"(wqkv), "s"(Qf), "s"(KVf));
    CODE_PF();
    const u16* wb = wqkv + which * 448 + g * 112;
    int kn = ((u & 3) * 2 + (u >> 2)) % 7;
#pragma unroll
    for (int i = 0; i < 2; ++i) {
      const unsigned d = lb + w * 1024 + i * K1_SLOT;
      dma16(xb + kn * 64, xo, d); dma16(wb + kn * (64 * 1344), wo2[0], d + 16384);
      dma16(xb + kn * 64 + 64 * 448, xo, d + 8192); dma16(wb + kn * (64 * 1344), wo2[1], d + 24576);
      kn = kn == 6 ? 0 : kn + 1;
    }
    SB();
    const int hh = w & 3, th = w >> 2, r0 = th * 64 + l31;
    const int xoff = r0 * 128 + ((h ^ ((r0 >> 1) & 7)) * 16);
    const int woff = 16384 + (8 * h + 2 * ((lane >> 2) & 3)) * 224 + (hh * 28 + ((lane >> 4) & 1) * 16 + 4 * (lane & 3)) * 2;
    const int head = g * 4 + hh, tile = tm * 4 + th * 2;
    f32x16 a0 = {}, a1 = {};
    u32x4 o0, o1, o2, o3;
    if (which == 2) {
      qkv_loop<true>(lds, lb + w * 1024, xb, wb, kn, xo, wo2[0], wo2[1], xoff, woff, a0, a1);
      o0 = packs<0>(a0); o1 = packs<1>(a0); o2 = packs<0>(a1); o3 = packs<1>(a1);
      if (l31 >= 28) { unsigned f = (l31 == 28) ? 0x3F803F80u : 0u; o0 = u32x4{f, f, f, f}; o1 = o0; o2 = o0; o3 = o0; }
    } else {
      qkv_loop<false>(lds, lb + w * 1024, xb, wb, kn, xo, wo2[0], wo2[1], xoff, woff, a0, a1);
      const float sc = which ? 1.0f : 0.18898223650461362f * 1.4426950408889634f;
      a0 *= sc; a1 *= sc;
      o0 = packs<0>(a0); o1 = packs<1>(a0); o2 = packs<0>(a1); o3 = packs<1>(a1);
      if (h) { o1[2] = which ? 0x3F80u : 0u; o1[3] = 0; o3[2] = o1[2]; o3[3] = 0; }
    }
    u32x4* o = (which == 0 ? Qf + ((head * 64 + tile) * 2) * 64 : KVf + ((head * 64 + tile) * 4 + (which & 2)) * 64) + lane;
    const int st = which == 0 ? 128 : 256;
    st128(o, o0); st128(o + 64, o1); st128(o + st, o2); st128(o + st + 64, o3);
  } else {
    const int b1 = xcd * 8 + idx, s2 = b1 >> 1, hb = b1 & 1;
    u32x4* F64 = (u32x4*)(lds + 56 * 1024);
    u32x4* Ap = (u32x4*)(lds + 72 * 1024);
    u32x4* Xf = (u32x4*)(lds + 88 * 1024);
    asm volatile("" ::"s"(bx), "s"(T));
    CODE_PF();
    {
      const int row = 8 * w + (lane >> 3);
      const unsigned xo = ((s2 + 32 * ((row & 32) + sw23(row & 31))) * 448 + ((lane & 7) ^ ((row >> 1) & 7)) * 8) * 2;
#pragma unroll
      for (int m = 0; m < 7; ++m) dma16(bx + 64 * m, xo, lb + m * 8192 + w * 1024);
      dma16(g_tabs.f64 + w * 512, lane * 16, lb + 56 * 1024 + w * 2048); dma16(g_tabs.f64 + w * 512 + 256, lane * 16, lb + 57 * 1024 + w * 2048);
    }
    SB();
    const int mt = w >> 2, o = 2 * (w & 3) + h;
    {
      u32x4 C, S;
      gen_cs(32 * mt + l31, 8 * o, 32, s2, 2047, 1.0f / 2048, C, S);
      Ap[(w * 2 + 0) * 64 + lane] = C; Ap[(w * 2 + 1) * 64 + lane] = S;
    }
    char* xp = lds + (32 * mt + l31) * 128 + ((o ^ ((l31 >> 1) & 7)) * 16);
    int xm[7];
#pragma unroll
    for (int b = 0; b < 7; ++b) { int m = b - o; m += m < 0 ? 7 : 0; m += m < 0 ? 7 : 0; xm[b] = m * 8192; asm volatile("" : "+v"(xm[b])); }
    VMCNT(2);
    RAW_BARRIER();
    {
      u32x4 xr[7], z[4];
#pragma unroll
      for (int b = 0; b < 7; ++b) xr[b] = *(const u32x4*)(xp + xm[b]);
      const float ca = hb ? C72 : C71, cb = hb ? C73 : C72, cc = hb ? C71 : C73, sa = hb ? S72 : S71, sb = hb ? -S73 : S72, sc = hb ? -S71 : S73;
      const float da = hb ? C73 : 1.0f, db = hb ? C71 : 1.0f, dc = hb ? C72 : 1.0f, ta = hb ? S73 : 0.0f, tb = hb ? -S71 : 0.0f, tc = hb ? S72 : 0.0f;
#pragma unroll
      for (int jp = 0; jp < 4; ++jp) {
        f32x2 v[7];
#pragma unroll
        for (int b = 0; b < 7; ++b) {
          v[b][0] = bflo(xr[(b - 2 * jp + 14) % 7][jp]);
          v[b][1] = bfhi(xr[(b - 2 * jp - 1 + 14) % 7][jp]);
        }
        f32x2 p1 = v[1] + v[6], p2 = v[2] + v[5], p3 = v[3] + v[4];
        f32x2 m1 = v[6] - v[1], m2 = v[5] - v[2], m3 = v[4] - v[3];
        f32x2 ra = v[0] + p1 * ca + p2 * cb + p3 * cc, ia = m1 * sa + m2 * sb + m3 * sc;
        f32x2 rb = v[0] + p1 * da + p2 * db + p3 * dc, ib = m1 * ta + m2 * tb + m3 * tc;
        z[0][jp] = pk2(ra[0], ra[1]); z[1][jp] = pk2(ia[0], ia[1]); z[2][jp] = pk2(rb[0], rb[1]); z[3][jp] = pk2(ib[0], ib[1]);
      }
#pragma unroll
      for (int c = 0; c < 4; ++c) *(u32x4*)(xp + c * 8192) = z[c];
    }
    VMCNT(0);
    RAW_BARRIER();
    const int wc = w & 3, ct = hb ? 3 + wc : (wc == 2 ? 0 : (wc == 3 ? 7 : wc + 1)), kt = w & 1;
    u32x4 ap[8];
    {
      f32x16 xre = {}, xim = {};
      const char* zp = lds + (w & 2) * 8192 + mt * 4096 + l31 * 128;
      const int zsw = (h ^ ((l31 >> 1) & 7)) * 16;
      u32x4 C[4], S[4], zr[4], zi[4];
#pragma unroll
      for (int tt = 0; tt < 4; ++tt) {
        zr[tt] = *(const u32x4*)(zp + (zsw ^ (tt * 32))); C[tt] = F64[((kt * 4 + tt) * 2) * 64 + lane];
        zi[tt] = *(const u32x4*)(zp + 8192 + (zsw ^ (tt * 32))); S[tt] = F64[((kt * 4 + tt) * 2 + 1) * 64 + lane];
      }
#pragma unroll
      for (int tt = 0; tt < 8; ++tt) ap[tt] = Ap[(mt * 8 + tt) * 64 + lane];
      SB();
#pragma unroll
      for (int tt = 0; tt < 4; ++tt) {
        xre = mfma(zr[tt], C[tt], xre); xim = mfma(zi[tt], C[tt], xim);
        xre = mfma(zi[tt], S[tt], xre); xim = mfma(zr[tt], S[tt] ^ 0x80008000u, xim);
      }
      u32x4* xq = Xf + (wc * 4 + mt * 2) * 128 + lane;
      xq[0] = packs<0>(xre); xq[64] = packs<0>(xim); xq[128] = packs<1>(xre); xq[192] = packs<1>(xim);
    }
    RAW_BARRIER();
    {
      f32x16 tre = {}, tim = {};
      u32x4 bf[8];
#pragma unroll
      for (int tt = 0; tt < 8; ++tt) bf[tt] = Xf[(wc * 8 + tt) * 64 + lane];
      SB();
#pragma unroll
      for (int tt = 0; tt < 4; ++tt) {
        tre = mfma(ap[2 * tt], bf[2 * tt], tre); tim = mfma(ap[2 * tt], bf[2 * tt + 1], tim);
        tre = mfma(ap[2 * tt + 1], bf[2 * tt + 1], tre); tim = mfma(ap[2 * tt + 1] ^ 0x80008000u, bf[2 * tt], tim);
      }
      u32x4* tp = T + (s2 * 16 + mt * 8 + h) * 232 + ct * 32 + l31;
      if (ct < 7 || l31 < 8)
#pragma unroll
      for (int q = 0; q < 4; ++q)
        st128(tp + 2 * q * 232, u32x4{pk2(tre[4 * q], tim[4 * q]), pk2(tre[4 * q + 1], tim[4 * q + 1]), pk2(tre[4 * q + 2], tim[4 * q + 2]), pk2(tre[4 * q + 3], tim[4 * q + 3])});
    }
  }
}

constexpr int K2_LDS = 84 * 1024;
DEV float xhalf_max(float v) {
  float a = v, b = v;
  asm volatile("s_nop 1\n\tv_permlane32_swap_b32 %0, %1\n\ts_nop 1" : "+v"(a), "+v"(b));
  return fmaxf(a, b);
}
DEV float max16(const f32x16& s) {
  float m = fmaxf(fmaxf(s[0], s[1]), s[2]);
#pragma unroll
  for (int i = 3; i < 15; i += 2) m = fmaxf(fmaxf(m, s[i]), s[i + 1]);
  return fmaxf(m, s[15]);
}
#define PIN16(a) asm volatile("" : "+v"(a[0]), "+v"(a[1]), "+v"(a[2]), "+v"(a[3]), "+v"(a[4]), "+v"(a[5]), "+v"(a[6]), "+v"(a[7]), \
                                   "+v"(a[8]), "+v"(a[9]), "+v"(a[10]), "+v"(a[11]), "+v"(a[12]), "+v"(a[13]), "+v"(a[14]), "+v"(a[15]))
#define EX4(s, i, P, j)                                                                                          \
  { const float t0_ = __builtin_amdgcn_exp2f(s[i]); SB(); P[j] = pk2(e0, e1); SB();                               \
    const float t1_ = __builtin_amdgcn_exp2f(s[i + 1]); SB(); P[j + 1] = pk2(e2, e3); SB();                       \
    e0 = t0_; e1 = t1_; e2 = __builtin_amdgcn_exp2f(s[i + 2]); SB(); e3 = __builtin_amdgcn_exp2f(s[i + 3]); SB(); }
#define TILE(sc, sn, K0, K1, Q0, Q1, oc, V0, op, V1, L0, L1, L2, L3)                 \
  sn = mfma(K0, Q0, f32x16{}); SB(); L0; EX4(sc, 0, p1, 2)                           \
  op = mfma(V1, p1, op); SB(); L1; EX4(sc, 4, p0, 0)                                 \
  sn = mfma(K1, Q1, sn); SB(); L2; EX4(sc, 8, p0, 2)                                 \
  oc = mfma(V0, p0, oc); SB(); L3; EX4(sc, 12, p1, 0)
#define KTILE(X, Y, P1, P2)                                                                                      \
  TILE(sa, sb, X##k0, X##k1, q0[1], q1[1], o[0], X##v0, o[3], Y##v1, , Y##v1 = (P1)[192], , )                     \
  TILE(sb, sa, X##k0, X##k1, q0[2], q1[2], o[1], X##v0, o[0], X##v1, , , , )                                      \
  TILE(sa, sb, X##k0, X##k1, q0[3], q1[3], o[2], X##v0, o[1], X##v1, X##k0 = (P2)[0], , X##k1 = (P2)[64], )       \
  TILE(sb, sa, Y##k0, Y##k1, q0[0], q1[0], o[3], X##v0, o[2], X##v1, , , , X##v0 = (P2)[128])

__global__ __launch_bounds__(384, 2) void k2_kernel(const u32x4* __restrict__ Qf, const u32x4* __restrict__ KVf, u32x4* __restrict__ Of,
                                                    const unsigned* __restrict__ T, unsigned* __restrict__ U,
                                                    const u16* __restrict__ Wr, const u16* __restrict__ Wi, const u16* __restrict__ wo) {
  extern __shared__ __attribute__((aligned(16))) char lds[];
  const int tid = threadIdx.x, lane = tid & 63, w = __builtin_amdgcn_readfirstlane(tid >> 6);
  const int xcd = blockIdx.x & 7, idx = blockIdx.x >> 3;
  const int l31 = lane & 31, h = lane >> 5;
  if (__builtin_expect(w < 4, 1)) {
    __builtin_amdgcn_s_setprio(2);
    const int head = xcd * 2 + (idx >> 4), qt = (idx & 15) * 4;
    const u32x4* kp = KVf + (head * 64 + w * 16) * 256 + lane;
    u32x4 q0[4], q1[4];
    u32x4 ek0 = kp[0], ek1 = kp[64];
#pragma unroll
    for (int i = 0; i < 4; ++i) { const u32x4* qp = Qf + (head * 64 + qt + ((w + i) & 3)) * 128 + lane; q0[i] = qp[0]; q1[i] = qp[64]; }
    u32x4 ev0 = kp[128], ev1 = kp[192], dk0 = kp[256], dk1 = kp[320], dv0 = kp[384], dv1 = {}, p0, p1 = {};
    f32x16 o[4] = {}, sa, sb;
    float m[4], e0 = 0.f, e1 = 0.f, e2 = 0.f, e3 = 0.f;
    {
      f32x16 s4[4];
#pragma unroll
      for (int i = 0; i < 4; ++i) { s4[i] = mfma(ek0, q0[i], f32x16{}); s4[i] = mfma(ek1, q1[i], s4[i]); }
#pragma unroll
      for (int i = 0; i < 4; ++i) {
        const unsigned nm = pk2(-xhalf_max(max16(s4[i])), 0.0f);
        m[i] = -bflo(nm);
        if (h) q1[i][2] = nm;
      }
      sa = s4[0] - m[0];
    }
#pragma unroll 1
    for (int it = 0; it < 8; ++it) {
      const u32x4* kn = kp + ((2 * it + 2) & 15) * 256;
      KTILE(e, d, kp + (2 * it + 1) * 256, kn)
      KTILE(d, e, kn, kn + 256)
    }
    p1[2] = pk2(e0, e1); p1[3] = pk2(e2, e3);
    o[3] = mfma(dv1, p1, o[3]);
    if (__builtin_expect(__ballot(h && !(o[0][12] + o[1][12] + o[2][12] + o[3][12] < 1.0e30f)) != 0ull, 0)) {
#pragma unroll
      for (int u = 0; u < 4; ++u) {
        f32x16 oo = {}; float mm = -3.0e38f;
        if (h) q1[u][2] = 0;
#pragma unroll 1
        for (int t = 0; t < 16; ++t) {
          const u32x4* p = kp + t * 256;
          f32x16 s = mfma(p[0], q0[u], f32x16{}); s = mfma(p[64], q1[u], s);
          float mn = fmaxf(mm, xhalf_max(max16(s)));
          float al = __builtin_amdgcn_exp2f(mm - mn);
          mm = mn;
#pragma unroll
          for (int i = 0; i < 16; ++i) { oo[i] *= al; s[i] = __builtin_amdgcn_exp2f(s[i] - mn); }
          oo = mfma(p[128], packs<0>(s), oo); oo = mfma(p[192], packs<1>(s), oo);
        }
        o[u] = oo; m[u] = mm;
      }
    }
#pragma unroll
    for (int u = 1; u < 4; ++u) {
      if (h) o[u][13] = m[u];
      f32x4* ex = (f32x4*)(lds + (((w + u) & 3) * 3 + u - 1) * 4096) + lane;
#pragma unroll
      for (int i = 0; i < 4; ++i) ex[i * 64] = f32x4{o[u][4 * i], o[u][4 * i + 1], o[u][4 * i + 2], o[u][4 * i + 3]};
    }
    const float l0 = xhalf_max(h ? o[0][12] : -1.0f);
    RAW_BARRIER();
    const char* ey = lds + w * 12288;
    f32x2 st[3];
#pragma unroll
    for (int u = 0; u < 3; ++u) st[u] = *(const f32x2*)(ey + u * 4096 + 3584 + l31 * 16);
    f32x4 y[12];
#pragma unroll
    for (int i = 0; i < 12; ++i) y[i] = *(const f32x4*)(ey + i * 1024 + lane * 16);
    SB();
    const float mm = fmaxf(fmaxf(st[0][1], st[1][1]), fmaxf(st[2][1], m[0]));
    float f0 = __builtin_amdgcn_exp2f(m[0] - mm), f[3];
#pragma unroll
    for (int u = 0; u < 3; ++u) f[u] = __builtin_amdgcn_exp2f(st[u][1] - mm);
    const float inv = __builtin_amdgcn_rcpf(l0 * f0 + st[0][0] * f[0] + st[1][0] * f[1] + st[2][0] * f[2]);
    f0 *= inv;
    f32x16 r = o[0] * f0;
    asm volatile("" : "+v"(r) : "v"(f0));
#pragma unroll
    for (int u = 0; u < 3; ++u) {
      const float fu = f[u] * inv;
#pragma unroll
      for (int i = 0; i < 4; ++i) {
        const f32x4 v = y[u * 4 + i];
        r[4 * i] += v[0] * fu; r[4 * i + 1] += v[1] * fu; r[4 * i + 2] += v[2] * fu; r[4 * i + 3] += v[3] * fu;
      }
    }
    u32x4 r1 = packs<1>(r);
    if (h) { r1[2] = 0; r1[3] = 0; }
    u32x4* op = Of + (((qt + w) * 16 + head) * 2) * 64 + lane;
    st128(op, packs<0>(r)); st128(op + 64, r1);
  } else if (idx < 29) {
    const int jq = xcd * 2 + w - 4, col = idx * 8 + (l31 >> 2), kb = col_bin(col), jr = 4 * jq + (lane & 3) + 512 * h;
    const unsigned toff = (4 * h * 16 + jq) * 928 + idx * 32 + l31;
    const bool cj = kb > 224;
    const int of = jr * 450 + 2 * kb, om = (2048 - jr) * 450 + 2 * (448 - kb), om0 = jr ? om : om - 921600;
    const float sr = col > 224 ? 0.0f : ((col == 0 || col == 224) ? 0.5f : 1.0f), si = cj ? -sr : sr;
    u32x4 mt[14];
    unsigned ta[16], ur[16], ui[16]; float wr[16], wi[16];
#pragma unroll
    for (int i = 0; i < 16; ++i) ta[i] = ald32(T + (8 * (i >> 2) + (i & 3)) * 14848, toff * 4);
#pragma unroll
    for (int g = 0; g < 16; ++g) {
      const unsigned o_ = g ? (cj ? om : of) + ((g & 7) + 16 * (g >> 3)) * (cj ? -28800 : 28800) : (cj ? om0 : of);
      ur[g] = ald16(Wr, o_); ui[g] = ald16(Wi, o_);
    }
    const unsigned* tabp = g_tabs.b2;
    asm volatile("" : "+s"(tabp));
#pragma unroll
    for (int t = 0; t < 14; ++t) mt[t] = ald128(tabp + t * 256, lane * 16);
    asm volatile("s_waitcnt vmcnt(0)" : "+v"(mt[0]), "+v"(mt[1]), "+v"(mt[2]), "+v"(mt[3]), "+v"(mt[4]), "+v"(mt[5]), "+v"(mt[6]), "+v"(mt[7]), "+v"(mt[8]),
                 "+v"(mt[9]), "+v"(mt[10]), "+v"(mt[11]), "+v"(mt[12]), "+v"(mt[13]) :: "memory");
    PIN16(ta); PIN16(ur); PIN16(ui);
#pragma unroll
    for (int g = 0; g < 16; ++g) { wr[g] = bflo(ur[g]); wi[g] = bflo(ui[g]); }
    SB();
    f32x16 xr = {}, xi = {};
#pragma unroll
    for (int t = 0; t < 4; ++t) {
      u32x4 b = {ta[4 * t], ta[4 * t + 1], ta[4 * t + 2], ta[4 * t + 3]};
      xr = mfma(mt[t], b, xr); xi = mfma(mt[4 + t], b, xi);
    }
    f32x16 yr, yi;
#pragma unroll
    for (int g = 0; g < 16; g += 2) {
      f32x2 c = {wr[g], wr[g + 1]}, d = {wi[g], wi[g + 1]};
      asm volatile("" : "+v"(c), "+v"(d));
      c *= sr; d *= si;
      const f32x2 a = {xr[g], xr[g + 1]}, b = {xi[g], xi[g + 1]}, r = a * c - b * d, i = a * d + b * c;
      yr[g] = r[0]; yr[g + 1] = r[1]; yi[g] = i[0]; yi[g + 1] = i[1];
    }
    u32x4 r0 = packs<0>(yr), r1 = packs<1>(yr), i0 = packs<0>(yi), i1 = packs<1>(yi);
    f32x16 ure = {}, uim = {};
    ure = mfma(mt[8], r0, ure); uim = mfma(mt[8], i0, uim);
    ure = mfma(mt[12], i0, ure); uim = mfma(mt[10], r0, uim);
    ure = mfma(mt[9], r1, ure); uim = mfma(mt[9], i1, uim);
    ure = mfma(mt[13], i1, ure); uim = mfma(mt[11], r1, uim);
#pragma unroll
    for (int g = 0; g < 16; ++g) ast32(U + ((g & 3) + 8 * (g >> 2)) * 14848, toff * 4, pk2(ure[g], uim[g]));
    {
      const unsigned off = ((idx * 2 + w - 4) * 64 + lane) * 128;
      if (off < 401408u) { unsigned pf = *(const unsigned*)((const char*)wo + off); asm volatile("" ::"v"(pf)); }
    }
  }
}

constexpr int ZS = 68;
constexpr int K3_LDS = 77 * 1024;

__global__ __launch_bounds__(512, 4) void k3_kernel(const u32x4* __restrict__ Of, const u16* __restrict__ wo, const u32x4* __restrict__ U,
                                                    u16* __restrict__ out) {
  extern __shared__ __attribute__((aligned(16))) char lds[];
  const int tid = threadIdx.x, lane = tid & 63, w = __builtin_amdgcn_readfirstlane(tid >> 6);
  const int xcd = blockIdx.x & 7, idx = blockIdx.x >> 3;
  const int l31 = lane & 31, h = lane >> 5;
  if (idx < 4 || idx >= 12) {
    if (w >= 4) return;
    const int u = idx < 4 ? idx : idx - 8, tq = xcd * 4 + u / 7, tn = u % 7;
    const int qi = w & 1, kh = w >> 1;
    u32x4 of[16];
    {
      const int r8 = lane >> 3;
      const unsigned so = (r8 * 448 + ((lane & 7) ^ ((r8 & 2) << 1)) * 8) * 2, l16 = lane * 16;
      const u16* src = wo + (kh * 224 + qi * 8) * 448 + tn * 64;
      const unsigned d = ldsaddr_of(lds) + (kh * 224 + qi * 8) * 128;
      const u32x4* op = Of + (((tq * 2 + qi) * 16 + kh * 8) * 2) * 64;
#pragma unroll
      for (int st = 0; st < 2; ++st) {
#pragma unroll
        for (int i = 0; i < 7; ++i) dma16(src + (st * 7 + i) * 16 * 448, so, d + (st * 7 + i) * 2048);
#pragma unroll
        for (int i = 0; i < 8; ++i) of[st * 8 + i] = ald128(op + (st * 8 + i) * 64, l16);
      }
    }
    const int q = (lane >> 2) & 3, p = lane & 3, pc = ((p & 1) << 1) | (p >> 1);
    const int colb = ((lane >> 4) & 1) * 32 + pc * 8;
    const int base = (kh * 224 + q) * 128 + (colb ^ (((q >> 1) ^ kh) << 6));
    f32x16 a0 = {}, a1 = {};
#pragma unroll
    for (int st = 0; st < 2; ++st) {
      if (st == 0) asm volatile("s_waitcnt vmcnt(15)" : "+v"(of[0]), "+v"(of[1]), "+v"(of[2]), "+v"(of[3]), "+v"(of[4]), "+v"(of[5]), "+v"(of[6]), "+v"(of[7]) :: "memory");
      else asm volatile("s_waitcnt vmcnt(0)" : "+v"(of[8]), "+v"(of[9]), "+v"(of[10]), "+v"(of[11]), "+v"(of[12]), "+v"(of[13]), "+v"(of[14]), "+v"(of[15]) :: "memory");
      RAW_BARRIER();
      u32x4 fx[3], fy[3];
#pragma unroll
      for (int n = 0; n < 10; ++n) {
        if (n < 8) {
          const int i = st * 8 + n, hd = i >> 1, s = i & 1;
          const char* pa = lds + (hd * 28 + 16 * s) * 128;
          const int ra = base + h * 512, rb = (s ? base : base + h * 512) + 1024;
          u32x2 x0 = ldtr(pa + ra), x1 = ldtr(pa + rb), y0 = ldtr(pa + (ra ^ 64)), y1 = ldtr(pa + (rb ^ 64));
          fx[n % 3] = u32x4{x0[0], x0[1], x1[0], x1[1]}; fy[n % 3] = u32x4{y0[0], y0[1], y1[0], y1[1]};
        }
        SB();
        if (n >= 2) { a0 = mfma(fx[(n - 2) % 3], of[st * 8 + n - 2], a0); a1 = mfma(fy[(n - 2) % 3], of[st * 8 + n - 2], a1); }
        SB();
      }
    }
    f32x4* ex = (f32x4*)(lds + 56 * 1024);
    f32x16 snd = a1, kp = a0;
#pragma unroll
    for (int i = 0; i < 4; ++i) ex[(w * 4 + i) * 64 + lane] = f32x4{snd[4 * i], snd[4 * i + 1], snd[4 * i + 2], snd[4 * i + 3]};
    RAW_BARRIER();
#pragma unroll
    for (int i = 0; i < 4; ++i) {
      f32x4 v = ex[((w ^ 2) * 4 + i) * 64 + lane];
      kp[4 * i] += v[0]; kp[4 * i + 1] += v[1]; kp[4 * i + 2] += v[2]; kp[4 * i + 3] += v[3];
    }
    u16* dst = out + ((tq * 2 + qi) * 32 + l31) * 896 + tn * 64 + kh * 32 + 8 * h;
    st128(dst, packs<0>(kp)); st128(dst + 16, packs<1>(kp));
  } else {
    const int b3 = xcd * 8 + idx - 4, s2 = b3 >> 1, sh = b3 & 1, r = w;
    u32x4* Dm = (u32x4*)lds;
    u32x4* F64 = (u32x4*)(lds + 61 * 1024);
    float* zs = (float*)lds;
    u32x4 ua[8], ub[8];
    if (__builtin_expect(w < 4, 1)) {
      const u32x4* up = U + s2 * 16 * 232 + (r ? 2 * r - 1 : 0) * 32;
      const unsigned uo = (h * 232 + sw23(l31)) * 16, uo2 = (h * 232 + (sw23(l31) & (r ? 31 : 7)) + (r ? 32 : 224)) * 16;
#pragma unroll
      for (int i = 0; i < 8; ++i) { ua[i] = ald128(up + i * 464, uo); ub[i] = ald128(up + i * 464, uo2); }
    } else {
      const unsigned pl = (h * 32 + sw23(l31)) * 16;
#pragma unroll
      for (int i = 0; i < 4; ++i) dma16(g_tabs.f64 + ((w & 3) * 4 + i) * 256, pl, ldsaddr_of(lds) + 61 * 1024 + ((w & 3) * 4 + i) * 1024);
    }
    SB();
    {
      const int s = s2 + 32 * (32 * sh + l31);
      const float NRM2 = 2.0f / (2048.0f * 448.0f);
      u32x4 dr, di;
#pragma unroll
      for (int i = 0; i < 4; ++i) {
        int n = __mul24(8 * w + 4 * h + i, s) & 2047;
        float c = cosr(n, 1.0f / 2048) * NRM2, sn = sinr(n, 1.0f / 2048) * NRM2;
        dr[i] = pk2(c, -sn); di[i] = pk2(sn, c);
      }
      Dm[(w * 2) * 64 + lane] = dr; Dm[(w * 2 + 1) * 64 + lane] = di;
    }
    RAW_BARRIER();
    if (__builtin_expect(w < 4, 1)) {
      f32x16 ar = {}, ai = {}, br = {}, bi = {};
#pragma unroll
      for (int t = 0; t < 8; ++t) {
        u32x4 dr = Dm[(t * 2) * 64 + lane], di = Dm[(t * 2 + 1) * 64 + lane];
        asm volatile("s_waitcnt vmcnt(%2)" : "+v"(ua[t]), "+v"(ub[t]) : "n"(14 - 2 * t) : "memory");
        ar = mfma(ua[t], dr, ar); ai = mfma(ua[t], di, ai); br = mfma(ub[t], dr, br); bi = mfma(ub[t], di, bi);
      }
      RAW_BARRIER();
      u32x4 yr[4] = {packs<0>(ar), packs<1>(ar), packs<0>(br), packs<1>(br)};
      u32x4 yi[4] = {packs<0>(ai), packs<1>(ai), packs<0>(bi), packs<1>(bi)};
      const unsigned k2 = r ? ~0u : 0u, k1 = (r || !h) ? ~0u : 0u;
      yr[2] &= k1; yr[3] &= k2; yi[2] &= k2; yi[3] &= k2;
      float* zp = zs + ((r ? 2 * r - 1 : 0) * 32 + l31) * ZS + 8 * h;
      float* zq = zp + (r ? 32 * ZS : 0);
      f32x16 zre[2] = {}, zim[2] = {};
      u32x4 C = F64[lane], S = F64[64 + lane];
#pragma unroll
      for (int n = 0; n < 12; ++n) {
        const int at = n >> 2, t = n & 3;
        if (n < 8) {
          const u32x4 Cn = F64[((n + 1) & 7) * 128 + lane], Sn = F64[((n + 1) & 7) * 128 + 64 + lane];
          SB();
          zim[at] = mfma(C, yi[t], zim[at]); zre[at] = mfma(C, yr[t], zre[at]);
          zim[at] = mfma(S, yr[t], zim[at]); zre[at] = mfma(S ^ 0x80008000u, yi[t], zre[at]);
          C = Cn; S = Sn;
          SB();
        }
        if (n >= 4) {
          const int off = (at - 1) * 32 + 16 * (t >> 1) + 4 * (t & 1);
          *(f32x4*)(zq + off) = f32x4{zim[at - 1][4 * t], zim[at - 1][4 * t + 1], zim[at - 1][4 * t + 2], zim[at - 1][4 * t + 3]};
          *(f32x4*)(zp + off) = f32x4{zre[at - 1][4 * t], zre[at - 1][4 * t + 1], zre[at - 1][4 * t + 2], zre[at - 1][4 * t + 3]};
        }
      }
    } else {
      VMCNT(0);
      RAW_BARRIER();
    }
    RAW_BARRIER();
    {
      const int o = tid & 15, row = tid >> 4, rot = ((o >> 1) + 4 * (o & 1)) % 7;
      float* zp = zs + row * ZS + 4 * o;
      f32x4 zl[7];
#pragma unroll
      for (int c = 0; c < 7; ++c) zl[c] = *(const f32x4*)(zp + c * 32 * ZS);
      float y[4][7];
#pragma unroll
      for (int jp = 0; jp < 2; ++jp) {
        f32x2 z[7];
#pragma unroll
        for (int c = 0; c < 7; ++c) z[c] = f32x2{zl[c][2 * jp], zl[c][2 * jp + 1]};
        f32x2 y0 = z[0] + z[1] + z[3] + z[5];
        f32x2 e1 = z[0] + z[1] * C71 + z[3] * C72 + z[5] * C73, o1 = z[2] * S71 + z[4] * S72 + z[6] * S73;
        f32x2 e2 = z[0] + z[1] * C72 + z[3] * C73 + z[5] * C71, o2 = z[2] * S72 - z[4] * S73 - z[6] * S71;
        f32x2 e3 = z[0] + z[1] * C73 + z[3] * C71 + z[5] * C72, o3 = z[2] * S73 - z[4] * S71 + z[6] * S72;
        f32x2 yy[7] = {y0, e1 - o1, e2 - o2, e3 - o3, e3 + o3, e2 + o2, e1 + o1};
#pragma unroll
        for (int b = 0; b < 7; ++b) { y[2 * jp][b] = yy[b][0]; y[2 * jp + 1][b] = yy[b][1]; }
      }
#pragma unroll
      for (int b = 0; b < 7; ++b) {
        int m = b - rot; m += m < 0 ? 7 : 0;
        *(u32x2*)(zp + m * 32 * ZS) = u32x2{pk2(y[0][b], y[1][(b + 1) % 7]), pk2(y[2][(b + 2) % 7], y[3][(b + 3) % 7])};
      }
      u16* dst = out + (s2 + 32 * (32 * sh + row)) * 896 + 448 + 4 * o;
#pragma unroll
      for (int m = 0; m < 7; ++m) { const u32x2 v_ = *(const u32x2*)(zp + m * 32 * ZS); asm volatile("global_store_dwordx2 %0, %1, off sc1" ::"v"(dst + 64 * m), "v"(v_) : "memory"); }
    }
  }
}

extern "C" void kernel_launch(void* const* d_in, const int* in_sizes, int n_in, void* d_out, int out_size, void* d_ws, size_t ws_size,
                              hipStream_t stream) {
  const u16* ax = (const u16*)d_in[0];
  const u16* wqkv = (const u16*)d_in[1];
  const u16* wo = (const u16*)d_in[2];
  const u16* bx = (const u16*)d_in[3];
  const u16* wr = (const u16*)d_in[4];
  const u16* wi = (const u16*)d_in[5];
  char* ws = (char*)d_ws;
  u32x4* Qf = (u32x4*)(ws);
  u32x4* KVf = (u32x4*)(ws + (2u << 20));
  u32x4* Of = (u32x4*)(ws + (6u << 20));
  unsigned* T = (unsigned*)(ws + (8u << 20));
  unsigned* U = (unsigned*)(ws + (10u << 20));
  (void)hipFuncSetAttribute((const void*)k1_kernel, hipFuncAttributeMaxDynamicSharedMemorySize, K1_LDS);
  (void)hipFuncSetAttribute((const void*)k2_kernel, hipFuncAttributeMaxDynamicSharedMemorySize, K2_LDS);
  (void)hipFuncSetAttribute((const void*)k3_kernel, hipFuncAttributeMaxDynamicSharedMemorySize, K3_LDS);
  k1_kernel<<<256, 512, K1_LDS, stream>>>(ax, wqkv, bx, Qf, KVf, (u32x4*)T);
  k2_kernel<<<256, 384, K2_LDS, stream>>>(Qf, KVf, Of, T, U, wr, wi, wo);
  k3_kernel<<<288, 512, K3_LDS, stream>>>(Of, wo, (const u32x4*)U, (u16*)d_out);
}
```

```cpp
#include <hip/hip_runtime.h>
#include <hip/hip_bf16.h>

typedef __attribute__((ext_vector_type(8))) __bf16 bf16x8;
typedef __attribute__((ext_vector_type(4))) __bf16 bf16x4;
typedef __attribute__((ext_vector_type(2))) __bf16 bf16x2;
typedef __attribute__((ext_vector_type(4))) short s16x4;
typedef __attribute__((ext_vector_type(16))) float f32x16;
typedef __attribute__((ext_vector_type(4))) float f32x4;
typedef __attribute__((ext_vector_type(2))) float f32x2;
typedef __attribute__((ext_vector_type(4))) unsigned u32x4;
typedef __attribute__((ext_vector_type(2))) unsigned u32x2;
typedef unsigned short u16;
#define LDSP __attribute__((address_space(3)))
#define DEV __device__ __forceinline__

DEV unsigned pk2(float lo, float hi) {
  f32x2 v = {lo, hi};
  return __builtin_bit_cast(unsigned, __builtin_convertvector(v, bf16x2));
}
DEV f32x16 mfma(u32x4 a, u32x4 b, f32x16 c) {
  return __builtin_amdgcn_mfma_f32_32x32x16_bf16(__builtin_bit_cast(bf16x8, a), __builtin_bit_cast(bf16x8, b), c, 0, 0, 0);
}
template <int S> DEV u32x4 packs(const f32x16& v) {
  u32x4 r = {pk2(v[8 * S + 0], v[8 * S + 1]), pk2(v[8 * S + 2], v[8 * S + 3]), pk2(v[8 * S + 4], v[8 * S + 5]), pk2(v[8 * S + 6], v[8 * S + 7])};
  return r;
}
DEV int sw23(int r) { return (r & ~12) | ((r & 4) << 1) | ((r & 8) >> 1); }
DEV float cosr(int n, float inv) { return __builtin_amdgcn_cosf((float)n * inv); }
DEV float sinr(int n, float inv) { return __builtin_amdgcn_sinf((float)n * inv); }
DEV void dma16(const void* base, unsigned voff, unsigned ldsaddr) {
  asm volatile("s_mov_b32 m0, %2\n\ts_nop 0\n\tglobal_load_lds_dwordx4 %0, %1" ::"v"(voff), "s"(base), "s"(ldsaddr) : "memory");
}
DEV unsigned ldsaddr_of(const char* p) { return (unsigned)(unsigned long long)(const LDSP char*)p; }
DEV u32x4 ald128(const void* base, unsigned off) {
  u32x4 r; asm volatile("global_load_dwordx4 %0, %1, %2" : "=v"(r) : "v"(off), "s"(base) : "memory"); return r;
}
DEV void st128(void* p, u32x4 v) { asm volatile("global_store_dwordx4 %0, %1, off sc1\n\ts_nop 1" ::"v"(p), "v"(v) : "memory"); }
#define SB() __builtin_amdgcn_sched_barrier(0)
DEV u32x2 ldtr(const char* l) {
  return __builtin_bit_cast(u32x2, __builtin_amdgcn_ds_read_tr16_b64_v4i16((LDSP s16x4*)l));
}
DEV float bflo(unsigned u) { return __builtin_bit_cast(float, u << 16); }
DEV float bfhi(unsigned u) { return __builtin_bit_cast(float, u & 0xffff0000u); }
#define RAW_BARRIER() do { asm volatile("s_waitcnt lgkmcnt(0)" ::: "memory"); __builtin_amdgcn_s_barrier(); asm volatile("" ::: "memory"); } while (0)
#define VMCNT(n) asm volatile("s_waitcnt vmcnt(" #n ")" ::: "memory")

constexpr float C71 = 0.62348980185873353f, C72 = -0.22252093395631440f, C73 = -0.90096886790241913f;
constexpr float S71 = 0.78183148246802981f, S72 = 0.97492791218182361f, S73 = 0.43388373911755812f;

DEV void gen_cs(int i, int k0, int mul, int add, int mask, float inv, u32x4& C, u32x4& S) {
  float c[8], s[8];
#pragma unroll
  for (int j = 0; j < 8; ++j) {
    int n = (__mul24(i, mul * k0 + add) + __mul24(i, mul) * j) & mask;
    c[j] = cosr(n, inv); s[j] = sinr(n, inv);
  }
#pragma unroll
  for (int j = 0; j < 4; ++j) { C[j] = pk2(c[2 * j], c[2 * j + 1]); S[j] = pk2(s[2 * j], s[2 * j + 1]); }
}

constexpr double ccos64(int n) {
  n &= 63; if (n > 32) n = 64 - n;
  double sg = 1.0; if (n > 16) { n = 32 - n; sg = -1.0; }
  if (n == 16) return 0.0;
  double x = 6.283185307179586476925 * n / 64.0, x2 = x * x, term = 1.0, sum = 1.0;
  for (int i = 1; i < 14; ++i) { term *= -x2 / ((2 * i - 1) * (2 * i)); sum += term; }
  return sg * sum;
}
constexpr unsigned cbf(double v) { unsigned u = __builtin_bit_cast(unsigned, (float)v); return (u + 0x7fffu + ((u >> 16) & 1u)) >> 16; }
constexpr int csw(int r) { return (r & ~12) | ((r & 4) << 1) | ((r & 8) >> 1); }
struct alignas(16) Tabs { unsigned f64[16 * 256]; unsigned b2[14 * 256]; };
constexpr Tabs make_tabs() {
  Tabs t{};
  unsigned c[64] = {}, sn[64] = {};
  for (int n = 0; n < 64; ++n) { c[n] = cbf(ccos64(n)); sn[n] = cbf(ccos64(n - 16 + 64)); }
  for (int f = 0; f < 8; ++f)
    for (int l = 0; l < 64; ++l)
      for (int d = 0; d < 4; ++d) {
        int i = 32 * (f >> 2) + (l & 31), k = 16 * (f & 3) + 8 * (l >> 5) + 2 * d;
        t.f64[((f * 2) * 64 + l) * 4 + d] = c[(i * k) & 63] | (c[(i * (k + 1)) & 63] << 16);
        t.f64[((f * 2 + 1) * 64 + l) * 4 + d] = sn[(i * k) & 63] | (sn[(i * (k + 1)) & 63] << 16);
      }
  for (int l = 0; l < 64; ++l)
    for (int d = 0; d < 4; ++d) {
      for (int f = 0; f < 4; ++f) {
        int n = (2 * csw(l & 31) * (8 * f + 4 * (l >> 5) + d)) & 63;
        t.b2[(f * 64 + l) * 4 + d] = c[n] | (sn[n] << 16);
        t.b2[((4 + f) * 64 + l) * 4 + d] = (sn[n] ^ 0x8000u) | (c[n] << 16);
      }
      for (int f = 0; f < 2; ++f) {
        int k = 16 * f + 8 * (l >> 5) + 2 * d, n0 = (2 * (l & 31) * k) & 63, n1 = (2 * (l & 31) * (k + 1)) & 63;
        t.b2[((8 + f) * 64 + l) * 4 + d] = c[n0] | (c[n1] << 16);
        t.b2[((10 + f) * 64 + l) * 4 + d] = sn[n0] | (sn[n1] << 16);
        t.b2[((12 + f) * 64 + l) * 4 + d] = (sn[n0] | (sn[n1] << 16)) ^ 0x80008000u;
      }
    }
  return t;
}
__device__ const Tabs g_tabs = make_tabs();
DEV unsigned ald32(const void* base, unsigned off) {
  unsigned r; asm volatile("global_load_dword %0, %1, %2" : "=v"(r) : "v"(off), "s"(base) : "memory"); return r;
}
DEV unsigned ald16(const void* base, unsigned off) {
  unsigned r; asm volatile("global_load_ushort %0, %1, %2" : "=v"(r) : "v"(off), "s"(base) : "memory"); return r;
}
DEV void ast32(void* base, unsigned off, unsigned v) { asm volatile("global_store_dword %0, %1, %2 sc1" ::"v"(off), "v"(v), "s"(base) : "memory"); }
DEV int col_bin(int c) { int ct = c >> 5, k = ct == 7 ? 7 * (c - 192) : 7 * ((ct ? ((ct + 1) & 1) * 32 : 0) + (c & 31)) + 64 * ((ct + 1) >> 1); return k >= 448 ? k - 448 : k; }

constexpr int K1_SLOT = 32 * 1024;
constexpr int K1_LDS = 128 * 1024;
constexpr unsigned CODE_BYTES = 26000;
struct QF { u32x4 x0[4], x1[4], w[4]; };

template <bool ISV>
DEV void qkv_loop(const char* lds, unsigned ld, const u16* xb, const u16* wb, int kn, unsigned xo, unsigned wo0, unsigned wo1,
                  int xoff, int woff, f32x16& a0, f32x16& a1, const char* wpf, unsigned& pf) {
  auto piece = [&](int slot, int i) {
    const unsigned d = ld + slot * K1_SLOT;
    if (i == 0) dma16(xb + kn * 64, xo, d);
    if (i == 1) dma16(wb + kn * (64 * 1344), wo0, d + 16384);
    if (i == 2) dma16(xb + kn * 64 + 64 * 448, xo, d + 8192);
    if (i == 3) { dma16(wb + kn * (64 * 1344), wo1, d + 24576); kn = kn == 6 ? 0 : kn + 1; }
  };
  auto rd = [&](QF& f, int slot, int ks) {
    const char* sl = lds + slot * K1_SLOT;
    f.x0[ks] = *(const u32x4*)(sl + (xoff ^ (ks * 32)));
    f.x1[ks] = *(const u32x4*)(sl + (xoff ^ (ks * 32)) + 4096);
    u32x2 wa = ldtr(sl + woff + ks * 16 * 224), wc = ldtr(sl + woff + ks * 16 * 224 + 224);
    f.w[ks] = u32x4{wa[0], wa[1], wc[0], wc[1]};
  };
  QF f[2];
#pragma unroll
  for (int kc = 0; kc < 8; ++kc) {
    if (kc < 7) { if (kc < 6) VMCNT(4); else VMCNT(0); __builtin_amdgcn_s_barrier(); asm volatile("" ::: "memory"); }
    if (kc == 6) asm volatile("global_load_dword %0, %1, off" : "+v"(pf) : "v"(wpf) : "memory");
#pragma unroll
    for (int ks = 0; ks < 4; ++ks) {
      if (kc < 7) rd(f[kc & 1], kc & 3, ks);
      if (kc > 0) {
        QF& g = f[(kc - 1) & 1];
        if (ISV) { a0 = mfma(g.x0[ks], g.w[ks], a0); a1 = mfma(g.x1[ks], g.w[ks], a1); }
        else     { a0 = mfma(g.w[ks], g.x0[ks], a0); a1 = mfma(g.w[ks], g.x1[ks], a1); }
      }
      if (kc < 5) piece((kc + 2) & 3, ks);
      SB();
    }
  }
}

__global__ __launch_bounds__(512, 2) void k1_kernel(const u16* __restrict__ ax, const u16* __restrict__ wqkv, const u16* __restrict__ bx,
                                                    u32x4* __restrict__ Qf, u32x4* __restrict__ KVf, u32x4* __restrict__ T,
                                                    const u16* __restrict__ Wr, const u16* __restrict__ Wi) {
  extern __shared__ __attribute__((aligned(16))) char lds[];
  const int tid = threadIdx.x, lane = tid & 63, w = __builtin_amdgcn_readfirstlane(tid >> 6);
  const int xcd = blockIdx.x & 7, idx = blockIdx.x >> 3;
  const int l31 = lane & 31, h = lane >> 5;
  const unsigned lb = ldsaddr_of(lds);
  unsigned long long pc0;
  asm volatile("s_getpc_b64 %0" : "=s"(pc0));
  const unsigned po = (tid & 255) * 128;
  unsigned pf = ald32(w < 4 ? (const char*)(pc0 & ~127ull) : (const char*)&g_tabs, (idx == 0 && po < (w < 4 ? CODE_BYTES : 30720u)) ? po : 0u);
  if (__builtin_expect(idx >= 8, 1)) {
    const int u = idx - 8, which = u >> 3, tm = (xcd & 3) * 4 + (u & 3), g = (xcd >> 2) * 2 + ((u >> 2) & 1);
    const int row = 8 * w + (lane >> 3);
    const unsigned xo = (row * 448 + ((lane & 7) ^ ((row >> 1) & 7)) * 8) * 2;
    unsigned wo2[2];
#pragma unroll
    for (int i = 0; i < 2; ++i) {
      int q = 64 * (w + 8 * i) + lane; if (q > 895) q = 895;
      int rho = q / 14, cc = q - rho * 14;
      wo2[i] = (((rho & ~7) + ((rho & 7) >> 1) + 4 * (rho & 1)) * 1344 + cc * 8) * 2;
    }
    const u16* xb = ax + tm * 128 * 448;
    const u16* wb = wqkv + which * 448 + g * 112;
    int kn = ((u & 3) * 2 + (u >> 2)) % 7;
#pragma unroll
    for (int i = 0; i < 2; ++i) {
      const unsigned d = lb + w * 1024 + i * K1_SLOT;
      dma16(xb + kn * 64, xo, d); dma16(wb + kn * (64 * 1344), wo2[0], d + 16384);
      dma16(xb + kn * 64 + 64 * 448, xo, d + 8192); dma16(wb + kn * (64 * 1344), wo2[1], d + 24576);
      kn = kn == 6 ? 0 : kn + 1;
    }
    SB();
    const int hh = w & 3, th = w >> 2, r0 = th * 64 + l31;
    const int xoff = r0 * 128 + ((h ^ ((r0 >> 1) & 7)) * 16);
    const int woff = 16384 + (8 * h + 2 * ((lane >> 2) & 3)) * 224 + (hh * 28 + ((lane >> 4) & 1) * 16 + 4 * (lane & 3)) * 2;
    const int head = g * 4 + hh, tile = tm * 4 + th * 2;
    const int pn = (u & 7) * 8 + w, pj = (pn & 1) ? 2041 - 8 * xcd - 64 * (pn >> 1) : 8 * xcd + 64 * (pn >> 1);
    const unsigned pw = (pj * 450 + (u < 8 ? l31 * 128 : 0)) & ~3u;
    const char* wpf = (const char*)(h ? Wi : Wr) + (pw > 921596u ? 921596u : pw);
    f32x16 a0 = {}, a1 = {};
    u32x4 o0, o1, o2, o3;
    if (which == 2) {
      qkv_loop<true>(lds, lb + w * 1024, xb, wb, kn, xo, wo2[0], wo2[1], xoff, woff, a0, a1, wpf, pf);
      o0 = packs<0>(a0); o1 = packs<1>(a0); o2 = packs<0>(a1); o3 = packs<1>(a1);
      if (l31 >= 28) { unsigned f = (l31 == 28) ? 0x3F803F80u : 0u; o0 = u32x4{f, f, f, f}; o1 = o0; o2 = o0; o3 = o0; }
    } else {
      qkv_loop<false>(lds, lb + w * 1024, xb, wb, kn, xo, wo2[0], wo2[1], xoff, woff, a0, a1, wpf, pf);
      const float sc = which ? 1.0f : 0.18898223650461362f * 1.4426950408889634f;
      a0 *= sc; a1 *= sc;
      o0 = packs<0>(a0); o1 = packs<1>(a0); o2 = packs<0>(a1); o3 = packs<1>(a1);
      if (h) { o1[2] = which ? 0x3F80u : 0u; o1[3] = 0; o3[2] = o1[2]; o3[3] = 0; }
    }
    u32x4* o = (which == 0 ? Qf + ((head * 64 + tile) * 2) * 64 : KVf + ((head * 64 + tile) * 4 + (which & 2)) * 64) + lane;
    const int st = which == 0 ? 128 : 256;
    st128(o, o0); st128(o + 64, o1); st128(o + st, o2); st128(o + st + 64, o3);
    asm volatile("s_waitcnt vmcnt(0)" : "+v"(pf) :: "memory");
  } else {
    const int b1 = xcd * 8 + idx, s2 = b1 >> 1, hb = b1 & 1;
    u32x4* F64 = (u32x4*)(lds + 56 * 1024);
    u32x4* Ap = (u32x4*)(lds + 72 * 1024);
    u32x4* Xf = (u32x4*)(lds + 88 * 1024);
    {
      const int row = 8 * w + (lane >> 3);
      const unsigned xo = ((s2 + 32 * ((row & 32) + sw23(row & 31))) * 448 + ((lane & 7) ^ ((row >> 1) & 7)) * 8) * 2;
#pragma unroll
      for (int m = 0; m < 7; ++m) dma16(bx + 64 * m, xo, lb + m * 8192 + w * 1024);
      dma16(g_tabs.f64 + w * 512, lane * 16, lb + 56 * 1024 + w * 2048); dma16(g_tabs.f64 + w * 512 + 256, lane * 16, lb + 57 * 1024 + w * 2048);
    }
    SB();
    const int mt = w >> 2, o = 2 * (w & 3) + h;
    {
      u32x4 C, S;
      gen_cs(32 * mt + l31, 8 * o, 32, s2, 2047, 1.0f / 2048, C, S);
      Ap[(w * 2 + 0) * 64 + lane] = C; Ap[(w * 2 + 1) * 64 + lane] = S;
    }
    char* xp = lds + (32 * mt + l31) * 128 + ((o ^ ((l31 >> 1) & 7)) * 16);
    int xm[7];
#pragma unroll
    for (int b = 0; b < 7; ++b) { int m = b - o; m += m < 0 ? 7 : 0; m += m < 0 ? 7 : 0; xm[b] = m * 8192; asm volatile("" : "+v"(xm[b])); }
    asm volatile("s_waitcnt vmcnt(2)" : "+v"(pf) :: "memory");
    RAW_BARRIER();
    {
      u32x4 xr[7], z[4];
#pragma unroll
      for (int b = 0; b < 7; ++b) xr[b] = *(const u32x4*)(xp + xm[b]);
      const float ca = hb ? C72 : C71, cb = hb ? C73 : C72, cc = hb ? C71 : C73, sa = hb ? S72 : S71, sb = hb ? -S73 : S72, sc = hb ? -S71 : S73;
      const float da = hb ? C73 : 1.0f, db = hb ? C71 : 1.0f, dc = hb ? C72 : 1.0f, ta = hb ? S73 : 0.0f, tb = hb ? -S71 : 0.0f, tc = hb ? S72 : 0.0f;
#pragma unroll
      for (int jp = 0; jp < 4; ++jp) {
        f32x2 v[7];
#pragma unroll
        for (int b = 0; b < 7; ++b) {
          v[b][0] = bflo(xr[(b - 2 * jp + 14) % 7][jp]);
          v[b][1] = bfhi(xr[(b - 2 * jp - 1 + 14) % 7][jp]);
        }
        f32x2 p1 = v[1] + v[6], p2 = v[2] + v[5], p3 = v[3] + v[4];
        f32x2 m1 = v[6] - v[1], m2 = v[5] - v[2], m3 = v[4] - v[3];
        f32x2 ra = v[0] + p1 * ca + p2 * cb + p3 * cc, ia = m1 * sa + m2 * sb + m3 * sc;
        f32x2 rb = v[0] + p1 * da + p2 * db + p3 * dc, ib = m1 * ta + m2 * tb + m3 * tc;
        z[0][jp] = pk2(ra[0], ra[1]); z[1][jp] = pk2(ia[0], ia[1]); z[2][jp] = pk2(rb[0], rb[1]); z[3][jp] = pk2(ib[0], ib[1]);
      }
#pragma unroll
      for (int c = 0; c < 4; ++c) *(u32x4*)(xp + c * 8192) = z[c];
    }
    VMCNT(0);
    RAW_BARRIER();
    const int wc = w & 3, ct = hb ? 3 + wc : (wc == 2 ? 0 : (wc == 3 ? 7 : wc + 1)), kt = w & 1;
    u32x4 ap[8];
    {
      f32x16 xre = {}, xim = {};
      const char* zp = lds + (w & 2) * 8192 + mt * 4096 + l31 * 128;
      const int zsw = (h ^ ((l31 >> 1) & 7)) * 16;
      u32x4 C[4], S[4], zr[4], zi[4];
#pragma unroll
      for (int tt = 0; tt < 4; ++tt) {
        zr[tt] = *(const u32x4*)(zp + (zsw ^ (tt * 32))); C[tt] = F64[((kt * 4 + tt) * 2) * 64 + lane];
        zi[tt] = *(const u32x4*)(zp + 8192 + (zsw ^ (tt * 32))); S[tt] = F64[((kt * 4 + tt) * 2 + 1) * 64 + lane];
      }
#pragma unroll
      for (int tt = 0; tt < 8; ++tt) ap[tt] = Ap[(mt * 8 + tt) * 64 + lane];
      SB();
#pragma unroll
      for (int tt = 0; tt < 4; ++tt) {
        xre = mfma(zr[tt], C[tt], xre); xim = mfma(zi[tt], C[tt], xim);
        xre = mfma(zi[tt], S[tt], xre); xim = mfma(zr[tt], S[tt] ^ 0x80008000u, xim);
      }
      u32x4* xq = Xf + (wc * 4 + mt * 2) * 128 + lane;
      xq[0] = packs<0>(xre); xq[64] = packs<0>(xim); xq[128] = packs<1>(xre); xq[192] = packs<1>(xim);
    }
    RAW_BARRIER();
    {
      f32x16 tre = {}, tim = {};
      u32x4 bf[8];
#pragma unroll
      for (int tt = 0; tt < 8; ++tt) bf[tt] = Xf[(wc * 8 + tt) * 64 + lane];
      SB();
#pragma unroll
      for (int tt = 0; tt < 4; ++tt) {
        tre = mfma(ap[2 * tt], bf[2 * tt], tre); tim = mfma(ap[2 * tt], bf[2 * tt + 1], tim);
        tre = mfma(ap[2 * tt + 1], bf[2 * tt + 1], tre); tim = mfma(ap[2 * tt + 1] ^ 0x80008000u, bf[2 * tt], tim);
      }
      u32x4* tp = T + (s2 * 16 + mt * 8 + h) * 232 + ct * 32 + l31;
      if (ct < 7 || l31 < 8)
#pragma unroll
      for (int q = 0; q < 4; ++q)
        st128(tp + 2 * q * 232, u32x4{pk2(tre[4 * q], tim[4 * q]), pk2(tre[4 * q + 1], tim[4 * q + 1]), pk2(tre[4 * q + 2], tim[4 * q + 2]), pk2(tre[4 * q + 3], tim[4 * q + 3])});
    }
  }
}

constexpr int K2_LDS = 84 * 1024;
DEV float xhalf_max(float v) {
  float a = v, b = v;
  asm volatile("s_nop 1\n\tv_permlane32_swap_b32 %0, %1\n\ts_nop 1" : "+v"(a), "+v"(b));
  return fmaxf(a, b);
}
DEV float max16(const f32x16& s) {
  float m = fmaxf(fmaxf(s[0], s[1]), s[2]);
#pragma unroll
  for (int i = 3; i < 15; i += 2) m = fmaxf(fmaxf(m, s[i]), s[i + 1]);
  return fmaxf(m, s[15]);
}
#define PIN16(a) asm volatile("" : "+v"(a[0]), "+v"(a[1]), "+v"(a[2]), "+v"(a[3]), "+v"(a[4]), "+v"(a[5]), "+v"(a[6]), "+v"(a[7]), \
                                   "+v"(a[8]), "+v"(a[9]), "+v"(a[10]), "+v"(a[11]), "+v"(a[12]), "+v"(a[13]), "+v"(a[14]), "+v"(a[15]))
#define EX4(s, i, P, j)                                                                                          \
  { const float t0_ = __builtin_amdgcn_exp2f(s[i]); SB(); P[j] = pk2(e0, e1); SB();                               \
    const float t1_ = __builtin_amdgcn_exp2f(s[i + 1]); SB(); P[j + 1] = pk2(e2, e3); SB();                       \
    e0 = t0_; e1 = t1_; e2 = __builtin_amdgcn_exp2f(s[i + 2]); SB(); e3 = __builtin_amdgcn_exp2f(s[i + 3]); SB(); }
#define TILE(sc, sn, K0, K1, Q0, Q1, oc, V0, op, V1, L0, L1, L2, L3)                 \
  sn = mfma(K0, Q0, f32x16{}); SB(); L0; EX4(sc, 0, p1, 2)                           \
  op = mfma(V1, p1, op); SB(); L1; EX4(sc, 4, p0, 0)                                 \
  sn = mfma(K1, Q1, sn); SB(); L2; EX4(sc, 8, p0, 2)                                 \
  oc = mfma(V0, p0, oc); SB(); L3; EX4(sc, 12, p1, 0)
#define KTILE(X, Y, P1, P2)                                                                                      \
  TILE(sa, sb, X##k0, X##k1, q0[1], q1[1], o[0], X##v0, o[3], Y##v1, , Y##v1 = (P1)[192], , )                     \
  TILE(sb, sa, X##k0, X##k1, q0[2], q1[2], o[1], X##v0, o[0], X##v1, , , , )                                      \
  TILE(sa, sb, X##k0, X##k1, q0[3], q1[3], o[2], X##v0, o[1], X##v1, X##k0 = (P2)[0], , X##k1 = (P2)[64], )       \
  TILE(sb, sa, Y##k0, Y##k1, q0[0], q1[0], o[3], X##v0, o[2], X##v1, , , , X##v0 = (P2)[128])

__global__ __launch_bounds__(384, 2) void k2_kernel(const u32x4* __restrict__ Qf, const u32x4* __restrict__ KVf, u32x4* __restrict__ Of,
                                                    const unsigned* __restrict__ T, unsigned* __restrict__ U,
                                                    const u16* __restrict__ Wr, const u16* __restrict__ Wi, const u16* __restrict__ wo) {
  extern __shared__ __attribute__((aligned(16))) char lds[];
  const int tid = threadIdx.x, lane = tid & 63, w = __builtin_amdgcn_readfirstlane(tid >> 6);
  const int xcd = blockIdx.x & 7, idx = blockIdx.x >> 3;
  const int l31 = lane & 31, h = lane >> 5;
  if (__builtin_expect(w < 4, 1)) {
    __builtin_amdgcn_s_setprio(2);
    const int head = xcd * 2 + (idx >> 4), qt = (idx & 15) * 4;
    const u32x4* kp = KVf + (head * 64 + w * 16) * 256 + lane;
    u32x4 q0[4], q1[4];
    u32x4 ek0 = kp[0], ek1 = kp[64];
#pragma unroll
    for (int i = 0; i < 4; ++i) { const u32x4* qp = Qf + (head * 64 + qt + ((w + i) & 3)) * 128 + lane; q0[i] = qp[0]; q1[i] = qp[64]; }
    u32x4 ev0 = kp[128], ev1 = kp[192], dk0 = kp[256], dk1 = kp[320], dv0 = kp[384], dv1 = {}, p0, p1 = {};
    f32x16 o[4] = {}, sa, sb;
    float m[4], e0 = 0.f, e1 = 0.f, e2 = 0.f, e3 = 0.f;
    {
      f32x16 s4[4];
#pragma unroll
      for (int i = 0; i < 4; ++i) { s4[i] = mfma(ek0, q0[i], f32x16{}); s4[i] = mfma(ek1, q1[i], s4[i]); }
#pragma unroll
      for (int i = 0; i < 4; ++i) {
        const unsigned nm = pk2(-xhalf_max(max16(s4[i])), 0.0f);
        m[i] = -bflo(nm);
        if (h) q1[i][2] = nm;
      }
      sa = s4[0] - m[0];
    }
#pragma unroll 1
    for (int it = 0; it < 8; ++it) {
      const u32x4* kn = kp + ((2 * it + 2) & 15) * 256;
      KTILE(e, d, kp + (2 * it + 1) * 256, kn)
      KTILE(d, e, kn, kn + 256)
    }
    p1[2] = pk2(e0, e1); p1[3] = pk2(e2, e3);
    o[3] = mfma(dv1, p1, o[3]);
    if (__builtin_expect(__ballot(h && !(o[0][12] + o[1][12] + o[2][12] + o[3][12] < 1.0e30f)) != 0ull, 0)) {
#pragma unroll
      for (int u = 0; u < 4; ++u) {
        f32x16 oo = {}; float mm = -3.0e38f;
        if (h) q1[u][2] = 0;
#pragma unroll 1
        for (int t = 0; t < 16; ++t) {
          const u32x4* p = kp + t * 256;
          f32x16 s = mfma(p[0], q0[u], f32x16{}); s = mfma(p[64], q1[u], s);
          float mn = fmaxf(mm, xhalf_max(max16(s)));
          float al = __builtin_amdgcn_exp2f(mm - mn);
          mm = mn;
#pragma unroll
          for (int i = 0; i < 16; ++i) { oo[i] *= al; s[i] = __builtin_amdgcn_exp2f(s[i] - mn); }
          oo = mfma(p[128], packs<0>(s), oo); oo = mfma(p[192], packs<1>(s), oo);
        }
        o[u] = oo; m[u] = mm;
      }
    }
#pragma unroll
    for (int u = 1; u < 4; ++u) {
      if (h) o[u][13] = m[u];
      f32x4* ex = (f32x4*)(lds + (((w + u) & 3) * 3 + u - 1) * 4096) + lane;
#pragma unroll
      for (int i = 0; i < 4; ++i) ex[i * 64] = f32x4{o[u][4 * i], o[u][4 * i + 1], o[u][4 * i + 2], o[u][4 * i + 3]};
    }
    const float l0 = xhalf_max(h ? o[0][12] : -1.0f);
    RAW_BARRIER();
    const char* ey = lds + w * 12288;
    f32x2 st[3];
#pragma unroll
    for (int u = 0; u < 3; ++u) st[u] = *(const f32x2*)(ey + u * 4096 + 3584 + l31 * 16);
    f32x4 y[12];
#pragma unroll
    for (int i = 0; i < 12; ++i) y[i] = *(const f32x4*)(ey + i * 1024 + lane * 16);
    SB();
    const float mm = fmaxf(fmaxf(st[0][1], st[1][1]), fmaxf(st[2][1], m[0]));
    float f0 = __builtin_amdgcn_exp2f(m[0] - mm), f[3];
#pragma unroll
    for (int u = 0; u < 3; ++u) f[u] = __builtin_amdgcn_exp2f(st[u][1] - mm);
    const float inv = __builtin_amdgcn_rcpf(l0 * f0 + st[0][0] * f[0] + st[1][0] * f[1] + st[2][0] * f[2]);
    f0 *= inv;
    f32x16 r = o[0] * f0;
    asm volatile("" : "+v"(r) : "v"(f0));
#pragma unroll
    for (int u = 0; u < 3; ++u) {
      const float fu = f[u] * inv;
#pragma unroll
      for (int i = 0; i < 4; ++i) {
        const f32x4 v = y[u * 4 + i];
        r[4 * i] += v[0] * fu; r[4 * i + 1] += v[1] * fu; r[4 * i + 2] += v[2] * fu; r[4 * i + 3] += v[3] * fu;
      }
    }
    u32x4 r1 = packs<1>(r);
    if (h) { r1[2] = 0; r1[3] = 0; }
    u32x4* op = Of + (((qt + w) * 16 + head) * 2) * 64 + lane;
    st128(op, packs<0>(r)); st128(op + 64, r1);
  } else if (idx < 29) {
    const int jq = xcd * 2 + w - 4, col = idx * 8 + (l31 >> 2), kb = col_bin(col), jr = 4 * jq + (lane & 3) + 512 * h;
    const unsigned toff = (4 * h * 16 + jq) * 928 + idx * 32 + l31;
    const bool cj = kb > 224;
    const int of = jr * 450 + 2 * kb, om = (2048 - jr) * 450 + 2 * (448 - kb), om0 = jr ? om : om - 921600;
    const float sr = col > 224 ? 0.0f : ((col == 0 || col == 224) ? 0.5f : 1.0f), si = cj ? -sr : sr;
    u32x4 mt[14];
    unsigned ta[16], ur[16], ui[16]; float wr[16], wi[16];
#pragma unroll
    for (int i = 0; i < 16; ++i) ta[i] = ald32(T + (8 * (i >> 2) + (i & 3)) * 14848, toff * 4);
#pragma unroll
    for (int g = 0; g < 16; ++g) {
      const unsigned o_ = g ? (cj ? om : of) + ((g & 7) + 16 * (g >> 3)) * (cj ? -28800 : 28800) : (cj ? om0 : of);
      ur[g] = ald16(Wr, o_); ui[g] = ald16(Wi, o_);
    }
    const unsigned* tabp = g_tabs.b2;
    asm volatile("" : "+s"(tabp));
#pragma unroll
    for (int t = 0; t < 14; ++t) mt[t] = ald128(tabp + t * 256, lane * 16);
    asm volatile("s_waitcnt vmcnt(0)" : "+v"(mt[0]), "+v"(mt[1]), "+v"(mt[2]), "+v"(mt[3]), "+v"(mt[4]), "+v"(mt[5]), "+v"(mt[6]), "+v"(mt[7]), "+v"(mt[8]),
                 "+v"(mt[9]), "+v"(mt[10]), "+v"(mt[11]), "+v"(mt[12]), "+v"(mt[13]) :: "memory");
    PIN16(ta); PIN16(ur); PIN16(ui);
#pragma unroll
    for (int g = 0; g < 16; ++g) { wr[g] = bflo(ur[g]); wi[g] = bflo(ui[g]); }
    SB();
    f32x16 xr = {}, xi = {};
#pragma unroll
    for (int t = 0; t < 4; ++t) {
      u32x4 b = {ta[4 * t], ta[4 * t + 1], ta[4 * t + 2], ta[4 * t + 3]};
      xr = mfma(mt[t], b, xr); xi = mfma(mt[4 + t], b, xi);
    }
    f32x16 yr, yi;
#pragma unroll
    for (int g = 0; g < 16; g += 2) {
      f32x2 c = {wr[g], wr[g + 1]}, d = {wi[g], wi[g + 1]};
      asm volatile("" : "+v"(c), "+v"(d));
      c *= sr; d *= si;
      const f32x2 a = {xr[g], xr[g + 1]}, b = {xi[g], xi[g + 1]}, r = a * c - b * d, i = a * d + b * c;
      yr[g] = r[0]; yr[g + 1] = r[1]; yi[g] = i[0]; yi[g + 1] = i[1];
    }
    u32x4 r0 = packs<0>(yr), r1 = packs<1>(yr), i0 = packs<0>(yi), i1 = packs<1>(yi);
    f32x16 ure = {}, uim = {};
    ure = mfma(mt[8], r0, ure); uim = mfma(mt[8], i0, uim);
    ure = mfma(mt[12], i0, ure); uim = mfma(mt[10], r0, uim);
    ure = mfma(mt[9], r1, ure); uim = mfma(mt[9], i1, uim);
    ure = mfma(mt[13], i1, ure); uim = mfma(mt[11], r1, uim);
#pragma unroll
    for (int g = 0; g < 16; ++g) ast32(U + ((g & 3) + 8 * (g >> 2)) * 14848, toff * 4, pk2(ure[g], uim[g]));
    {
      const unsigned off = ((idx * 2 + w - 4) * 64 + lane) * 128;
      if (off < 401408u) { unsigned pf = *(const unsigned*)((const char*)wo + off); asm volatile("" ::"v"(pf)); }
    }
  }
}

constexpr int ZS = 68;
constexpr int K3_LDS = 77 * 1024;

__global__ __launch_bounds__(512, 4) void k3_kernel(const u32x4* __restrict__ Of, const u16* __restrict__ wo, const u32x4* __restrict__ U,
                                                    u16* __restrict__ out) {
  extern __shared__ __attribute__((aligned(16))) char lds[];
  const int tid = threadIdx.x, lane = tid & 63, w = __builtin_amdgcn_readfirstlane(tid >> 6);
  const int xcd = blockIdx.x & 7, idx = blockIdx.x >> 3;
  const int l31 = lane & 31, h = lane >> 5;
  if (idx < 4 || idx >= 12) {
    if (w >= 4) return;
    const int u = idx < 4 ? idx : idx - 8, tq = xcd * 4 + u / 7, tn = u % 7;
    const int qi = w & 1, kh = w >> 1;
    u32x4 of[16];
    {
      const int r8 = lane >> 3;
      const unsigned so = (r8 * 448 + ((lane & 7) ^ ((r8 & 2) << 1)) * 8) * 2, l16 = lane * 16;
      const u16* src = wo + (kh * 224 + qi * 8) * 448 + tn * 64;
      const unsigned d = ldsaddr_of(lds) + (kh * 224 + qi * 8) * 128;
      const u32x4* op = Of + (((tq * 2 + qi) * 16 + kh * 8) * 2) * 64;
#pragma unroll
      for (int st = 0; st < 2; ++st) {
#pragma unroll
        for (int i = 0; i < 7; ++i) dma16(src + (st * 7 + i) * 16 * 448, so, d + (st * 7 + i) * 2048);
#pragma unroll
        for (int i = 0; i < 8; ++i) of[st * 8 + i] = ald128(op + (st * 8 + i) * 64, l16);
      }
    }
    const int q = (lane >> 2) & 3, p = lane & 3, pc = ((p & 1) << 1) | (p >> 1);
    const int colb = ((lane >> 4) & 1) * 32 + pc * 8;
    const int base = (kh * 224 + q) * 128 + (colb ^ (((q >> 1) ^ kh) << 6));
    f32x16 a0 = {}, a1 = {};
#pragma unroll
    for (int st = 0; st < 2; ++st) {
      if (st == 0) asm volatile("s_waitcnt vmcnt(15)" : "+v"(of[0]), "+v"(of[1]), "+v"(of[2]), "+v"(of[3]), "+v"(of[4]), "+v"(of[5]), "+v"(of[6]), "+v"(of[7]) :: "memory");
      else asm volatile("s_waitcnt vmcnt(0)" : "+v"(of[8]), "+v"(of[9]), "+v"(of[10]), "+v"(of[11]), "+v"(of[12]), "+v"(of[13]), "+v"(of[14]), "+v"(of[15]) :: "memory");
      RAW_BARRIER();
      u32x4 fx[3], fy[3];
#pragma unroll
      for (int n = 0; n < 10; ++n) {
        if (n < 8) {
          const int i = st * 8 + n, hd = i >> 1, s = i & 1;
          const char* pa = lds + (hd * 28 + 16 * s) * 128;
          const int ra = base + h * 512, rb = (s ? base : base + h * 512) + 1024;
          u32x2 x0 = ldtr(pa + ra), x1 = ldtr(pa + rb), y0 = ldtr(pa + (ra ^ 64)), y1 = ldtr(pa + (rb ^ 64));
          fx[n % 3] = u32x4{x0[0], x0[1], x1[0], x1[1]}; fy[n % 3] = u32x4{y0[0], y0[1], y1[0], y1[1]};
        }
        SB();
        if (n >= 2) { a0 = mfma(fx[(n - 2) % 3], of[st * 8 + n - 2], a0); a1 = mfma(fy[(n - 2) % 3], of[st * 8 + n - 2], a1); }
        SB();
      }
    }
    f32x4* ex = (f32x4*)(lds + 56 * 1024);
    f32x16 snd = a1, kp = a0;
#pragma unroll
    for (int i = 0; i < 4; ++i) ex[(w * 4 + i) * 64 + lane] = f32x4{snd[4 * i], snd[4 * i + 1], snd[4 * i + 2], snd[4 * i + 3]};
    RAW_BARRIER();
#pragma unroll
    for (int i = 0; i < 4; ++i) {
      f32x4 v = ex[((w ^ 2) * 4 + i) * 64 + lane];
      kp[4 * i] += v[0]; kp[4 * i + 1] += v[1]; kp[4 * i + 2] += v[2]; kp[4 * i + 3] += v[3];
    }
    u16* dst = out + ((tq * 2 + qi) * 32 + l31) * 896 + tn * 64 + kh * 32 + 8 * h;
    st128(dst, packs<0>(kp)); st128(dst + 16, packs<1>(kp));
  } else {
    const int b3 = xcd * 8 + idx - 4, s2 = b3 >> 1, sh = b3 & 1, r = w;
    u32x4* Dm = (u32x4*)lds;
    u32x4* F64 = (u32x4*)(lds + 61 * 1024);
    float* zs = (float*)lds;
    u32x4 ua[8], ub[8];
    if (__builtin_expect(w < 4, 1)) {
      const u32x4* up = U + s2 * 16 * 232 + (r ? 2 * r - 1 : 0) * 32;
      const unsigned uo = (h * 232 + sw23(l31)) * 16, uo2 = (h * 232 + (sw23(l31) & (r ? 31 : 7)) + (r ? 32 : 224)) * 16;
#pragma unroll
      for (int i = 0; i < 8; ++i) { ua[i] = ald128(up + i * 464, uo); ub[i] = ald128(up + i * 464, uo2); }
    } else {
      const unsigned pl = (h * 32 + sw23(l31)) * 16;
#pragma unroll
      for (int i = 0; i < 4; ++i) dma16(g_tabs.f64 + ((w & 3) * 4 + i) * 256, pl, ldsaddr_of(lds) + 61 * 1024 + ((w & 3) * 4 + i) * 1024);
    }
    SB();
    {
      const int s = s2 + 32 * (32 * sh + l31);
      const float NRM2 = 2.0f / (2048.0f * 448.0f);
      u32x4 dr, di;
#pragma unroll
      for (int i = 0; i < 4; ++i) {
        int n = __mul24(8 * w + 4 * h + i, s) & 2047;
        float c = cosr(n, 1.0f / 2048) * NRM2, sn = sinr(n, 1.0f / 2048) * NRM2;
        dr[i] = pk2(c, -sn); di[i] = pk2(sn, c);
      }
      Dm[(w * 2) * 64 + lane] = dr; Dm[(w * 2 + 1) * 64 + lane] = di;
    }
    RAW_BARRIER();
    if (__builtin_expect(w < 4, 1)) {
      f32x16 ar = {}, ai = {}, br = {}, bi = {};
#pragma unroll
      for (int t = 0; t < 8; ++t) {
        u32x4 dr = Dm[(t * 2) * 64 + lane], di = Dm[(t * 2 + 1) * 64 + lane];
        asm volatile("s_waitcnt vmcnt(%2)" : "+v"(ua[t]), "+v"(ub[t]) : "n"(14 - 2 * t) : "memory");
        ar = mfma(ua[t], dr, ar); ai = mfma(ua[t], di, ai); br = mfma(ub[t], dr, br); bi = mfma(ub[t], di, bi);
      }
      RAW_BARRIER();
      u32x4 yr[4] = {packs<0>(ar), packs<1>(ar), packs<0>(br), packs<1>(br)};
      u32x4 yi[4] = {packs<0>(ai), packs<1>(ai), packs<0>(bi), packs<1>(bi)};
      const unsigned k2 = r ? ~0u : 0u, k1 = (r || !h) ? ~0u : 0u;
      yr[2] &= k1; yr[3] &= k2; yi[2] &= k2; yi[3] &= k2;
      float* zp = zs + ((r ? 2 * r - 1 : 0) * 32 + l31) * ZS + 8 * h;
      float* zq = zp + (r ? 32 * ZS : 0);
      f32x16 zre[2] = {}, zim[2] = {};
      u32x4 C = F64[lane], S = F64[64 + lane];
#pragma unroll
      for (int n = 0; n < 12; ++n) {
        const int at = n >> 2, t = n & 3;
        if (n < 8) {
          const u32x4 Cn = F64[((n + 1) & 7) * 128 + lane], Sn = F64[((n + 1) & 7) * 128 + 64 + lane];
          SB();
          zim[at] = mfma(C, yi[t], zim[at]); zre[at] = mfma(C, yr[t], zre[at]);
          zim[at] = mfma(S, yr[t], zim[at]); zre[at] = mfma(S ^ 0x80008000u, yi[t], zre[at]);
          C = Cn; S = Sn;
          SB();
        }
        if (n >= 4) {
          const int off = (at - 1) * 32 + 16 * (t >> 1) + 4 * (t & 1);
          *(f32x4*)(zq + off) = f32x4{zim[at - 1][4 * t], zim[at - 1][4 * t + 1], zim[at - 1][4 * t + 2], zim[at - 1][4 * t + 3]};
          *(f32x4*)(zp + off) = f32x4{zre[at - 1][4 * t], zre[at - 1][4 * t + 1], zre[at - 1][4 * t + 2], zre[at - 1][4 * t + 3]};
        }
      }
    } else {
      VMCNT(0);
      RAW_BARRIER();
    }
    RAW_BARRIER();
    {
      const int o = tid & 15, row = tid >> 4, rot = ((o >> 1) + 4 * (o & 1)) % 7;
      float* zp = zs + row * ZS + 4 * o;
      f32x4 zl[7];
#pragma unroll
      for (int c = 0; c < 7; ++c) zl[c] = *(const f32x4*)(zp + c * 32 * ZS);
      float y[4][7];
#pragma unroll
      for (int jp = 0; jp < 2; ++jp) {
        f32x2 z[7];
#pragma unroll
        for (int c = 0; c < 7; ++c) z[c] = f32x2{zl[c][2 * jp], zl[c][2 * jp + 1]};
        f32x2 y0 = z[0] + z[1] + z[3] + z[5];
        f32x2 e1 = z[0] + z[1] * C71 + z[3] * C72 + z[5] * C73, o1 = z[2] * S71 + z[4] * S72 + z[6] * S73;
        f32x2 e2 = z[0] + z[1] * C72 + z[3] * C73 + z[5] * C71, o2 = z[2] * S72 - z[4] * S73 - z[6] * S71;
        f32x2 e3 = z[0] + z[1] * C73 + z[3] * C71 + z[5] * C72, o3 = z[2] * S73 - z[4] * S71 + z[6] * S72;
        f32x2 yy[7] = {y0, e1 - o1, e2 - o2, e3 - o3, e3 + o3, e2 + o2, e1 + o1};
#pragma unroll
        for (int b = 0; b < 7; ++b) { y[2 * jp][b] = yy[b][0]; y[2 * jp + 1][b] = yy[b][1]; }
      }
#pragma unroll
      for (int b = 0; b < 7; ++b) {
        int m = b - rot; m += m < 0 ? 7 : 0;
        *(u32x2*)(zp + m * 32 * ZS) = u32x2{pk2(y[0][b], y[1][(b + 1) % 7]), pk2(y[2][(b + 2) % 7], y[3][(b + 3) % 7])};
      }
      u16* dst = out + (s2 + 32 * (32 * sh + row)) * 896 + 448 + 4 * o;
#pragma unroll
      for (int m = 0; m < 7; ++m) { const u32x2 v_ = *(const u32x2*)(zp + m * 32 * ZS); asm volatile("global_store_dwordx2 %0, %1, off sc1" ::"v"(dst + 64 * m), "v"(v_) : "memory"); }
    }
  }
}

extern "C" void kernel_launch(void* const* d_in, const int* in_sizes, int n_in, void* d_out, int out_size, void* d_ws, size_t ws_size,
                              hipStream_t stream) {
  const u16* ax = (const u16*)d_in[0];
  const u16* wqkv = (const u16*)d_in[1];
  const u16* wo = (const u16*)d_in[2];
  const u16* bx = (const u16*)d_in[3];
  const u16* wr = (const u16*)d_in[4];
  const u16* wi = (const u16*)d_in[5];
  char* ws = (char*)d_ws;
  u32x4* Qf = (u32x4*)(ws);
  u32x4* KVf = (u32x4*)(ws + (2u << 20));
  u32x4* Of = (u32x4*)(ws + (6u << 20));
  unsigned* T = (unsigned*)(ws + (8u << 20));
  unsigned* U = (unsigned*)(ws + (10u << 20));
  (void)hipFuncSetAttribute((const void*)k1_kernel, hipFuncAttributeMaxDynamicSharedMemorySize, K1_LDS);
  (void)hipFuncSetAttribute((const void*)k2_kernel, hipFuncAttributeMaxDynamicSharedMemorySize, K2_LDS);
  (void)hipFuncSetAttribute((const void*)k3_kernel, hipFuncAttributeMaxDynamicSharedMemorySize, K3_LDS);
  k1_kernel<<<256, 512, K1_LDS, stream>>>(ax, wqkv, bx, Qf, KVf, (u32x4*)T, wr, wi);
  k2_kernel<<<256, 384, K2_LDS, stream>>>(Qf, KVf, Of, T, U, wr, wi, wo);
  k3_kernel<<<288, 512, K3_LDS, stream>>>(Of, wo, (const u32x4*)U, (u16*)d_out);
}
```

```cpp
#include <hip/hip_runtime.h>
#include <hip/hip_bf16.h>

typedef __attribute__((ext_vector_type(8))) __bf16 bf16x8;
typedef __attribute__((ext_vector_type(4))) __bf16 bf16x4;
typedef __attribute__((ext_vector_type(2))) __bf16 bf16x2;
typedef __attribute__((ext_vector_type(4))) short s16x4;
typedef __attribute__((ext_vector_type(16))) float f32x16;
typedef __attribute__((ext_vector_type(4))) float f32x4;
typedef __attribute__((ext_vector_type(2))) float f32x2;
typedef __attribute__((ext_vector_type(4))) unsigned u32x4;
typedef __attribute__((ext_vector_type(2))) unsigned u32x2;
typedef unsigned short u16;
#define LDSP __attribute__((address_space(3)))
#define DEV __device__ __forceinline__

DEV unsigned pk2(float lo, float hi) {
  f32x2 v = {lo, hi};
  return __builtin_bit_cast(unsigned, __builtin_convertvector(v, bf16x2));
}
DEV f32x16 mfma(u32x4 a, u32x4 b, f32x16 c) {
  return __builtin_amdgcn_mfma_f32_32x32x16_bf16(__builtin_bit_cast(bf16x8, a), __builtin_bit_cast(bf16x8, b), c, 0, 0, 0);
}
template <int S> DEV u32x4 packs(const f32x16& v) {
  u32x4 r = {pk2(v[8 * S + 0], v[8 * S + 1]), pk2(v[8 * S + 2], v[8 * S + 3]), pk2(v[8 * S + 4], v[8 * S + 5]), pk2(v[8 * S + 6], v[8 * S + 7])};
  return r;
}
DEV int sw23(int r) { return (r & ~12) | ((r & 4) << 1) | ((r & 8) >> 1); }
DEV float cosr(int n, float inv) { return __builtin_amdgcn_cosf((float)n * inv); }
DEV float sinr(int n, float inv) { return __builtin_amdgcn_sinf((float)n * inv); }
DEV void dma16(const void* base, unsigned voff, unsigned ldsaddr) {
  asm volatile("s_mov_b32 m0, %2\n\ts_nop 0\n\tglobal_load_lds_dwordx4 %0, %1" ::"v"(voff), "s"(base), "s"(ldsaddr) : "memory");
}
DEV unsigned ldsaddr_of(const char* p) { return (unsigned)(unsigned long long)(const LDSP char*)p; }
DEV u32x4 ald128(const void* base, unsigned off) {
  u32x4 r; asm volatile("global_load_dwordx4 %0, %1, %2" : "=v"(r) : "v"(off), "s"(base) : "memory"); return r;
}
DEV void st128(void* p, u32x4 v) { asm volatile("global_store_dwordx4 %0, %1, off sc1\n\ts_nop 1" ::"v"(p), "v"(v) : "memory"); }
#define SB() __builtin_amdgcn_sched_barrier(0)
DEV u32x2 ldtr(const char* l) {
  return __builtin_bit_cast(u32x2, __builtin_amdgcn_ds_read_tr16_b64_v4i16((LDSP s16x4*)l));
}
DEV float bflo(unsigned u) { return __builtin_bit_cast(float, u << 16); }
DEV float bfhi(unsigned u) { return __builtin_bit_cast(float, u & 0xffff0000u); }
#define RAW_BARRIER() do { asm volatile("s_waitcnt lgkmcnt(0)" ::: "memory"); __builtin_amdgcn_s_barrier(); asm volatile("" ::: "memory"); } while (0)
#define VMCNT(n) asm volatile("s_waitcnt vmcnt(" #n ")" ::: "memory")

constexpr float C71 = 0.62348980185873353f, C72 = -0.22252093395631440f, C73 = -0.90096886790241913f;
constexpr float S71 = 0.78183148246802981f, S72 = 0.97492791218182361f, S73 = 0.43388373911755812f;

DEV void gen_cs(int i, int k0, int mul, int add, int mask, float inv, u32x4& C, u32x4& S) {
  float c[8], s[8];
#pragma unroll
  for (int j = 0; j < 8; ++j) {
    int n = (__mul24(i, mul * k0 + add) + __mul24(i, mul) * j) & mask;
    c[j] = cosr(n, inv); s[j] = sinr(n, inv);
  }
#pragma unroll
  for (int j = 0; j < 4; ++j) { C[j] = pk2(c[2 * j], c[2 * j + 1]); S[j] = pk2(s[2 * j], s[2 * j + 1]); }
}

constexpr double ccos64(int n) {
  n &= 63; if (n > 32) n = 64 - n;
  double sg = 1.0; if (n > 16) { n = 32 - n; sg = -1.0; }
  if (n == 16) return 0.0;
  double x = 6.283185307179586476925 * n / 64.0, x2 = x * x, term = 1.0, sum = 1.0;
  for (int i = 1; i < 14; ++i) { term *= -x2 / ((2 * i - 1) * (2 * i)); sum += term; }
  return sg * sum;
}
constexpr unsigned cbf(double v) { unsigned u = __builtin_bit_cast(unsigned, (float)v); return (u + 0x7fffu + ((u >> 16) & 1u)) >> 16; }
constexpr int csw(int r) { return (r & ~12) | ((r & 4) << 1) | ((r & 8) >> 1); }
struct alignas(16) Tabs { unsigned f64[16 * 256]; unsigned b2[14 * 256]; };
constexpr Tabs make_tabs() {
  Tabs t{};
  unsigned c[64] = {}, sn[64] = {};
  for (int n = 0; n < 64; ++n) { c[n] = cbf(ccos64(n)); sn[n] = cbf(ccos64(n - 16 + 64)); }
  for (int f = 0; f < 8; ++f)
    for (int l = 0; l < 64; ++l)
      for (int d = 0; d < 4; ++d) {
        int i = 32 * (f >> 2) + (l & 31), k = 16 * (f & 3) + 8 * (l >> 5) + 2 * d;
        t.f64[((f * 2) * 64 + l) * 4 + d] = c[(i * k) & 63] | (c[(i * (k + 1)) & 63] << 16);
        t.f64[((f * 2 + 1) * 64 + l) * 4 + d] = sn[(i * k) & 63] | (sn[(i * (k + 1)) & 63] << 16);
      }
  for (int l = 0; l < 64; ++l)
    for (int d = 0; d < 4; ++d) {
      for (int f = 0; f < 4; ++f) {
        int n = (2 * csw(l & 31) * (8 * f + 4 * (l >> 5) + d)) & 63;
        t.b2[(f * 64 + l) * 4 + d] = c[n] | (sn[n] << 16);
        t.b2[((4 + f) * 64 + l) * 4 + d] = (sn[n] ^ 0x8000u) | (c[n] << 16);
      }
      for (int f = 0; f < 2; ++f) {
        int k = 16 * f + 8 * (l >> 5) + 2 * d, n0 = (2 * (l & 31) * k) & 63, n1 = (2 * (l & 31) * (k + 1)) & 63;
        t.b2[((8 + f) * 64 + l) * 4 + d] = c[n0] | (c[n1] << 16);
        t.b2[((10 + f) * 64 + l) * 4 + d] = sn[n0] | (sn[n1] << 16);
        t.b2[((12 + f) * 64 + l) * 4 + d] = (sn[n0] | (sn[n1] << 16)) ^ 0x80008000u;
      }
    }
  return t;
}
__device__ const Tabs g_tabs = make_tabs();
DEV unsigned ald32(const void* base, unsigned off) {
  unsigned r; asm volatile("global_load_dword %0, %1, %2" : "=v"(r) : "v"(off), "s"(base) : "memory"); return r;
}
DEV unsigned ald16(const void* base, unsigned off) {
  unsigned r; asm volatile("global_load_ushort %0, %1, %2" : "=v"(r) : "v"(off), "s"(base) : "memory"); return r;
}
DEV void ast32(void* base, unsigned off, unsigned v) { asm volatile("global_store_dword %0, %1, %2 sc1" ::"v"(off), "v"(v), "s"(base) : "memory"); }
DEV int col_bin(int c) { int ct = c >> 5, k = ct == 7 ? 7 * (c - 192) : 7 * ((ct ? ((ct + 1) & 1) * 32 : 0) + (c & 31)) + 64 * ((ct + 1) >> 1); return k >= 448 ? k - 448 : k; }

constexpr int K1_SLOT = 32 * 1024;
constexpr int K1_LDS = 128 * 1024;
constexpr unsigned CODE_BYTES = 26000;
struct QF { u32x4 x0[4], x1[4], w[4]; };

template <bool ISV>
DEV void qkv_loop(const char* lds, unsigned ld, const u16* xb, const u16* wb, int kn, unsigned xo, unsigned wo0, unsigned wo1,
                  int xoff, int woff, f32x16& a0, f32x16& a1) {
  auto piece = [&](int slot, int i) {
    const unsigned d = ld + slot * K1_SLOT;
    if (i == 0) dma16(xb + kn * 64, xo, d);
    if (i == 1) dma16(wb + kn * (64 * 1344), wo0, d + 16384);
    if (i == 2) dma16(xb + kn * 64 + 64 * 448, xo, d + 8192);
    if (i == 3) { dma16(wb + kn * (64 * 1344), wo1, d + 24576); kn = kn == 6 ? 0 : kn + 1; }
  };
  auto rd = [&](QF& f, int slot, int ks) {
    const char* sl = lds + slot * K1_SLOT;
    f.x0[ks] = *(const u32x4*)(sl + (xoff ^ (ks * 32)));
    f.x1[ks] = *(const u32x4*)(sl + (xoff ^ (ks * 32)) + 4096);
    u32x2 wa = ldtr(sl + woff + ks * 16 * 224), wc = ldtr(sl + woff + ks * 16 * 224 + 224);
    f.w[ks] = u32x4{wa[0], wa[1], wc[0], wc[1]};
  };
  QF f[2];
#pragma unroll
  for (int kc = 0; kc < 8; ++kc) {
    if (kc < 7) { if (kc < 6) VMCNT(4); else VMCNT(0); __builtin_amdgcn_s_barrier(); asm volatile("" ::: "memory"); }
#pragma unroll
    for (int ks = 0; ks < 4; ++ks) {
      if (kc < 7) rd(f[kc & 1], kc & 3, ks);
      if (kc > 0) {
        QF& g = f[(kc - 1) & 1];
        if (ISV) { a0 = mfma(g.x0[ks], g.w[ks], a0); a1 = mfma(g.x1[ks], g.w[ks], a1); }
        else     { a0 = mfma(g.w[ks], g.x0[ks], a0); a1 = mfma(g.w[ks], g.x1[ks], a1); }
      }
      if (kc < 5) piece((kc + 2) & 3, ks);
      SB();
    }
  }
}

__global__ __launch_bounds__(512, 2) void k1_kernel(const u16* __restrict__ ax, const u16* __restrict__ wqkv, const u16* __restrict__ bx,
                                                    u32x4* __restrict__ Qf, u32x4* __restrict__ KVf, u32x4* __restrict__ T) {
  extern __shared__ __attribute__((aligned(16))) char lds[];
  const int tid = threadIdx.x, lane = tid & 63, w = __builtin_amdgcn_readfirstlane(tid >> 6);
  const int xcd = blockIdx.x & 7, idx = blockIdx.x >> 3;
  const int l31 = lane & 31, h = lane >> 5;
  const unsigned lb = ldsaddr_of(lds);
  unsigned long long pc0;
  asm volatile("s_getpc_b64 %0" : "=s"(pc0));
  const unsigned po = (tid & 255) * 128;
  unsigned pf = ald32(w < 4 ? (const char*)(pc0 & ~127ull) : (const char*)&g_tabs, (idx == 0 && po < (w < 4 ? CODE_BYTES : 30720u)) ? po : 0u);
  if (__builtin_expect(idx >= 8, 1)) {
    const int u = idx - 8, which = u >> 3, tm = (xcd & 3) * 4 + (u & 3), g = (xcd >> 2) * 2 + ((u >> 2) & 1);
    const int row = 8 * w + (lane >> 3);
    const unsigned xo = (row * 448 + ((lane & 7) ^ ((row >> 1) & 7)) * 8) * 2;
    unsigned wo2[2];
#pragma unroll
    for (int i = 0; i < 2; ++i) {
      int q = 64 * (w + 8 * i) + lane; if (q > 895) q = 895;
      int rho = q / 14, cc = q - rho * 14;
      wo2[i] = (((rho & ~7) + ((rho & 7) >> 1) + 4 * (rho & 1)) * 1344 + cc * 8) * 2;
    }
    const u16* xb = ax + tm * 128 * 448;
    const u16* wb = wqkv + which * 448 + g * 112;
    int kn = ((u & 3) * 2 + (u >> 2)) % 7;
#pragma unroll
    for (int i = 0; i < 2; ++i) {
      const unsigned d = lb + w * 1024 + i * K1_SLOT;
      dma16(xb + kn * 64, xo, d); dma16(wb + kn * (64 * 1344), wo2[0], d + 16384);
      dma16(xb + kn * 64 + 64 * 448, xo, d + 8192); dma16(wb + kn * (64 * 1344), wo2[1], d + 24576);
      kn = kn == 6 ? 0 : kn + 1;
    }
    SB();
    const int hh = w & 3, th = w >> 2, r0 = th * 64 + l31;
    const int xoff = r0 * 128 + ((h ^ ((r0 >> 1) & 7)) * 16);
    const int woff = 16384 + (8 * h + 2 * ((lane >> 2) & 3)) * 224 + (hh * 28 + ((lane >> 4) & 1) * 16 + 4 * (lane & 3)) * 2;
    const int head = g * 4 + hh, tile = tm * 4 + th * 2;
    f32x16 a0 = {}, a1 = {};
    u32x4 o0, o1, o2, o3;
    if (which == 2) {
      qkv_loop<true>(lds, lb + w * 1024, xb, wb, kn, xo, wo2[0], wo2[1], xoff, woff, a0, a1);
      o0 = packs<0>(a0); o1 = packs<1>(a0); o2 = packs<0>(a1); o3 = packs<1>(a1);
      if (l31 >= 28) { unsigned f = (l31 == 28) ? 0x3F803F80u : 0u; o0 = u32x4{f, f, f, f}; o1 = o0; o2 = o0; o3 = o0; }
    } else {
      qkv_loop<false>(lds, lb + w * 1024, xb, wb, kn, xo, wo2[0], wo2[1], xoff, woff, a0, a1);
      const float sc = which ? 1.0f : 0.18898223650461362f * 1.4426950408889634f;
      a0 *= sc; a1 *= sc;
      o0 = packs<0>(a0); o1 = packs<1>(a0); o2 = packs<0>(a1); o3 = packs<1>(a1);
      if (h) { o1[2] = which ? 0x3F80u : 0u; o1[3] = 0; o3[2] = o1[2]; o3[3] = 0; }
    }
    asm volatile("" ::"v"(pf));
    u32x4* o = (which == 0 ? Qf + ((head * 64 + tile) * 2) * 64 : KVf + ((head * 64 + tile) * 4 + (which & 2)) * 64) + lane;
    const int st = which == 0 ? 128 : 256;
    st128(o, o0); st128(o + 64, o1); st128(o + st, o2); st128(o + st + 64, o3);
  } else {
    const int b1 = xcd * 8 + idx, s2 = b1 >> 1, hb = b1 & 1;
    u32x4* F64 = (u32x4*)(lds + 56 * 1024);
    u32x4* Ap = (u32x4*)(lds + 72 * 1024);
    u32x4* Xf = (u32x4*)(lds + 88 * 1024);
    {
      const int row = 8 * w + (lane >> 3);
      const unsigned xo = ((s2 + 32 * ((row & 32) + sw23(row & 31))) * 448 + ((lane & 7) ^ ((row >> 1) & 7)) * 8) * 2;
#pragma unroll
      for (int m = 0; m < 7; ++m) dma16(bx + 64 * m, xo, lb + m * 8192 + w * 1024);
      dma16(g_tabs.f64 + w * 512, lane * 16, lb + 56 * 1024 + w * 2048); dma16(g_tabs.f64 + w * 512 + 256, lane * 16, lb + 57 * 1024 + w * 2048);
    }
    SB();
    const int mt = w >> 2, o = 2 * (w & 3) + h;
    {
      u32x4 C, S;
      gen_cs(32 * mt + l31, 8 * o, 32, s2, 2047, 1.0f / 2048, C, S);
      Ap[(w * 2 + 0) * 64 + lane] = C; Ap[(w * 2 + 1) * 64 + lane] = S;
    }
    char* xp = lds + (32 * mt + l31) * 128 + ((o ^ ((l31 >> 1) & 7)) * 16);
    int xm[7];
#pragma unroll
    for (int b = 0; b < 7; ++b) { int m = b - o; m += m < 0 ? 7 : 0; m += m < 0 ? 7 : 0; xm[b] = m * 8192; asm volatile("" : "+v"(xm[b])); }
    asm volatile("s_waitcnt vmcnt(2)" : "+v"(pf) :: "memory");
    RAW_BARRIER();
    {
      u32x4 xr[7], z[4];
#pragma unroll
      for (int b = 0; b < 7; ++b) xr[b] = *(const u32x4*)(xp + xm[b]);
      const float ca = hb ? C72 : C71, cb = hb ? C73 : C72, cc = hb ? C71 : C73, sa = hb ? S72 : S71, sb = hb ? -S73 : S72, sc = hb ? -S71 : S73;
      const float da = hb ? C73 : 1.0f, db = hb ? C71 : 1.0f, dc = hb ? C72 : 1.0f, ta = hb ? S73 : 0.0f, tb = hb ? -S71 : 0.0f, tc = hb ? S72 : 0.0f;
#pragma unroll
      for (int jp = 0; jp < 4; ++jp) {
        f32x2 v[7];
#pragma unroll
        for (int b = 0; b < 7; ++b) {
          v[b][0] = bflo(xr[(b - 2 * jp + 14) % 7][jp]);
          v[b][1] = bfhi(xr[(b - 2 * jp - 1 + 14) % 7][jp]);
        }
        f32x2 p1 = v[1] + v[6], p2 = v[2] + v[5], p3 = v[3] + v[4];
        f32x2 m1 = v[6] - v[1], m2 = v[5] - v[2], m3 = v[4] - v[3];
        f32x2 ra = v[0] + p1 * ca + p2 * cb + p3 * cc, ia = m1 * sa + m2 * sb + m3 * sc;
        f32x2 rb = v[0] + p1 * da + p2 * db + p3 * dc, ib = m1 * ta + m2 * tb + m3 * tc;
        z[0][jp] = pk2(ra[0], ra[1]); z[1][jp] = pk2(ia[0], ia[1]); z[2][jp] = pk2(rb[0], rb[1]); z[3][jp] = pk2(ib[0], ib[1]);
      }
#pragma unroll
      for (int c = 0; c < 4; ++c) *(u32x4*)(xp + c * 8192) = z[c];
    }
    VMCNT(0);
    RAW_BARRIER();
    const int wc = w & 3, ct = hb ? 3 + wc : (wc == 2 ? 0 : (wc == 3 ? 7 : wc + 1)), kt = w & 1;
    u32x4 ap[8];
    {
      f32x16 xre = {}, xim = {};
      const char* zp = lds + (w & 2) * 8192 + mt * 4096 + l31 * 128;
      const int zsw = (h ^ ((l31 >> 1) & 7)) * 16;
      u32x4 C[4], S[4], zr[4], zi[4];
#pragma unroll
      for (int tt = 0; tt < 4; ++tt) {
        zr[tt] = *(const u32x4*)(zp + (zsw ^ (tt * 32))); C[tt] = F64[((kt * 4 + tt) * 2) * 64 + lane];
        zi[tt] = *(const u32x4*)(zp + 8192 + (zsw ^ (tt * 32))); S[tt] = F64[((kt * 4 + tt) * 2 + 1) * 64 + lane];
      }
#pragma unroll
      for (int tt = 0; tt < 8; ++tt) ap[tt] = Ap[(mt * 8 + tt) * 64 + lane];
      SB();
#pragma unroll
      for (int tt = 0; tt < 4; ++tt) {
        xre = mfma(zr[tt], C[tt], xre); xim = mfma(zi[tt], C[tt], xim);
        xre = mfma(zi[tt], S[tt], xre); xim = mfma(zr[tt], S[tt] ^ 0x80008000u, xim);
      }
      u32x4* xq = Xf + (wc * 4 + mt * 2) * 128 + lane;
      xq[0] = packs<0>(xre); xq[64] = packs<0>(xim); xq[128] = packs<1>(xre); xq[192] = packs<1>(xim);
    }
    RAW_BARRIER();
    {
      f32x16 tre = {}, tim = {};
      u32x4 bf[8];
#pragma unroll
      for (int tt = 0; tt < 8; ++tt) bf[tt] = Xf[(wc * 8 + tt) * 64 + lane];
      SB();
#pragma unroll
      for (int tt = 0; tt < 4; ++tt) {
        tre = mfma(ap[2 * tt], bf[2 * tt], tre); tim = mfma(ap[2 * tt], bf[2 * tt + 1], tim);
        tre = mfma(ap[2 * tt + 1], bf[2 * tt + 1], tre); tim = mfma(ap[2 * tt + 1] ^ 0x80008000u, bf[2 * tt], tim);
      }
      u32x4* tp = T + (s2 * 16 + mt * 8 + h) * 232 + ct * 32 + l31;
      if (ct < 7 || l31 < 8)
#pragma unroll
      for (int q = 0; q < 4; ++q)
        st128(tp + 2 * q * 232, u32x4{pk2(tre[4 * q], tim[4 * q]), pk2(tre[4 * q + 1], tim[4 * q + 1]), pk2(tre[4 * q + 2], tim[4 * q + 2]), pk2(tre[4 * q + 3], tim[4 * q + 3])});
    }
  }
}

constexpr int K2_LDS = 84 * 1024;
DEV float xhalf_max(float v) {
  float a = v, b = v;
  asm volatile("s_nop 1\n\tv_permlane32_swap_b32 %0, %1\n\ts_nop 1" : "+v"(a), "+v"(b));
  return fmaxf(a, b);
}
DEV float max16(const f32x16& s) {
  float m = fmaxf(fmaxf(s[0], s[1]), s[2]);
#pragma unroll
  for (int i = 3; i < 15; i += 2) m = fmaxf(fmaxf(m, s[i]), s[i + 1]);
  return fmaxf(m, s[15]);
}
#define PIN16(a) asm volatile("" : "+v"(a[0]), "+v"(a[1]), "+v"(a[2]), "+v"(a[3]), "+v"(a[4]), "+v"(a[5]), "+v"(a[6]), "+v"(a[7]), \
                                   "+v"(a[8]), "+v"(a[9]), "+v"(a[10]), "+v"(a[11]), "+v"(a[12]), "+v"(a[13]), "+v"(a[14]), "+v"(a[15]))
#define EX4(s, i, P, j)                                                                                          \
  { const float t0_ = __builtin_amdgcn_exp2f(s[i]); SB(); P[j] = pk2(e0, e1); SB();                               \
    const float t1_ = __builtin_amdgcn_exp2f(s[i + 1]); SB(); P[j + 1] = pk2(e2, e3); SB();                       \
    e0 = t0_; e1 = t1_; e2 = __builtin_amdgcn_exp2f(s[i + 2]); SB(); e3 = __builtin_amdgcn_exp2f(s[i + 3]); SB(); }
#define TILE(sc, sn, K0, K1, Q0, Q1, oc, V0, op, V1, L0, L1, L2, L3)                 \
  sn = mfma(K0, Q0, f32x16{}); SB(); L0; EX4(sc, 0, p1, 2)                           \
  op = mfma(V1, p1, op); SB(); L1; EX4(sc, 4, p0, 0)                                 \
  sn = mfma(K1, Q1, sn); SB(); L2; EX4(sc, 8, p0, 2)                                 \
  oc = mfma(V0, p0, oc); SB(); L3; EX4(sc, 12, p1, 0)
#define KTILE(X, Y, P1, P2)                                                                                      \
  TILE(sa, sb, X##k0, X##k1, q0[1], q1[1], o[0], X##v0, o[3], Y##v1, , Y##v1 = (P1)[192], , )                     \
  TILE(sb, sa, X##k0, X##k1, q0[2], q1[2], o[1], X##v0, o[0], X##v1, , , , )                                      \
  TILE(sa, sb, X##k0, X##k1, q0[3], q1[3], o[2], X##v0, o[1], X##v1, X##k0 = (P2)[0], , X##k1 = (P2)[64], )       \
  TILE(sb, sa, Y##k0, Y##k1, q0[0], q1[0], o[3], X##v0, o[2], X##v1, , , , X##v0 = (P2)[128])

__global__ __launch_bounds__(384, 2) void k2_kernel(const u32x4* __restrict__ Qf, const u32x4* __restrict__ KVf, u32x4* __restrict__ Of,
                                                    const unsigned* __restrict__ T, unsigned* __restrict__ U,
                                                    const u16* __restrict__ Wr, const u16* __restrict__ Wi, const u16* __restrict__ wo) {
  extern __shared__ __attribute__((aligned(16))) char lds[];
  const int tid = threadIdx.x, lane = tid & 63, w = __builtin_amdgcn_readfirstlane(tid >> 6);
  const int xcd = blockIdx.x & 7, idx = blockIdx.x >> 3;
  const int l31 = lane & 31, h = lane >> 5;
  if (__builtin_expect(w < 4, 1)) {
    __builtin_amdgcn_s_setprio(2);
    const int head = xcd * 2 + (idx >> 4), qt = (idx & 15) * 4;
    const u32x4* kp = KVf + (head * 64 + w * 16) * 256 + lane;
    u32x4 q0[4], q1[4];
    u32x4 ek0 = kp[0], ek1 = kp[64];
#pragma unroll
    for (int i = 0; i < 4; ++i) { const u32x4* qp = Qf + (head * 64 + qt + ((w + i) & 3)) * 128 + lane; q0[i] = qp[0]; q1[i] = qp[64]; }
    u32x4 ev0 = kp[128], ev1 = kp[192], dk0 = kp[256], dk1 = kp[320], dv0 = kp[384], dv1 = {}, p0, p1 = {};
    f32x16 o[4] = {}, sa, sb;
    float m[4], e0 = 0.f, e1 = 0.f, e2 = 0.f, e3 = 0.f;
    {
      f32x16 s4[4];
#pragma unroll
      for (int i = 0; i < 4; ++i) { s4[i] = mfma(ek0, q0[i], f32x16{}); s4[i] = mfma(ek1, q1[i], s4[i]); }
#pragma unroll
      for (int i = 0; i < 4; ++i) {
        const unsigned nm = pk2(-xhalf_max(max16(s4[i])), 0.0f);
        m[i] = -bflo(nm);
        if (h) q1[i][2] = nm;
      }
      sa = s4[0] - m[0];
    }
#pragma unroll 1
    for (int it = 0; it < 8; ++it) {
      const u32x4* kn = kp + ((2 * it + 2) & 15) * 256;
      KTILE(e, d, kp + (2 * it + 1) * 256, kn)
      KTILE(d, e, kn, kn + 256)
    }
    p1[2] = pk2(e0, e1); p1[3] = pk2(e2, e3);
    o[3] = mfma(dv1, p1, o[3]);
    if (__builtin_expect(__ballot(h && !(o[0][12] + o[1][12] + o[2][12] + o[3][12] < 1.0e30f &&
                                         fminf(fminf(o[0][12], o[1][12]), fminf(o[2][12], o[3][12])) > 1.0e-20f)) != 0ull, 0)) {
#pragma unroll
      for (int u = 0; u < 4; ++u) {
        f32x16 oo = {}; float mm = -3.0e38f;
        if (h) q1[u][2] = 0;
#pragma unroll 1
        for (int t = 0; t < 16; ++t) {
          const u32x4* p = kp + t * 256;
          f32x16 s = mfma(p[0], q0[u], f32x16{}); s = mfma(p[64], q1[u], s);
          float mn = fmaxf(mm, xhalf_max(max16(s)));
          float al = __builtin_amdgcn_exp2f(mm - mn);
          mm = mn;
#pragma unroll
          for (int i = 0; i < 16; ++i) { oo[i] *= al; s[i] = __builtin_amdgcn_exp2f(s[i] - mn); }
          oo = mfma(p[128], packs<0>(s), oo); oo = mfma(p[192], packs<1>(s), oo);
        }
        o[u] = oo; m[u] = mm;
      }
    }
#pragma unroll
    for (int u = 1; u < 4; ++u) {
      if (h) o[u][13] = m[u];
      f32x4* ex = (f32x4*)(lds + (((w + u) & 3) * 3 + u - 1) * 4096) + lane;
#pragma unroll
      for (int i = 0; i < 4; ++i) ex[i * 64] = f32x4{o[u][4 * i], o[u][4 * i + 1], o[u][4 * i + 2], o[u][4 * i + 3]};
    }
    const float l0 = xhalf_max(h ? o[0][12] : -1.0f);
    RAW_BARRIER();
    const char* ey = lds + w * 12288;
    f32x2 st[3];
#pragma unroll
    for (int u = 0; u < 3; ++u) st[u] = *(const f32x2*)(ey + u * 4096 + 3584 + l31 * 16);
    f32x4 y[12];
#pragma unroll
    for (int i = 0; i < 12; ++i) y[i] = *(const f32x4*)(ey + i * 1024 + lane * 16);
    SB();
    const float mm = fmaxf(fmaxf(st[0][1], st[1][1]), fmaxf(st[2][1], m[0]));
    float f0 = __builtin_amdgcn_exp2f(m[0] - mm), f[3];
#pragma unroll
    for (int u = 0; u < 3; ++u) f[u] = __builtin_amdgcn_exp2f(st[u][1] - mm);
    const float inv = __builtin_amdgcn_rcpf(l0 * f0 + st[0][0] * f[0] + st[1][0] * f[1] + st[2][0] * f[2]);
    f0 *= inv;
    f32x16 r = o[0] * f0;
    asm volatile("" : "+v"(r) : "v"(f0));
#pragma unroll
    for (int u = 0; u < 3; ++u) {
      const float fu = f[u] * inv;
#pragma unroll
      for (int i = 0; i < 4; ++i) {
        const f32x4 v = y[u * 4 + i];
        r[4 * i] += v[0] * fu; r[4 * i + 1] += v[1] * fu; r[4 * i + 2] += v[2] * fu; r[4 * i + 3] += v[3] * fu;
      }
    }
    u32x4 r1 = packs<1>(r);
    if (h) { r1[2] = 0; r1[3] = 0; }
    u32x4* op = Of + (((qt + w) * 16 + head) * 2) * 64 + lane;
    st128(op, packs<0>(r)); st128(op + 64, r1);
  } else if (idx < 29) {
    const int jq = xcd * 2 + w - 4, col = idx * 8 + (l31 >> 2), kb = col_bin(col), jr = 4 * jq + (lane & 3) + 512 * h;
    const unsigned toff = (4 * h * 16 + jq) * 928 + idx * 32 + l31;
    const bool cj = kb > 224;
    const int of = jr * 450 + 2 * kb, om = (2048 - jr) * 450 + 2 * (448 - kb), om0 = jr ? om : om - 921600;
    const float sr = col > 224 ? 0.0f : ((col == 0 || col == 224) ? 0.5f : 1.0f), si = cj ? -sr : sr;
    u32x4 mt[14];
    unsigned ta[16], ur[16], ui[16]; float wr[16], wi[16];
#pragma unroll
    for (int i = 0; i < 16; ++i) ta[i] = ald32(T + (8 * (i >> 2) + (i & 3)) * 14848, toff * 4);
#pragma unroll
    for (int g = 0; g < 16; ++g) {
      const unsigned o_ = g ? (cj ? om : of) + ((g & 7) + 16 * (g >> 3)) * (cj ? -28800 : 28800) : (cj ? om0 : of);
      ur[g] = ald16(Wr, o_); ui[g] = ald16(Wi, o_);
    }
    const unsigned* tabp = g_tabs.b2;
    asm volatile("" : "+s"(tabp));
#pragma unroll
    for (int t = 0; t < 14; ++t) mt[t] = ald128(tabp + t * 256, lane * 16);
    const unsigned wpo = ((idx * 2 + w - 4) * 64 + lane) * 128;
    unsigned pf = ald32(wo, wpo < 401408u ? wpo : 0u);
    asm volatile("s_waitcnt vmcnt(0)" : "+v"(pf), "+v"(mt[0]), "+v"(mt[1]), "+v"(mt[2]), "+v"(mt[3]), "+v"(mt[4]), "+v"(mt[5]), "+v"(mt[6]), "+v"(mt[7]), "+v"(mt[8]),
                 "+v"(mt[9]), "+v"(mt[10]), "+v"(mt[11]), "+v"(mt[12]), "+v"(mt[13]) :: "memory");
    PIN16(ta); PIN16(ur); PIN16(ui);
#pragma unroll
    for (int g = 0; g < 16; ++g) { wr[g] = bflo(ur[g]); wi[g] = bflo(ui[g]); }
    SB();
    f32x16 xr = {}, xi = {};
#pragma unroll
    for (int t = 0; t < 4; ++t) {
      u32x4 b = {ta[4 * t], ta[4 * t + 1], ta[4 * t + 2], ta[4 * t + 3]};
      xr = mfma(mt[t], b, xr); xi = mfma(mt[4 + t], b, xi);
    }
    f32x16 yr, yi;
#pragma unroll
    for (int g = 0; g < 16; g += 2) {
      f32x2 c = {wr[g], wr[g + 1]}, d = {wi[g], wi[g + 1]};
      asm volatile("" : "+v"(c), "+v"(d));
      c *= sr; d *= si;
      const f32x2 a = {xr[g], xr[g + 1]}, b = {xi[g], xi[g + 1]}, r = a * c - b * d, i = a * d + b * c;
      yr[g] = r[0]; yr[g + 1] = r[1]; yi[g] = i[0]; yi[g + 1] = i[1];
    }
    u32x4 r0 = packs<0>(yr), r1 = packs<1>(yr), i0 = packs<0>(yi), i1 = packs<1>(yi);
    f32x16 ure = {}, uim = {};
    ure = mfma(mt[8], r0, ure); uim = mfma(mt[8], i0, uim);
    ure = mfma(mt[12], i0, ure); uim = mfma(mt[10], r0, uim);
    ure = mfma(mt[9], r1, ure); uim = mfma(mt[9], i1, uim);
    ure = mfma(mt[13], i1, ure); uim = mfma(mt[11], r1, uim);
#pragma unroll
    for (int g = 0; g < 16; ++g) ast32(U + ((g & 3) + 8 * (g >> 2)) * 14848, toff * 4, pk2(ure[g], uim[g]));
  }
}

constexpr int ZS = 68;
constexpr int K3_LDS = 77 * 1024;

__global__ __launch_bounds__(512, 4) void k3_kernel(const u32x4* __restrict__ Of, const u16* __restrict__ wo, const u32x4* __restrict__ U,
                                                    u16* __restrict__ out) {
  extern __shared__ __attribute__((aligned(16))) char lds[];
  const int tid = threadIdx.x, lane = tid & 63, w = __builtin_amdgcn_readfirstlane(tid >> 6);
  const int xcd = blockIdx.x & 7, idx = blockIdx.x >> 3;
  const int l31 = lane & 31, h = lane >> 5;
  if (idx < 4 || idx >= 12) {
    if (w >= 4) return;
    const int u = idx < 4 ? idx : idx - 8, tq = xcd * 4 + u / 7, tn = u % 7;
    const int qi = w & 1, kh = w >> 1;
    u32x4 of[16];
    {
      const int r8 = lane >> 3;
      const unsigned so = (r8 * 448 + ((lane & 7) ^ ((r8 & 2) << 1)) * 8) * 2, l16 = lane * 16;
      const u16* src = wo + (kh * 224 + qi * 8) * 448 + tn * 64;
      const unsigned d = ldsaddr_of(lds) + (kh * 224 + qi * 8) * 128;
      const u32x4* op = Of + (((tq * 2 + qi) * 16 + kh * 8) * 2) * 64;
#pragma unroll
      for (int st = 0; st < 2; ++st) {
#pragma unroll
        for (int i = 0; i < 7; ++i) dma16(src + (st * 7 + i) * 16 * 448, so, d + (st * 7 + i) * 2048);
#pragma unroll
        for (int i = 0; i < 8; ++i) of[st * 8 + i] = ald128(op + (st * 8 + i) * 64, l16);
      }
    }
    const int q = (lane >> 2) & 3, p = lane & 3, pc = ((p & 1) << 1) | (p >> 1);
    const int colb = ((lane >> 4) & 1) * 32 + pc * 8;
    const int base = (kh * 224 + q) * 128 + (colb ^ (((q >> 1) ^ kh) << 6));
    f32x16 a0 = {}, a1 = {};
#pragma unroll
    for (int st = 0; st < 2; ++st) {
      if (st == 0) asm volatile("s_waitcnt vmcnt(15)" : "+v"(of[0]), "+v"(of[1]), "+v"(of[2]), "+v"(of[3]), "+v"(of[4]), "+v"(of[5]), "+v"(of[6]), "+v"(of[7]) :: "memory");
      else asm volatile("s_waitcnt vmcnt(0)" : "+v"(of[8]), "+v"(of[9]), "+v"(of[10]), "+v"(of[11]), "+v"(of[12]), "+v"(of[13]), "+v"(of[14]), "+v"(of[15]) :: "memory");
      RAW_BARRIER();
      u32x4 fx[3], fy[3];
#pragma unroll
      for (int n = 0; n < 10; ++n) {
        if (n < 8) {
          const int i = st * 8 + n, hd = i >> 1, s = i & 1;
          const char* pa = lds + (hd * 28 + 16 * s) * 128;
          const int ra = base + h * 512, rb = (s ? base : base + h * 512) + 1024;
          u32x2 x0 = ldtr(pa + ra), x1 = ldtr(pa + rb), y0 = ldtr(pa + (ra ^ 64)), y1 = ldtr(pa + (rb ^ 64));
          fx[n % 3] = u32x4{x0[0], x0[1], x1[0], x1[1]}; fy[n % 3] = u32x4{y0[0], y0[1], y1[0], y1[1]};
        }
        SB();
        if (n >= 2) { a0 = mfma(fx[(n - 2) % 3], of[st * 8 + n - 2], a0); a1 = mfma(fy[(n - 2) % 3], of[st * 8 + n - 2], a1); }
        SB();
      }
    }
    f32x4* ex = (f32x4*)(lds + 56 * 1024);
    f32x16 snd = a1, kp = a0;
#pragma unroll
    for (int i = 0; i < 4; ++i) ex[(w * 4 + i) * 64 + lane] = f32x4{snd[4 * i], snd[4 * i + 1], snd[4 * i + 2], snd[4 * i + 3]};
    RAW_BARRIER();
#pragma unroll
    for (int i = 0; i < 4; ++i) {
      f32x4 v = ex[((w ^ 2) * 4 + i) * 64 + lane];
      kp[4 * i] += v[0]; kp[4 * i + 1] += v[1]; kp[4 * i + 2] += v[2]; kp[4 * i + 3] += v[3];
    }
    u16* dst = out + ((tq * 2 + qi) * 32 + l31) * 896 + tn * 64 + kh * 32 + 8 * h;
    st128(dst, packs<0>(kp)); st128(dst + 16, packs<1>(kp));
  } else {
    const int b3 = xcd * 8 + idx - 4, s2 = b3 >> 1, sh = b3 & 1, r = w;
    u32x4* Dm = (u32x4*)lds;
    u32x4* F64 = (u32x4*)(lds + 61 * 1024);
    float* zs = (float*)lds;
    u32x4 ua[8], ub[8];
    if (__builtin_expect(w < 4, 1)) {
      const u32x4* up = U + s2 * 16 * 232 + (r ? 2 * r - 1 : 0) * 32;
      const unsigned uo = (h * 232 + sw23(l31)) * 16, uo2 = (h * 232 + (sw23(l31) & (r ? 31 : 7)) + (r ? 32 : 224)) * 16;
#pragma unroll
      for (int i = 0; i < 8; ++i) { ua[i] = ald128(up + i * 464, uo); ub[i] = ald128(up + i * 464, uo2); }
    } else {
      const unsigned pl = (h * 32 + sw23(l31)) * 16;
#pragma unroll
      for (int i = 0; i < 4; ++i) dma16(g_tabs.f64 + ((w & 3) * 4 + i) * 256, pl, ldsaddr_of(lds) + 61 * 1024 + ((w & 3) * 4 + i) * 1024);
    }
    SB();
    {
      const int s = s2 + 32 * (32 * sh + l31);
      const float NRM2 = 2.0f / (2048.0f * 448.0f);
      u32x4 dr, di;
#pragma unroll
      for (int i = 0; i < 4; ++i) {
        int n = __mul24(8 * w + 4 * h + i, s) & 2047;
        float c = cosr(n, 1.0f / 2048) * NRM2, sn = sinr(n, 1.0f / 2048) * NRM2;
        dr[i] = pk2(c, -sn); di[i] = pk2(sn, c);
      }
      Dm[(w * 2) * 64 + lane] = dr; Dm[(w * 2 + 1) * 64 + lane] = di;
    }
    RAW_BARRIER();
    if (__builtin_expect(w < 4, 1)) {
      f32x16 ar = {}, ai = {}, br = {}, bi = {};
#pragma unroll
      for (int t = 0; t < 8; ++t) {
        u32x4 dr = Dm[(t * 2) * 64 + lane], di = Dm[(t * 2 + 1) * 64 + lane];
        asm volatile("s_waitcnt vmcnt(%2)" : "+v"(ua[t]), "+v"(ub[t]) : "n"(14 - 2 * t) : "memory");
        ar = mfma(ua[t], dr, ar); ai = mfma(ua[t], di, ai); br = mfma(ub[t], dr, br); bi = mfma(ub[t], di, bi);
      }
      RAW_BARRIER();
      u32x4 yr[4] = {packs<0>(ar), packs<1>(ar), packs<0>(br), packs<1>(br)};
      u32x4 yi[4] = {packs<0>(ai), packs<1>(ai), packs<0>(bi), packs<1>(bi)};
      const unsigned k2 = r ? ~0u : 0u, k1 = (r || !h) ? ~0u : 0u;
      yr[2] &= k1; yr[3] &= k2; yi[2] &= k2; yi[3] &= k2;
      float* zp = zs + ((r ? 2 * r - 1 : 0) * 32 + l31) * ZS + 8 * h;
      float* zq = zp + (r ? 32 * ZS : 0);
      f32x16 zre[2] = {}, zim[2] = {};
      u32x4 C = F64[lane], S = F64[64 + lane];
#pragma unroll
      for (int n = 0; n < 12; ++n) {
        const int at = n >> 2, t = n & 3;
        if (n < 8) {
          const u32x4 Cn = F64[((n + 1) & 7) * 128 + lane], Sn = F64[((n + 1) & 7) * 128 + 64 + lane];
          SB();
          zim[at] = mfma(C, yi[t], zim[at]); zre[at] = mfma(C, yr[t], zre[at]);
          zim[at] = mfma(S, yr[t], zim[at]); zre[at] = mfma(S ^ 0x80008000u, yi[t], zre[at]);
          C = Cn; S = Sn;
          SB();
        }
        if (n >= 4) {
          const int off = (at - 1) * 32 + 16 * (t >> 1) + 4 * (t & 1);
          *(f32x4*)(zq + off) = f32x4{zim[at - 1][4 * t], zim[at - 1][4 * t + 1], zim[at - 1][4 * t + 2], zim[at - 1][4 * t + 3]};
          *(f32x4*)(zp + off) = f32x4{zre[at - 1][4 * t], zre[at - 1][4 * t + 1], zre[at - 1][4 * t + 2], zre[at - 1][4 * t + 3]};
        }
      }
    } else {
      VMCNT(0);
      RAW_BARRIER();
    }
    RAW_BARRIER();
    {
      const int o = tid & 15, row = tid >> 4, rot = ((o >> 1) + 4 * (o & 1)) % 7;
      float* zp = zs + row * ZS + 4 * o;
      f32x4 zl[7];
#pragma unroll
      for (int c = 0; c < 7; ++c) zl[c] = *(const f32x4*)(zp + c * 32 * ZS);
      float y[4][7];
#pragma unroll
      for (int jp = 0; jp < 2; ++jp) {
        f32x2 z[7];
#pragma unroll
        for (int c = 0; c < 7; ++c) z[c] = f32x2{zl[c][2 * jp], zl[c][2 * jp + 1]};
        f32x2 y0 = z[0] + z[1] + z[3] + z[5];
        f32x2 e1 = z[0] + z[1] * C71 + z[3] * C72 + z[5] * C73, o1 = z[2] * S71 + z[4] * S72 + z[6] * S73;
        f32x2 e2 = z[0] + z[1] * C72 + z[3] * C73 + z[5] * C71, o2 = z[2] * S72 - z[4] * S73 - z[6] * S71;
        f32x2 e3 = z[0] + z[1] * C73 + z[3] * C71 + z[5] * C72, o3 = z[2] * S73 - z[4] * S71 + z[6] * S72;
        f32x2 yy[7] = {y0, e1 - o1, e2 - o2, e3 - o3, e3 + o3, e2 + o2, e1 + o1};
#pragma unroll
        for (int b = 0; b < 7; ++b) { y[2 * jp][b] = yy[b][0]; y[2 * jp + 1][b] = yy[b][1]; }
      }
#pragma unroll
      for (int b = 0; b < 7; ++b) {
        int m = b - rot; m += m < 0 ? 7 : 0;
        *(u32x2*)(zp + m * 32 * ZS) = u32x2{pk2(y[0][b], y[1][(b + 1) % 7]), pk2(y[2][(b + 2) % 7], y[3][(b + 3) % 7])};
      }
      u16* dst = out + (s2 + 32 * (32 * sh + row)) * 896 + 448 + 4 * o;
#pragma unroll
      for (int m = 0; m < 7; ++m) { const u32x2 v_ = *(const u32x2*)(zp + m * 32 * ZS); asm volatile("global_store_dwordx2 %0, %1, off sc1" ::"v"(dst + 64 * m), "v"(v_) : "memory"); }
    }
  }
}

extern "C" void kernel_launch(void* const* d_in, const int* in_sizes, int n_in, void* d_out, int out_size, void* d_ws, size_t ws_size,
                              hipStream_t stream) {
  const u16* ax = (const u16*)d_in[0];
  const u16* wqkv = (const u16*)d_in[1];
  const u16* wo = (const u16*)d_in[2];
  const u16* bx = (const u16*)d_in[3];
  const u16* wr = (const u16*)d_in[4];
  const u16* wi = (const u16*)d_in[5];
  char* ws = (char*)d_ws;
  u32x4* Qf = (u32x4*)(ws);
  u32x4* KVf = (u32x4*)(ws + (2u << 20));
  u32x4* Of = (u32x4*)(ws + (6u << 20));
  unsigned* T = (unsigned*)(ws + (8u << 20));
  unsigned* U = (unsigned*)(ws + (10u << 20));
  (void)hipFuncSetAttribute((const void*)k1_kernel, hipFuncAttributeMaxDynamicSharedMemorySize, K1_LDS);
  (void)hipFuncSetAttribute((const void*)k2_kernel, hipFuncAttributeMaxDynamicSharedMemorySize, K2_LDS);
  (void)hipFuncSetAttribute((const void*)k3_kernel, hipFuncAttributeMaxDynamicSharedMemorySize, K3_LDS);
  k1_kernel<<<256, 512, K1_LDS, stream>>>(ax, wqkv, bx, Qf, KVf, (u32x4*)T);
  k2_kernel<<<256, 384, K2_LDS, stream>>>(Qf, KVf, Of, T, U, wr, wi, wo);
  k3_kernel<<<288, 512, K3_LDS, stream>>>(Of, wo, (const u32x4*)U, (u16*)d_out);
}
```
